```python
import torch

def _softmax(x, axis=-1):
    return torch.softmax(x, dim=axis)
def _gelu(x):
    return torch.nn.functional.gelu(x, approximate="tanh")
def _silu(x):
    return torch.nn.functional.silu(x)
def _sigmoid(x):
    return torch.sigmoid(x)
def _layernorm(x, axis=-1, eps=1e-5):
    m = x.mean(dim=axis, keepdim=True)
    v = x.var(dim=axis, keepdim=True, unbiased=False)
    return (x - m) / torch.sqrt(v + eps)
def _scatter_add(tgt, idx, src, axis=0):
    return tgt.clone().index_add_(axis, idx, src)
def _segment_sum(src, seg, n):
    out = torch.zeros((n,) + src.shape[1:], dtype=src.dtype)
    return out.index_add_(0, seg, src)
def _pad(x, pw):
    flat = tuple(v for pair in reversed(pw) for v in pair)
    return torch.nn.functional.pad(x, flat)

kernel_name = 'parallel_sum__grouped_kv_mqa__glu_gated_reglu__prompt'

def setup_inputs():

    _w = (lambda a: (lambda t: t.to(torch.bfloat16) if t.is_floating_point() else t)(torch.as_tensor(a)))
    def _A_setup():
        B, S, D, H = 1, 2048, 576, 32
        Dh = D // H
        return {
            "x": _w((lambda *s: torch.randn(*s, dtype=torch.bfloat16) * 0.05)(B, S, D)),
            "wq": _w((lambda *s: torch.randn(*s, dtype=torch.bfloat16) * 0.05)(D, H * Dh)),
            "wk": _w((lambda *s: torch.randn(*s, dtype=torch.bfloat16) * 0.05)(D, Dh)),
            "wv": _w((lambda *s: torch.randn(*s, dtype=torch.bfloat16) * 0.05)(D, Dh)),
            "wo": _w((lambda *s: torch.randn(*s, dtype=torch.bfloat16) * 0.05)(H * Dh, D)),
        }
    _A_in = _A_setup()

    def _B_setup():
        B, S, D, F = 1, 2048, 576, 4608
        return {
            "x": _w((lambda *s: torch.randn(*s, dtype=torch.bfloat16) * 0.05)(B, S, D)),
            "W_gate": _w((lambda *s: torch.randn(*s, dtype=torch.bfloat16) * 0.4)(D, F) * (2.0 / D) ** 0.5),
            "W_up": _w((lambda *s: torch.randn(*s, dtype=torch.bfloat16) * 0.4)(D, F) * (2.0 / D) ** 0.5),
            "W_down": _w((lambda *s: torch.randn(*s, dtype=torch.bfloat16) * 0.4)(F, D) * (1.0 / F) ** 0.5),
        }
    _B_in = _B_setup()

    return {
        "A_x": _A_in["x"],
        "A_wq": _A_in["wq"],
        "A_wk": _A_in["wk"],
        "A_wv": _A_in["wv"],
        "A_wo": _A_in["wo"],
        "B_x": _B_in["x"],
        "B_W_gate": _B_in["W_gate"],
        "B_W_up": _B_in["W_up"],
        "B_W_down": _B_in["W_down"],
    }

def reference(A_x, A_wq, A_wk, A_wv, A_wo, B_x, B_W_gate, B_W_up, B_W_down):
    def _A_ref(x, wq, wk, wv, wo):
        B, S, D = x.shape
        H = 32
        Dh = D // H
        q = (lambda x, *d: x.permute(*d))((x @ wq).reshape(B, S, H, Dh), 0, 2, 1, 3)
        k = x @ wk
        v = x @ wv
        scores = torch.einsum("bhqd,bkd->bhqk", q, k) * (Dh ** -0.5)
        p = _softmax(scores, axis=-1)
        o = torch.einsum("bhqk,bkd->bhqd", p, v)
        return (lambda x, *d: x.permute(*d))(o, 0, 2, 1, 3).reshape(B, S, H * Dh) @ wo

    def _B_ref(x, W_gate, W_up, W_down):
        gate = torch.einsum("bsd,df->bsf", x, W_gate)
        up = torch.einsum("bsd,df->bsf", x, W_up)
        gate = gate * (lambda x, dt: x.to(dt))(gate > 0, gate.dtype)
        h = gate * up
        return torch.einsum("bsf,fd->bsd", h, W_down)

    x = A_x
    xn = _layernorm(x, axis=-1)
    return x + _A_ref(xn, A_wq, A_wk, A_wv, A_wo) + _B_ref(xn, B_W_gate, B_W_up, B_W_down)
```

```cpp
#include <hip/hip_runtime.h>
#include <hip/hip_bf16.h>
typedef unsigned short u16;
__device__ __forceinline__ float bf2f(u16 v){ return __uint_as_float(((unsigned)v)<<16); }
__device__ __forceinline__ u16 f2bf(float f){ unsigned u=__float_as_uint(f); u += 0x7fffu + ((u>>16)&1u); return (u16)(u>>16); }
__device__ __forceinline__ float rbf(float f){ return bf2f(f2bf(f)); }
constexpr int S=2048, D=576, H=32, DH=18, F=4608;

__global__ void ln_k(const u16* x, u16* xn){
  int row=blockIdx.x; int t=threadIdx.x;
  float s=0; for(int i=t;i<D;i+=64) s+=bf2f(x[row*D+i]);
  for(int o=32;o;o>>=1) s+=__shfl_xor(s,o);
  float mean=s/D; float q=0; for(int i=t;i<D;i+=64){float d=bf2f(x[row*D+i])-mean; q+=d*d;}
  for(int o=32;o;o>>=1) q+=__shfl_xor(q,o);
  float m=rbf(mean), v=rbf(q/D); float sd=rbf(sqrtf(rbf(v+1e-5f)));
  for(int i=t;i<D;i+=64){ float d=rbf(bf2f(x[row*D+i])-m); xn[row*D+i]=f2bf(d/sd);}
}
__global__ void gemm_k(const u16* A,const u16* B,u16* C,int M,int N,int K){
  int col=blockIdx.x*64+threadIdx.x; int row=blockIdx.y; if(col>=N) return;
  float acc=0; for(int k=0;k<K;k++) acc+=bf2f(A[row*K+k])*bf2f(B[k*N+col]);
  C[row*N+col]=f2bf(acc);
}
__global__ void attn_k(const u16* q,const u16* k,const u16* v,u16* o){
  int row=blockIdx.x, h=blockIdx.y, t=threadIdx.x;
  float qq[DH]; for(int d=0;d<DH;d++) qq[d]=bf2f(q[row*D+h*DH+d]);
  float m=-1e30f;
  for(int j=t;j<S;j+=64){ float s=0; for(int d=0;d<DH;d++) s+=qq[d]*bf2f(k[j*DH+d]); s*=0.23570226f; m=fmaxf(m,s);}
  for(int o_=32;o_;o_>>=1) m=fmaxf(m,__shfl_xor(m,o_));
  float l=0; float acc[DH]; for(int d=0;d<DH;d++) acc[d]=0;
  for(int j=t;j<S;j+=64){ float s=0; for(int d=0;d<DH;d++) s+=qq[d]*bf2f(k[j*DH+d]); s*=0.23570226f; float p=__expf(s-m); l+=p; for(int d=0;d<DH;d++) acc[d]+=p*bf2f(v[j*DH+d]); }
  for(int o_=32;o_;o_>>=1){ l+=__shfl_xor(l,o_); for(int d=0;d<DH;d++) acc[d]+=__shfl_xor(acc[d],o_);}
  if(t==0) for(int d=0;d<DH;d++) o[row*D+h*DH+d]=f2bf(acc[d]/l);
}
__global__ void h_k(const u16* g,const u16* u,u16* h,int n){ int i=blockIdx.x*256+threadIdx.x; if(i<n){ float gg=bf2f(g[i]); gg=gg>0?gg:0.f; h[i]=f2bf(gg*bf2f(u[i])); } }
__global__ void fin_k(const u16* x,const u16* a,const u16* b,u16* o,int n){ int i=blockIdx.x*256+threadIdx.x; if(i<n) o[i]=f2bf(rbf(bf2f(x[i])+bf2f(a[i]))+bf2f(b[i])); }

extern "C" void kernel_launch(void* const* d_in, const int* in_sizes, int n_in, void* d_out, int out_size, void* d_ws, size_t ws_size, hipStream_t stream) {
  const u16* x=(const u16*)d_in[0]; const u16* wq=(const u16*)d_in[1]; const u16* wk=(const u16*)d_in[2]; const u16* wv=(const u16*)d_in[3]; const u16* wo=(const u16*)d_in[4];
  const u16* wg=(const u16*)d_in[6]; const u16* wu=(const u16*)d_in[7]; const u16* wd=(const u16*)d_in[8];
  u16* ws=(u16*)d_ws; u16* xn=ws; u16* q=xn+S*D; u16* k=q+S*D; u16* v=k+S*32; u16* o=v+S*32; u16* a=o+S*D; u16* g=a+S*D; u16* u=g+S*F; u16* h=u+S*F; u16* b=h+S*F;
  ln_k<<<S,64,0,stream>>>(x,xn);
  gemm_k<<<dim3(D/64,S),64,0,stream>>>(xn,wq,q,S,D,D);
  gemm_k<<<dim3(1,S),64,0,stream>>>(xn,wk,k,S,DH,D);
  gemm_k<<<dim3(1,S),64,0,stream>>>(xn,wv,v,S,DH,D);
  attn_k<<<dim3(S,H),64,0,stream>>>(q,k,v,o);
  gemm_k<<<dim3(D/64,S),64,0,stream>>>(o,wo,a,S,D,D);
  gemm_k<<<dim3(F/64,S),64,0,stream>>>(xn,wg,g,S,F,D);
  gemm_k<<<dim3(F/64,S),64,0,stream>>>(xn,wu,u,S,F,D);
  h_k<<<S*F/256,256,0,stream>>>(g,u,h,S*F);
  gemm_k<<<dim3(D/64,S),64,0,stream>>>(h,wd,b,S,D,F);
  fin_k<<<S*D/256,256,0,stream>>>(x,a,b,(u16*)d_out,S*D);
}
```

```cpp
#include <hip/hip_runtime.h>
#include <hip/hip_bf16.h>
#include <stdint.h>

typedef unsigned short u16;
typedef short bf16x8 __attribute__((ext_vector_type(8)));
typedef short s16x4 __attribute__((ext_vector_type(4)));
typedef float f32x4 __attribute__((ext_vector_type(4)));
typedef float f32x16 __attribute__((ext_vector_type(16)));
typedef int i32x4 __attribute__((ext_vector_type(4)));
typedef unsigned u32x4 __attribute__((ext_vector_type(4)));
typedef unsigned u32x2 __attribute__((ext_vector_type(2)));
#define LAS __attribute__((address_space(3)))
#define GAS __attribute__((address_space(1)))
#define DI __device__ __forceinline__

constexpr int SEQ = 2048, DM = 576, NH = 32, DH = 18, FF = 4608;

constexpr size_t WS_FLAGS = 0;
constexpr size_t WS_XN    = 4096;
constexpr size_t WS_WQX   = WS_XN + (size_t)SEQ * DM * 2;
constexpr size_t WS_Q     = WS_WQX + (size_t)DM * 1024 * 2;
constexpr size_t WS_KF1   = WS_Q + (size_t)NH * SEQ * 32 * 2;
constexpr size_t WS_KF2   = WS_KF1 + 64 * 1024;
constexpr size_t WS_VF    = WS_KF2 + 64 * 256;
constexpr size_t WS_PART  = WS_VF + 128 * 1024;
constexpr size_t WS_O     = WS_PART + (size_t)256 * 8 * 18 * 1024;
constexpr size_t WS_END   = WS_O + (size_t)256 * 128 * 80;
constexpr int FL_KV = 0, FL_KMAXA = 64, FL_KMAXB = 128, FL_TMO = 192;

DI float bf2f(u16 v) { return __uint_as_float(((unsigned)v) << 16); }
DI float bflo(unsigned v) { return __uint_as_float(v << 16); }
DI float bfhi(unsigned v) { return __uint_as_float(v & 0xffff0000u); }
typedef __bf16 bf16x2_t __attribute__((ext_vector_type(2)));
typedef float f32x2 __attribute__((ext_vector_type(2)));
DI unsigned cvtpk(float lo, float hi) { return __builtin_bit_cast(unsigned, __builtin_convertvector((f32x2){lo, hi}, bf16x2_t)); }
DI float rbf(float f) { return bflo(cvtpk(f, f)); }
DI i32x4 make_srd(const void* p, unsigned bytes) {
    unsigned long long a = (unsigned long long)p; i32x4 s;
    s.x = __builtin_amdgcn_readfirstlane((unsigned)a); s.y = __builtin_amdgcn_readfirstlane((unsigned)(a >> 32) & 0xffffu);
    s.z = (int)bytes; s.w = 0x00020000; return s;
}
DI void dma16(i32x4 srd, unsigned voff, unsigned soff, unsigned lds_addr) {
    unsigned keep;
    asm volatile("s_mov_b32 %0, m0\n\ts_mov_b32 m0, %4\n\ts_nop 0\n\tbuffer_load_dwordx4 %1, %2, %3 offen lds\n\ts_mov_b32 m0, %0"
                 : "=&s"(keep) : "v"(voff), "s"(srd), "s"(soff), "s"(lds_addr) : "memory");
}
#define WAIT_BAR(N) asm volatile("s_waitcnt vmcnt(" #N ") lgkmcnt(0)\n\ts_barrier" ::: "memory")
#define MFMA16(a, b, c) __builtin_amdgcn_mfma_f32_16x16x32_bf16(a, b, c, 0, 0, 0)
#define MFMA32(a, b, c) __builtin_amdgcn_mfma_f32_32x32x16_bf16(a, b, c, 0, 0, 0)
#define MFMA32K8(a, b, c) __builtin_amdgcn_mfma_f32_32x32x8bf16_1k(a, b, c, 0, 0, 0)
typedef short v4i16_t __attribute__((ext_vector_type(4)));
DI s16x4 lds_tr(LAS const char* p) { return __builtin_bit_cast(s16x4, __builtin_amdgcn_ds_read_tr16_b64_v4i16((LAS v4i16_t*)p)); }
DI bf16x8 lds_b128(LAS const char* p) { return *(LAS const bf16x8*)p; }
#define RLX_AGENT __ATOMIC_RELAXED, __HIP_MEMORY_SCOPE_AGENT

__global__ __launch_bounds__(64) void k1_prep(const u16* __restrict__ x, const u16* __restrict__ wq, const u16* __restrict__ wk,
                                               const u16* __restrict__ wv, char* __restrict__ ws) {
    const int b = blockIdx.x, l = threadIdx.x;
    if (b < 128) {
        const int tok = b * 16 + (l & 15), c = l >> 4;
        const u32x4* src = (const u32x4*)(x + (size_t)tok * DM + 8 * c);
        u32x4 v[18];
#pragma unroll
        for (int j = 0; j < 18; ++j) v[j] = src[j * 4];
        float s = 0.f;
#pragma unroll
        for (int j = 0; j < 18; ++j)
#pragma unroll
            for (int e = 0; e < 4; ++e) s += bflo(v[j][e]) + bfhi(v[j][e]);
        s += __shfl_xor(s, 16); s += __shfl_xor(s, 32);
        const float mean = s * (1.0f / DM);
        float q = 0.f;
#pragma unroll
        for (int j = 0; j < 18; ++j)
#pragma unroll
            for (int e = 0; e < 4; ++e) { const float a = bflo(v[j][e]) - mean, bb = bfhi(v[j][e]) - mean; q += a * a + bb * bb; }
        q += __shfl_xor(q, 16); q += __shfl_xor(q, 32);
        const float m = rbf(mean), var = rbf(q * (1.0f / DM));
        const float sd = rbf(sqrtf(rbf(var + 1e-5f)));
        const float rs = 1.0f / sd;
        char* dst = ws + WS_XN + ((size_t)(b >> 3) * 18 * 8 + (b & 7)) * 1024 + l * 16;
#pragma unroll
        for (int j = 0; j < 18; ++j) {
            u32x4 o;
#pragma unroll
            for (int e = 0; e < 4; ++e) o[e] = cvtpk(rbf(bflo(v[j][e]) - m) * rs, rbf(bfhi(v[j][e]) - m) * rs);
            *(u32x4*)(dst + (size_t)j * 8 * 1024) = o;
        }
    } else if (b < 128 + 1152) {
        const int idx = (b - 128) * 64 + l;
        const int k = idx >> 7, s = (idx >> 3) & 15, mat = (idx >> 2) & 1, t = idx & 3;
        const u16* wc = (s & 1) ? wv : wk;
        u16 o[8];
#pragma unroll
        for (int e = 0; e < 8; ++e) {
            const int rho = mat * 8 + e; const int head = 2 * s + (t >> 1);
            u16 val = 0;
            if ((t & 1) == 0) val = wq[(size_t)k * DM + head * DH + rho];
            else if (rho < 2) val = wq[(size_t)k * DM + head * DH + 16 + rho];
            else if (rho >= 4) { const int cc = (t == 1 ? 0 : 12) + rho - 4; if (cc < (t == 1 ? 12 : 18)) val = wc[k * DH + cc]; }
            o[e] = val;
        }
        u32x4 w; w[0] = o[0] | ((unsigned)o[1] << 16); w[1] = o[2] | ((unsigned)o[3] << 16); w[2] = o[4] | ((unsigned)o[5] << 16); w[3] = o[6] | ((unsigned)o[7] << 16);
        *(u32x4*)(ws + WS_WQX + (size_t)idx * 16) = w;
    } else if (b < 1408) {
        const int ks = b - 1280, dh = l & 31;
        if (dh >= 18) { const unsigned val = dh == 18 ? 0x3f803f80u : 0u; *(u32x4*)(ws + WS_VF + ks * 1024 + l * 16) = (u32x4){val, val, val, val}; }
        if (ks < 64 && l >= 32) *(unsigned*)(ws + WS_KF2 + ks * 256 + l * 4) = 0u;
    } else {
        u32x4* f = (u32x4*)(ws + WS_FLAGS);
#pragma unroll
        for (int i = 0; i < 4; ++i) f[i * 64 + l] = (u32x4){0u, 0u, 0u, 0u};
    }
}

constexpr int ST1 = 49152;
constexpr int LDS_D0 = 0, LDS_D1 = 49152, LDS_H = 86016, LDS_BYTES = 159744;

template <int NM>
DI void gemm_step(f32x4 (&acc)[10][4], LAS const char* ap, LAS const char* bp) {
    bf16x8 bfr[4];
#pragma unroll
    for (int n = 0; n < 4; ++n) bfr[n] = lds_b128(bp + n * 1024);
#pragma unroll
    for (int m = 0; m < NM; ++m) {
        const s16x4 lo = lds_tr(ap + m * 4096), hi = lds_tr(ap + m * 4096 + 2048);
        const bf16x8 a = __builtin_shufflevector(lo, hi, 0, 1, 2, 3, 4, 5, 6, 7);
#pragma unroll
        for (int n = 0; n < 4; ++n) acc[m][n] = MFMA16(a, bfr[n], acc[m][n]);
    }
}

__global__ __launch_bounds__(512, 2) void k2_main(const u16* __restrict__ wg, const u16* __restrict__ wu, const u16* __restrict__ wd, char* __restrict__ ws) {
    __shared__ __attribute__((aligned(1024))) char lds[LDS_BYTES];
    const int tid = threadIdx.x, l = tid & 63;
    const int w = __builtin_amdgcn_readfirstlane(tid >> 6);
    const int b = blockIdx.x, s = 2 * (b & 7) + ((b >> 3) & 1), r = b >> 4;
    const int tg = w >> 2, fg = w & 3;
    const unsigned lds0 = (unsigned)(uintptr_t)lds;
    LAS const char* L = (LAS const char*)lds;
    unsigned* flags = (unsigned*)(ws + WS_FLAGS);

    const int dmat = w & 1, dR = (w >> 1) & 1, dcg0 = 5 * (w >> 2);
    const int di = l >> 2, dsig = l & 3;
    const int dmi = dsig ^ (2 * dmat + ((di >> 2) & 1)), dkrow = 8 * (di >> 2) + (di & 3);
    const i32x4 srd_w = make_srd(dmat ? wu : wg, DM * FF * 2);
    const i32x4 srd_q = make_srd(ws + WS_WQX, DM * 1024 * 2);
    const i32x4 srd_x = make_srd(ws + WS_XN, SEQ * DM * 2);
    const i32x4 srd_d = make_srd(wd, FF * DM * 2);
    const unsigned vo_w = dkrow * (FF * 2) + 16 * dmi, vo_q = dkrow * 2048 + 16 * dmi, vo_x = l * 16;
    const unsigned vo_d = dkrow * (DM * 2) + 32 * dmi;
    const unsigned dlds = dR * 2048 + dmat * 1024;
    auto issue1 = [&](int j, unsigned slot) {
        const unsigned so = (32 * j + 4 * dR) * (FF * 2) + 288 * s * 2;
#pragma unroll
        for (int i = 0; i < 4; ++i) dma16(srd_w, vo_w, so + (dcg0 + i) * 64, lds0 + slot + (dcg0 + i) * 4096 + dlds);
        if (w < 4) dma16(srd_w, vo_w, so + 4 * 64, lds0 + slot + 4 * 4096 + dlds);
        else dma16(srd_q, vo_q, (32 * j + 4 * dR) * 2048 + (64 * s + 32 * dmat) * 2, lds0 + slot + 9 * 4096 + dlds);
        dma16(srd_x, vo_x, ((r * 18 + j) * 8 + w) * 1024, lds0 + slot + 40960 + w * 1024);
    };
    auto issue2 = [&](int j, unsigned slot) {
        const unsigned so = (288 * s + 32 * j + 4 * dR) * (DM * 2) + 16 * dmat;
#pragma unroll
        for (int i = 0; i < 4; ++i) dma16(srd_d, vo_d, so + (dcg0 + i) * 128, lds0 + slot + (dcg0 + i) * 4096 + dlds);
        if (w < 4) dma16(srd_d, vo_d, so + 4 * 128, lds0 + slot + 4 * 4096 + dlds);
    };
    const int g4 = l >> 4, q4 = (l >> 2) & 3, p4 = l & 3, matp = p4 >> 1;
    const unsigned a_off = matp * 1024 + 16 * (4 * (4 * g4 + q4) + (fg ^ (2 * matp + (g4 & 1)))) + 8 * (p4 & 1);

    f32x4 acc[10][4];
#pragma unroll
    for (int m = 0; m < 10; ++m)
#pragma unroll
        for (int n = 0; n < 4; ++n) acc[m][n] = (f32x4){0.f, 0.f, 0.f, 0.f};

    issue1(0, 0); issue1(1, ST1);
    {
        unsigned slot = 0, slot2 = 2 * ST1;
        for (int j = 0; j < 18; ++j) {
            if (j < 17) { WAIT_BAR(6); } else { WAIT_BAR(4); }
            if (j < 16) issue1(j + 2, slot2);
            else if (j == 16) issue2(0, LDS_D0);
            else issue2(1, LDS_D1);
            gemm_step<10>(acc, L + slot + a_off, L + slot + 40960 + tg * 4096 + l * 16);
            slot = (slot == 2 * ST1) ? 0 : slot + ST1;
            slot2 = (slot2 == 2 * ST1) ? 0 : slot2 + ST1;
        }
    }
    asm volatile("s_waitcnt lgkmcnt(0)\n\ts_barrier" ::: "memory");
    {
        const int tok = l & 15;
#pragma unroll
        for (int cg = 0; cg < 9; ++cg)
#pragma unroll
            for (int pr = 0; pr < 2; ++pr) {
                float h[4];
#pragma unroll
                for (int e = 0; e < 4; ++e) {
                    auto sw = __builtin_amdgcn_permlane32_swap(__float_as_uint(acc[cg][2 * pr][e]), __float_as_uint(acc[cg][2 * pr + 1][e]), false, false);
                    const float g = __uint_as_float(sw[0]), u = __uint_as_float(sw[1]);
                    h[e] = fmaxf(g, 0.f) * u;
                }
                const int n = 4 * tg + 2 * pr + (l >> 5);
                u32x2 o; o[0] = cvtpk(h[0], h[1]); o[1] = cvtpk(h[2], h[3]);
                *(LAS u32x2*)((LAS char*)lds + LDS_H + (cg * 8 + n) * 1024 + (fg * 16 + tok) * 16 + 8 * (g4 & 1)) = o;
            }
    }
    {
        const float qs = 0.23570226039551584f * 1.4426950408889634f;
        const int head = 2 * s + (fg >> 1), tok = l & 15;
        u16* qp = (u16*)(ws + WS_Q) + ((size_t)head * SEQ + 128 * r + 64 * tg + tok) * 32;
        if ((fg & 1) == 0) {
#pragma unroll
            for (int n = 0; n < 4; ++n) { u32x2 o; o[0] = cvtpk(acc[9][n][0] * qs, acc[9][n][1] * qs); o[1] = cvtpk(acc[9][n][2] * qs, acc[9][n][3] * qs);
                *(u32x2*)(qp + n * 16 * 32 + 4 * g4) = o; }
        } else {
            if (g4 == 0) {
#pragma unroll
                for (int n = 0; n < 4; ++n) *(unsigned*)(qp + n * 16 * 32 + 16) = cvtpk(acc[9][n][0] * qs, acc[9][n][1] * qs);
            }
            const int e = s >> 1;
            if (tg == (e >> 2)) {
                f32x4 cv = acc[9][0];
                if ((e & 3) == 1) cv = acc[9][1]; else if ((e & 3) == 2) cv = acc[9][2]; else if ((e & 3) == 3) cv = acc[9][3];
                const int c0 = (fg == 1 ? 0 : 12) + 4 * (g4 - 1);
                const bool act = g4 >= 1 && c0 < 18;
                const unsigned p0 = cvtpk(cv[0], cv[1]), p1 = cvtpk(cv[2], cv[3]);
                if ((s & 1) == 0) {
                    const int kt = 4 * r + (e >> 1), key = 16 * (e & 1) + tok;
                    if (act) {
                        if (c0 < 16) __hip_atomic_store((GAS unsigned long long*)(ws + WS_KF1 + kt * 1024 + ((c0 >> 3) * 32 + key) * 16 + (c0 & 4) * 2), ((unsigned long long)p1 << 32) | p0, RLX_AGENT);
                        else __hip_atomic_store((GAS unsigned*)(ws + WS_KF2 + kt * 256 + key * 4), p0, RLX_AGENT);
                    }
                    float n2 = 0.f;
                    if (act) { n2 = bflo(p0) * bflo(p0) + bfhi(p0) * bfhi(p0); if (c0 < 16) n2 += bflo(p1) * bflo(p1) + bfhi(p1) * bfhi(p1); }
                    n2 += __shfl_xor(n2, 16); n2 += __shfl_xor(n2, 32);
#pragma unroll
                    for (int o = 1; o < 16; o <<= 1) n2 = fmaxf(n2, __shfl_xor(n2, o));
                    if (l == 0) __hip_atomic_fetch_max((GAS unsigned*)(flags + (fg == 1 ? FL_KMAXA : FL_KMAXB)), __float_as_uint(n2), RLX_AGENT);
                } else {
                    const int ks = 8 * r + e, hh = (tok >> 2) & 1, jj = 4 * (tok >> 3) + (tok & 3);
                    GAS u16* vb = (GAS u16*)(ws + WS_VF + ks * 1024 + hh * 512 + jj * 2);
                    if (act) {
                        __hip_atomic_store(vb + (c0 + 0) * 8, (u16)(p0 & 0xffff), RLX_AGENT);
                        __hip_atomic_store(vb + (c0 + 1) * 8, (u16)(p0 >> 16), RLX_AGENT);
                        if (c0 < 16) { __hip_atomic_store(vb + (c0 + 2) * 8, (u16)(p1 & 0xffff), RLX_AGENT);
                                       __hip_atomic_store(vb + (c0 + 3) * 8, (u16)(p1 >> 16), RLX_AGENT); }
                    }
                }
            }
        }
    }
    WAIT_BAR(0);
    if (tid == 0) __hip_atomic_fetch_add((GAS unsigned*)(flags + FL_KV), 1u, RLX_AGENT);

#pragma unroll
    for (int m = 0; m < 9; ++m)
#pragma unroll
        for (int n = 0; n < 4; ++n) acc[m][n] = (f32x4){0.f, 0.f, 0.f, 0.f};
    for (int j = 0; j < 9; ++j) {
        if (j > 0) { WAIT_BAR(0); if (j < 8) issue2(j + 1, (j & 1) ? LDS_D0 : LDS_D1); }
        gemm_step<9>(acc, L + ((j & 1) ? LDS_D1 : LDS_D0) + a_off, L + LDS_H + (j * 8 + 4 * tg) * 1024 + l * 16);
    }
    {
        char* pp = ws + WS_PART + ((size_t)(r * 16 + s) * 8 + w) * 18 * 1024 + l * 16;
#pragma unroll
        for (int m = 0; m < 9; ++m)
#pragma unroll
            for (int pr = 0; pr < 2; ++pr) {
                u32x4 o; o[0] = cvtpk(acc[m][2 * pr][0], acc[m][2 * pr][1]); o[1] = cvtpk(acc[m][2 * pr][2], acc[m][2 * pr][3]);
                o[2] = cvtpk(acc[m][2 * pr + 1][0], acc[m][2 * pr + 1][1]); o[3] = cvtpk(acc[m][2 * pr + 1][2], acc[m][2 * pr + 1][3]);
                *(u32x4*)(pp + (m * 2 + pr) * 1024) = o;
            }
    }

    if (w == 0) {
        unsigned spins = 0;
        while ((unsigned)__builtin_amdgcn_readfirstlane(__hip_atomic_load((GAS unsigned*)(flags + FL_KV), RLX_AGENT)) < 256u) {
            __builtin_amdgcn_s_sleep(2);
            if (++spins > (1u << 20)) { if (l == 0) __hip_atomic_store((GAS unsigned*)(flags + FL_TMO), 1u, RLX_AGENT); break; }
        }
        __builtin_amdgcn_fence(__ATOMIC_ACQUIRE, "agent");
    }
    WAIT_BAR(0);
    {
        const int hh = w >> 2, kq = w & 3, head = 2 * s + hh, h5 = l >> 5;
        const float kmax2 = __uint_as_float(__hip_atomic_load((GAS unsigned*)(flags + FL_KMAXA), RLX_AGENT)) + __uint_as_float(__hip_atomic_load((GAS unsigned*)(flags + FL_KMAXB), RLX_AGENT));
        bf16x8 q1[4]; u32x2 q2[4];
#pragma unroll
        for (int ct = 0; ct < 4; ++ct) {
            const u16* qp = (const u16*)(ws + WS_Q) + ((size_t)head * SEQ + 128 * r + 32 * ct + (l & 31)) * 32;
            const u32x4 a = *(const u32x4*)(qp + 8 * h5);
            const unsigned t = h5 ? 0u : *(const unsigned*)(qp + 16);
            q1[ct] = __builtin_bit_cast(bf16x8, a);
            float n2 = bflo(t) * bflo(t) + bfhi(t) * bfhi(t);
#pragma unroll
            for (int e = 0; e < 4; ++e) n2 += bflo(a[e]) * bflo(a[e]) + bfhi(a[e]) * bfhi(a[e]);
            n2 += __shfl_xor(n2, 32);
            const float c = sqrtf(n2 * kmax2) * 1.001f + 0.01f;
            const float chi = rbf(c), clo = c - chi;
            q2[ct][0] = t; q2[ct][1] = h5 ? 0u : cvtpk(-chi, -clo);
        }
        const unsigned kone = h5 ? 0u : 0x3f803f80u;
        const char* kf1 = ws + WS_KF1 + kq * 16 * 1024 + l * 16;
        const char* kf2 = ws + WS_KF2 + kq * 16 * 256 + l * 4;
        const char* vf = ws + WS_VF + kq * 32 * 1024 + l * 16;
        LAS float* red = (LAS float*)lds;
        LAS unsigned* bad = (LAS unsigned*)(lds + 98304 + 8192);
        LAS float* mx = (LAS float*)(lds + 98304);
        for (int attempt = 0; attempt < 2; ++attempt) {
            if (attempt == 1) {
                float m[4] = {-INFINITY, -INFINITY, -INFINITY, -INFINITY};
                for (int i = 0; i < 16; ++i) {
                    const bf16x8 k1 = *(const bf16x8*)(kf1 + i * 1024);
                    const u32x2 k2 = {*(const unsigned*)(kf2 + i * 256), kone};
#pragma unroll
                    for (int ct = 0; ct < 4; ++ct) {
                        const u32x2 qz = {q2[ct][0], 0u};
                        f32x16 sc = MFMA32(k1, q1[ct], (f32x16){});
                        sc = MFMA32K8(__builtin_bit_cast(s16x4, k2), __builtin_bit_cast(s16x4, qz), sc);
#pragma unroll
                        for (int e = 0; e < 16; ++e) m[ct] = fmaxf(m[ct], sc[e]);
                    }
                }
#pragma unroll
                for (int ct = 0; ct < 4; ++ct) { m[ct] = fmaxf(m[ct], __shfl_xor(m[ct], 32)); if (l < 32) mx[(w * 4 + ct) * 32 + l] = m[ct]; }
                __syncthreads();
#pragma unroll
                for (int ct = 0; ct < 4; ++ct) {
                    float mm = mx[((hh * 4 + 0) * 4 + ct) * 32 + (l & 31)];
#pragma unroll
                    for (int k = 1; k < 4; ++k) mm = fmaxf(mm, mx[((hh * 4 + k) * 4 + ct) * 32 + (l & 31)]);
                    const float chi = rbf(mm), clo = mm - chi;
                    q2[ct][1] = h5 ? 0u : cvtpk(-chi, -clo);
                }
            }
            f32x16 o[4];
#pragma unroll
            for (int ct = 0; ct < 4; ++ct) o[ct] = (f32x16){};
            bf16x8 k1 = *(const bf16x8*)(kf1), v0 = *(const bf16x8*)(vf), v1 = *(const bf16x8*)(vf + 1024);
            unsigned k2a = *(const unsigned*)(kf2);
            for (int i = 0; i < 16; ++i) {
                const int in = i < 15 ? i + 1 : 15;
                const bf16x8 k1n = *(const bf16x8*)(kf1 + in * 1024), v0n = *(const bf16x8*)(vf + in * 2048), v1n = *(const bf16x8*)(vf + in * 2048 + 1024);
                const unsigned k2n = *(const unsigned*)(kf2 + in * 256);
                const u32x2 k2 = {k2a, kone};
#pragma unroll
                for (int ct = 0; ct < 4; ++ct) {
                    f32x16 sc = MFMA32(k1, q1[ct], (f32x16){});
                    sc = MFMA32K8(__builtin_bit_cast(s16x4, k2), __builtin_bit_cast(s16x4, q2[ct]), sc);
                    u32x4 p0, p1;
#pragma unroll
                    for (int e = 0; e < 4; ++e) {
                        p0[e] = cvtpk(__builtin_amdgcn_exp2f(sc[2 * e]), __builtin_amdgcn_exp2f(sc[2 * e + 1]));
                        p1[e] = cvtpk(__builtin_amdgcn_exp2f(sc[8 + 2 * e]), __builtin_amdgcn_exp2f(sc[8 + 2 * e + 1]));
                    }
                    o[ct] = MFMA32(v0, __builtin_bit_cast(bf16x8, p0), o[ct]);
                    o[ct] = MFMA32(v1, __builtin_bit_cast(bf16x8, p1), o[ct]);
                }
                k1 = k1n; v0 = v0n; v1 = v1n; k2a = k2n;
            }
            if (tid == 0) *bad = 0u;
#pragma unroll
            for (int ct = 0; ct < 4; ++ct)
#pragma unroll
                for (int e = 0; e < 12; ++e) red[((w * 4 + ct) * 12 + e) * 64 + l] = o[ct][e];
            __syncthreads();
            for (int ch = tid; ch < 640; ch += 512) {
                const int tok = ch / 5, c5 = ch - tok * 5, ct = tok >> 5, q32 = tok & 31;
                float val[8];
#pragma unroll
                for (int i = 0; i < 8; ++i) {
                    const int e = 8 * c5 + i;
                    float rsl = 0.f;
                    if (e < 36) {
                        const int h2 = e >= 18 ? 1 : 0, dh = e - 18 * h2;
                        const int reg = (dh & 3) + 4 * (dh >> 3), ln = ((dh >> 2) & 1) * 32 + q32;
                        float a = 0.f, ls = 0.f;
#pragma unroll
                        for (int k = 0; k < 4; ++k) { a += red[(((h2 * 4 + k) * 4 + ct) * 12 + reg) * 64 + ln]; ls += red[(((h2 * 4 + k) * 4 + ct) * 12 + 10) * 64 + q32]; }
                        if (!(ls >= 8.67e-19f && ls < 3.0e38f)) *bad = 1u;
                        rsl = a / ls;
                    }
                    val[i] = rsl;
                }
                u32x4 ov; ov[0] = cvtpk(val[0], val[1]); ov[1] = cvtpk(val[2], val[3]); ov[2] = cvtpk(val[4], val[5]); ov[3] = cvtpk(val[6], val[7]);
                *(u32x4*)(ws + WS_O + ((size_t)(r * 16 + s) * 128 + tok) * 80 + c5 * 16) = ov;
            }
            __syncthreads();
            const unsigned isbad = *bad;
            __syncthreads();
            if (!isbad) break;
        }
    }
}

__global__ __launch_bounds__(576) void k3_final(const u16* __restrict__ x, const u16* __restrict__ wo, const char* __restrict__ ws, u16* __restrict__ out) {
    __shared__ __attribute__((aligned(1024))) char lds[9 * 10240];
    const int tid = threadIdx.x, l = tid & 63;
    const int w = __builtin_amdgcn_readfirstlane(tid >> 6);
    const int b = blockIdx.x, r = b >> 4, u = b & 15, tq = u >> 2, v = u & 3;
    const int n0 = 144 * v + 16 * w, cg = n0 >> 6, mi = (n0 >> 4) & 3, tg = tq >> 1, pr = tq & 1;
    const int g4 = l >> 4, q4 = (l >> 2) & 3, p4 = l & 3;
    const unsigned lds0 = (unsigned)(uintptr_t)lds + w * 10240;
    const i32x4 srd_o = make_srd(wo, DM * DM * 2);
    const int dR = l >> 5, di = (l >> 1) & 15, dch = l & 1, dk = 8 * (di >> 2) + (di & 3) + 4 * dR;
    f32x4 acc[2] = {(f32x4){0.f, 0.f, 0.f, 0.f}, (f32x4){0.f, 0.f, 0.f, 0.f}};
    const char* ob = ws + WS_O + (size_t)r * 16 * 128 * 80;
    const int tl0 = 32 * tq + (l & 15);
    for (int half = 0; half < 2; ++half) {
#pragma unroll
        for (int i = 0; i < 10; ++i) {
            const int kp = 32 * (half * 10 + i) + dk, sp = kp / 40, d = kp - 40 * sp, row = 36 * sp + (d < 36 ? d : 35);
            dma16(srd_o, row * (DM * 2) + (n0 + 8 * dch) * 2, 0, lds0 + i * 1024);
        }
        bf16x8 bf[10][2];
#pragma unroll
        for (int i = 0; i < 10; ++i) {
            const int kp = 32 * (half * 10 + i) + 8 * g4, sp = kp / 40, d = kp - 40 * sp;
#pragma unroll
            for (int nn = 0; nn < 2; ++nn) bf[i][nn] = *(const bf16x8*)(ob + ((size_t)sp * 128 + tl0 + 16 * nn) * 80 + d * 2);
        }
        asm volatile("s_waitcnt vmcnt(0)" ::: "memory");
        LAS const char* ap = (LAS const char*)lds + w * 10240 + (4 * g4 + q4) * 32 + 8 * p4;
#pragma unroll
        for (int i = 0; i < 10; ++i) {
            const s16x4 lo = lds_tr(ap + i * 1024), hi = lds_tr(ap + i * 1024 + 512);
            const bf16x8 a = __builtin_shufflevector(lo, hi, 0, 1, 2, 3, 4, 5, 6, 7);
            acc[0] = MFMA16(a, bf[i][0], acc[0]); acc[1] = MFMA16(a, bf[i][1], acc[1]);
        }
        asm volatile("s_waitcnt lgkmcnt(0)" ::: "memory");
    }
    const char* pp = ws + WS_PART + ((((size_t)r * 16 * 8 + tg * 4 + mi) * 9 + cg) * 2 + pr) * 1024 + l * 16;
#pragma unroll
    for (int sp = 0; sp < 16; ++sp) {
        const u32x4 p = *(const u32x4*)(pp + (size_t)sp * 8 * 18 * 1024);
        acc[0][0] += bflo(p[0]); acc[0][1] += bfhi(p[0]); acc[0][2] += bflo(p[1]); acc[0][3] += bfhi(p[1]);
        acc[1][0] += bflo(p[2]); acc[1][1] += bfhi(p[2]); acc[1][2] += bflo(p[3]); acc[1][3] += bfhi(p[3]);
    }
#pragma unroll
    for (int nn = 0; nn < 2; ++nn) {
        const size_t off = (size_t)(128 * r + tl0 + 16 * nn) * DM + n0 + 4 * g4;
        const u32x2 xv = *(const u32x2*)(x + off);
        u32x2 o; o[0] = cvtpk(acc[nn][0] + bflo(xv[0]), acc[nn][1] + bfhi(xv[0])); o[1] = cvtpk(acc[nn][2] + bflo(xv[1]), acc[nn][3] + bfhi(xv[1]));
        *(u32x2*)(out + off) = o;
    }
}

extern "C" void kernel_launch(void* const* d_in, const int* in_sizes, int n_in, void* d_out, int out_size, void* d_ws, size_t ws_size, hipStream_t stream) {
    const u16* x = (const u16*)d_in[0]; const u16* wq = (const u16*)d_in[1]; const u16* wk = (const u16*)d_in[2]; const u16* wv = (const u16*)d_in[3];
    const u16* wo = (const u16*)d_in[4]; const u16* wg = (const u16*)d_in[6]; const u16* wu = (const u16*)d_in[7]; const u16* wd = (const u16*)d_in[8];
    char* ws = (char*)d_ws;
    k1_prep<<<1409, 64, 0, stream>>>(x, wq, wk, wv, ws);
    k2_main<<<256, 512, 0, stream>>>(wg, wu, wd, ws);
    k3_final<<<256, 576, 0, stream>>>(x, wo, ws, (u16*)d_out);
}
```

```cpp
#include <hip/hip_runtime.h>
#include <hip/hip_bf16.h>
#include <stdint.h>

typedef unsigned short u16;
typedef short bf16x8 __attribute__((ext_vector_type(8)));
typedef short s16x4 __attribute__((ext_vector_type(4)));
typedef float f32x4 __attribute__((ext_vector_type(4)));
typedef float f32x16 __attribute__((ext_vector_type(16)));
typedef int i32x4 __attribute__((ext_vector_type(4)));
typedef unsigned u32x4 __attribute__((ext_vector_type(4)));
typedef unsigned u32x2 __attribute__((ext_vector_type(2)));
typedef __bf16 bf16x2_t __attribute__((ext_vector_type(2)));
typedef float f32x2 __attribute__((ext_vector_type(2)));
typedef short v4i16_t __attribute__((ext_vector_type(4)));
#define LAS __attribute__((address_space(3)))
#define GAS __attribute__((address_space(1)))
#define DI __device__ __forceinline__

constexpr int SEQ = 2048, DM = 576, NH = 32, DH = 18, FF = 4608;

constexpr int KVCH = 37888;
constexpr int KV_V = 16384, KV_K2 = 35840;
constexpr int WOIMG = 120 * 1024;
constexpr size_t WS_FLAGS = 0;
constexpr size_t WS_XN    = 16384;
constexpr size_t WS_WQX   = WS_XN + (size_t)SEQ * DM * 2;
constexpr size_t WS_WO    = WS_WQX + (size_t)DM * 1024 * 2;
constexpr size_t WS_KV    = WS_WO + (size_t)8 * WOIMG;
constexpr size_t WS_O     = WS_KV + 4 * KVCH;
constexpr size_t WS_PART  = WS_O + (size_t)256 * 128 * 80;
constexpr size_t WS_END   = WS_PART + (size_t)256 * 8 * 18 * 1024;
constexpr int FL_KV = 0, FL_KMAXA = 64, FL_KMAXB = 128, FL_TMO = 192, FL_P = 256;
constexpr unsigned O_EMPTY = 0xffffffffu;

DI float bflo(unsigned v) { return __uint_as_float(v << 16); }
DI float bfhi(unsigned v) { return __uint_as_float(v & 0xffff0000u); }
DI unsigned cvtpk(float lo, float hi) { return __builtin_bit_cast(unsigned, __builtin_convertvector((f32x2){lo, hi}, bf16x2_t)); }
DI float rbf(float f) { return bflo(cvtpk(f, f)); }
DI i32x4 make_srd(const void* p, unsigned bytes) {
    unsigned long long a = (unsigned long long)p; i32x4 s;
    s.x = __builtin_amdgcn_readfirstlane((unsigned)a); s.y = __builtin_amdgcn_readfirstlane((unsigned)(a >> 32) & 0xffffu);
    s.z = (int)bytes; s.w = 0x00020000; return s;
}
DI void dma16(i32x4 srd, unsigned voff, unsigned soff, unsigned lds_addr) {
    unsigned keep;
    asm volatile("s_mov_b32 %0, m0\n\ts_mov_b32 m0, %4\n\ts_nop 0\n\tbuffer_load_dwordx4 %1, %2, %3 offen lds\n\ts_mov_b32 m0, %0"
                 : "=&s"(keep) : "v"(voff), "s"(srd), "s"(soff), "s"(lds_addr) : "memory");
}
DI void dma16_sc1(i32x4 srd, unsigned voff, unsigned soff, unsigned lds_addr) {
    unsigned keep;
    asm volatile("s_mov_b32 %0, m0\n\ts_mov_b32 m0, %4\n\ts_nop 0\n\tbuffer_load_dwordx4 %1, %2, %3 offen sc1 lds\n\ts_mov_b32 m0, %0"
                 : "=&s"(keep) : "v"(voff), "s"(srd), "s"(soff), "s"(lds_addr) : "memory");
}
DI void dma4(i32x4 srd, unsigned voff, unsigned lds_addr) {
    unsigned keep;
    asm volatile("s_mov_b32 %0, m0\n\ts_mov_b32 m0, %3\n\ts_nop 0\n\tbuffer_load_dword %1, %2, 0 offen lds\n\ts_mov_b32 m0, %0"
                 : "=&s"(keep) : "v"(voff), "s"(srd), "s"(lds_addr) : "memory");
}
#define WAIT_BAR(N) asm volatile("s_waitcnt vmcnt(" #N ") lgkmcnt(0)\n\ts_barrier" ::: "memory")
#define LBAR() asm volatile("s_waitcnt lgkmcnt(0)\n\ts_barrier" ::: "memory")
#define SB() __builtin_amdgcn_sched_barrier(0)
#define FENCE() asm volatile("" ::: "memory")
#define MFMA16(a, b, c) __builtin_amdgcn_mfma_f32_16x16x32_bf16(a, b, c, 0, 0, 0)
#define MFMA32(a, b, c) __builtin_amdgcn_mfma_f32_32x32x16_bf16(a, b, c, 0, 0, 0)
#define MFMA32K8(a, b, c) __builtin_amdgcn_mfma_f32_32x32x8bf16_1k(a, b, c, 0, 0, 0)
#define EXP2(x) __builtin_amdgcn_exp2f(x)
DI s16x4 lds_tr(LAS const char* p) { return __builtin_bit_cast(s16x4, __builtin_amdgcn_ds_read_tr16_b64_v4i16((LAS v4i16_t*)p)); }
DI bf16x8 lds_b128(LAS const char* p) { return *(LAS const bf16x8*)p; }
DI bf16x8 lds_afrag(LAS const char* p) { const s16x4 lo = lds_tr(p), hi = lds_tr(p + 2048); return __builtin_shufflevector(lo, hi, 0, 1, 2, 3, 4, 5, 6, 7); }
#define RLX_AGENT __ATOMIC_RELAXED, __HIP_MEMORY_SCOPE_AGENT
DI int fresh_lane() { int t = __builtin_amdgcn_mbcnt_hi(~0u, __builtin_amdgcn_mbcnt_lo(~0u, 0u)); asm volatile("" : "+v"(t)); return t; }
DI void st_agent64(void* p, unsigned lo, unsigned hi) { __hip_atomic_store((GAS unsigned long long*)p, ((unsigned long long)hi << 32) | lo, RLX_AGENT); }
DI void wait_count(unsigned* word, unsigned want, unsigned* tmo) {
    unsigned spins = 0;
    while ((unsigned)__builtin_amdgcn_readfirstlane(__hip_atomic_load((GAS unsigned*)word, RLX_AGENT)) < want) {
        __builtin_amdgcn_s_sleep(1);
        if (++spins > (1u << 20)) { __hip_atomic_store((GAS unsigned*)tmo, 1u, RLX_AGENT); break; }
    }
    FENCE();
}
#define STAMP_DECL
#define STAMP(K, i)
#define STAMP_END(K)

constexpr int K1_BLOCKS = 945;
__global__ __launch_bounds__(256) void k1_prep(const u16* __restrict__ x, const u16* __restrict__ wq, const u16* __restrict__ wk, const u16* __restrict__ wv,
                                                const u16* __restrict__ wo, const u16* __restrict__ wg, const u16* __restrict__ wu, const u16* __restrict__ wd,
                                                char* __restrict__ ws) {
    const int b = blockIdx.x, tid = threadIdx.x, l = tid & 63;
    STAMP_DECL;
    STAMP(0, 0);
    if (b < 128) {
        const int tl = tid >> 4, tok = b * 16 + tl, sub = l & 15;
        const u32x4* src = (const u32x4*)(x + (size_t)tok * DM);
        u32x4 v[5];
#pragma unroll
        for (int i = 0; i < 4; ++i) v[i] = src[sub + 16 * i];
        v[4] = sub < 8 ? src[64 + sub] : (u32x4){0u, 0u, 0u, 0u};
        float s = 0.f;
#pragma unroll
        for (int i = 0; i < 5; ++i)
#pragma unroll
            for (int e = 0; e < 4; ++e) s += bflo(v[i][e]) + bfhi(v[i][e]);
#pragma unroll
        for (int o = 1; o < 16; o <<= 1) s += __shfl_xor(s, o);
        const float mean = s * (1.0f / DM);
        float q = 0.f;
#pragma unroll
        for (int i = 0; i < 5; ++i)
            if (i < 4 || sub < 8) {
#pragma unroll
                for (int e = 0; e < 4; ++e) { const float a = bflo(v[i][e]) - mean, bb = bfhi(v[i][e]) - mean; q += a * a + bb * bb; }
            }
#pragma unroll
        for (int o = 1; o < 16; o <<= 1) q += __shfl_xor(q, o);
        const float m = rbf(mean), var = rbf(q * (1.0f / DM));
        const float sd = rbf(sqrtf(rbf(var + 1e-5f)));
        const float rs = 1.0f / sd;
        char* dst = ws + WS_XN + ((size_t)(b >> 3) * 18 * 8 + (b & 7)) * 1024 + tl * 16;
#pragma unroll
        for (int i = 0; i < 5; ++i)
            if (i < 4 || sub < 8) {
                u32x4 o;
#pragma unroll
                for (int e = 0; e < 4; ++e) o[e] = cvtpk(rbf(bflo(v[i][e]) - m) * rs, rbf(bfhi(v[i][e]) - m) * rs);
                const int c = sub + 16 * i;
                *(u32x4*)(dst + (size_t)(c >> 2) * 8192 + (c & 3) * 256) = o;
            }
    } else if (b < 256) {
        __shared__ __attribute__((aligned(16))) char sink[1024];
        const int g = b & 7, i0 = ((b - 128) >> 3) * 1024 + tid;
        const unsigned dst = (unsigned)(uintptr_t)sink + __builtin_amdgcn_readfirstlane(tid >> 6) * 256;
        const i32x4 sg = make_srd(wg, DM * FF * 2), su = make_srd(wu, DM * FF * 2), sd = make_srd(wd, FF * DM * 2);
#pragma unroll
        for (int i = 0; i < 4; ++i) {
            const int li = i0 + 256 * i;
            const int k0 = li, k1 = li - 5184, k2 = li - 10368;
            dma4(sg, k0 < 5184 ? (unsigned)((k0 / 9) * (FF * 2) + g * 1152 + (k0 % 9) * 128) : 0xfffffff0u, dst);
            dma4(su, (k1 >= 0 && k1 < 5184) ? (unsigned)((k1 / 9) * (FF * 2) + g * 1152 + (k1 % 9) * 128) : 0xfffffff0u, dst);
            dma4(sd, (k2 >= 0 && k2 < 5184) ? (unsigned)(g * 576 * (DM * 2) + k2 * 128) : 0xfffffff0u, dst);
        }
        asm volatile("s_waitcnt vmcnt(0)" ::: "memory");
    } else if (b < 544) {
        const int idx = (b - 256) * 256 + tid;
        const int k = idx >> 7, s = (idx >> 3) & 15, mat = (idx >> 2) & 1, t = idx & 3;
        const u16* wc = (s & 1) ? wv : wk;
        u16 o[8];
#pragma unroll
        for (int e = 0; e < 8; ++e) {
            const int rho = mat * 8 + e; const int head = 2 * s + (t >> 1);
            u16 val = 0;
            if ((t & 1) == 0) val = wq[(size_t)k * DM + head * DH + rho];
            else if (rho < 2) val = wq[(size_t)k * DM + head * DH + 16 + rho];
            else if (rho >= 4) { const int cc = (t == 1 ? 0 : 12) + rho - 4; if (cc < (t == 1 ? 12 : 18)) val = wc[k * DH + cc]; }
            o[e] = val;
        }
        u32x4 w; w[0] = o[0] | ((unsigned)o[1] << 16); w[1] = o[2] | ((unsigned)o[3] << 16); w[2] = o[4] | ((unsigned)o[5] << 16); w[3] = o[6] | ((unsigned)o[7] << 16);
        *(u32x4*)(ws + WS_WQX + (size_t)idx * 16) = w;
    } else if (b < 784) {
        const int idx = (b - 544) * 256 + tid, cgp = idx / 7680, rem = idx - cgp * 7680, p = rem >> 6, pos = rem & 63;
        const int m0 = cgp < 4 ? 5 * cgp : 20 + 4 * (cgp - 4);
        const int pp = p / 40, js = (p % 40) >> 1, R = p & 1, i = pos >> 2, ch = (pos & 3) ^ (2 * ((i >> 2) & 1));
        const int kp = 32 * js + 8 * (i >> 2) + (i & 3) + 4 * R, sp = kp / 40, d = kp - 40 * sp, hd = d >= 20 ? 1 : 0, dd = d - 20 * hd;
        const int row = 36 * sp + 18 * hd + (dd < 18 ? dd : 17), col = min(16 * (m0 + 2 * pp) + 8 * ch, DM - 8);
        *(u32x4*)(ws + WS_WO + (size_t)idx * 16) = *(const u32x4*)(wo + (size_t)row * DM + col);
    } else if (b < 944) {
        u32x4* p = (u32x4*)(ws + WS_O) + (b - 784) * 1024 + tid;
#pragma unroll
        for (int i = 0; i < 4; ++i) p[i * 256] = (u32x4){O_EMPTY, O_EMPTY, O_EMPTY, O_EMPTY};
    } else {
        const int c = tid >> 6, rest = tid & 63;
        *(u32x4*)(ws + WS_KV + c * KVCH + KV_V + rest * 304 + 18 * 16) = (u32x4){0x3f803f80u, 0x3f803f80u, 0x3f803f80u, 0x3f803f80u};
        u32x4* f = (u32x4*)(ws + WS_FLAGS);
#pragma unroll
        for (int i = 0; i < 4; ++i) f[i * 256 + tid] = (u32x4){0u, 0u, 0u, 0u};
    }
    STAMP(0, 1); STAMP_END(0);
}

constexpr int ST1 = 49152;
constexpr int LDS_D0 = 0, LDS_D1 = 49152, LDS_H = 86016, LDS_Q1 = 36864, LDS_Q2 = 45056;
constexpr int LDS_D2 = 126976;
constexpr int LDS_KV0 = 125952;
constexpr int LDS_ZERO = 122880;
constexpr int LDS_CTL = LDS_ZERO + 640;
constexpr int LDS_MX = LDS_CTL + 64;
constexpr int LDS_RED = 3 * KVCH - 24 * 1024;
constexpr int LDS_BYTES = 163840;
DI unsigned wo_lds(int p) { return p < 37 ? LDS_KV0 + p * 1024 : p < 74 ? (p - 37) * 1024 : p < 111 ? KVCH + (p - 74) * 1024 : 2 * KVCH + (p - 111) * 1024; }

template <int NM, bool FIRST, int ND, int DPG, class Dma>
DI void gemm_step(f32x4 (&acc)[10][4], bf16x8 (&ca)[2], bf16x8 (&cb)[4], bf16x8 (&nb)[4], LAS const char* ap, LAS const char* bp, const Dma& dma) {
    bf16x8 a[10];
#pragma unroll
    for (int n = 0; n < 4; ++n) nb[n] = lds_b128(bp + n * 1024);
    a[0] = lds_afrag(ap); a[1] = lds_afrag(ap + 4096); a[2] = lds_afrag(ap + 2 * 4096);
    SB();
    int d = 0;
    if (!FIRST) {
#pragma unroll
        for (int k = 0; k < DPG; ++k) if (d < ND) dma(d++);
#pragma unroll
        for (int n = 0; n < 4; ++n) acc[NM - 2][n] = MFMA16(ca[0], cb[n], acc[NM - 2][n]);
        SB();
#pragma unroll
        for (int k = 0; k < DPG; ++k) if (d < ND) dma(d++);
#pragma unroll
        for (int n = 0; n < 4; ++n) acc[NM - 1][n] = MFMA16(ca[1], cb[n], acc[NM - 1][n]);
        SB();
    }
#pragma unroll
    for (int m = 0; m < NM - 2; ++m) {
        if (m + 3 < NM) a[m + 3] = lds_afrag(ap + (m + 3) * 4096);
#pragma unroll
        for (int k = 0; k < DPG; ++k) if (d < ND) dma(d++);
#pragma unroll
        for (int n = 0; n < 4; ++n) acc[m][n] = MFMA16(a[m], nb[n], acc[m][n]);
        SB();
    }
    ca[0] = a[NM - 2]; ca[1] = a[NM - 1];
}
template <int NM>
DI void gemm_flush(f32x4 (&acc)[10][4], const bf16x8 (&ca)[2], const bf16x8 (&cb)[4]) {
#pragma unroll
    for (int n = 0; n < 4; ++n) acc[NM - 2][n] = MFMA16(ca[0], cb[n], acc[NM - 2][n]);
#pragma unroll
    for (int n = 0; n < 4; ++n) acc[NM - 1][n] = MFMA16(ca[1], cb[n], acc[NM - 1][n]);
}

__global__ __launch_bounds__(512, 2) void k2_main(const u16* __restrict__ x, const u16* __restrict__ wg, const u16* __restrict__ wu,
                                                   const u16* __restrict__ wd, char* __restrict__ ws, u16* __restrict__ out) {
    __shared__ __attribute__((aligned(1024))) char lds[LDS_BYTES];
    const int tid = threadIdx.x, l = tid & 63;
    const int w = __builtin_amdgcn_readfirstlane(tid >> 6);
    const int b = blockIdx.x, s = 2 * (b & 7) + ((b >> 3) & 1), r = b >> 4;
    const int tg = w >> 2, fg = w & 3;
    const unsigned lds0 = (unsigned)(uintptr_t)lds;
    LAS char* L = (LAS char*)lds;
    unsigned* flags = (unsigned*)(ws + WS_FLAGS);

    const int dmat = w & 1, dR = (w >> 1) & 1, dcg0 = 5 * (w >> 2);
    const int di = l >> 2, dsig = l & 3;
    const int dmi = dsig ^ (2 * dmat + ((di >> 2) & 1)), dkrow = 8 * (di >> 2) + (di & 3);
    const i32x4 srd_w = make_srd(dmat ? wu : wg, DM * FF * 2);
    const i32x4 srd_q = make_srd(ws + WS_WQX, DM * 1024 * 2);
    const i32x4 srd_x = make_srd(ws + WS_XN, SEQ * DM * 2);
    const i32x4 srd_d = make_srd(wd, FF * DM * 2);
    const unsigned vo_w = dkrow * (FF * 2) + 16 * dmi, vo_q = dkrow * 2048 + 16 * dmi, vo_x = l * 16;
    const unsigned vo_d = dkrow * (DM * 2) + 32 * dmi;
    const unsigned dlds = lds0 + dcg0 * 4096 + dR * 2048 + dmat * 1024;
    auto piece1 = [&](int j, unsigned slot, int i) {
        const unsigned so = (32 * j + 4 * dR) * (FF * 2) + 288 * s * 2 + dcg0 * 64;
        if (i < 4) dma16(srd_w, vo_w, so + i * 64, dlds + slot + i * 4096);
        else if (i == 4) {
            if (w < 4) dma16(srd_w, vo_w, so + 4 * 64, dlds + slot + 4 * 4096);
            else dma16(srd_q, vo_q, (32 * j + 4 * dR) * 2048 + (64 * s + 32 * dmat) * 2, dlds + slot + 4 * 4096);
        } else dma16(srd_x, vo_x, ((r * 18 + j) * 8 + w) * 1024, lds0 + slot + 40960 + w * 1024);
    };
    auto piece2 = [&](int j, unsigned slot, int i) {
        const int ii = (w >= 4 && i == 4) ? 3 : i;
        dma16(srd_d, vo_d, (288 * s + 32 * j + 4 * dR) * (DM * 2) + 16 * dmat + (dcg0 + ii) * 128, dlds + slot + ii * 4096);
    };
    const int g4 = l >> 4, q4 = (l >> 2) & 3, p4 = l & 3, matp = p4 >> 1;
    const unsigned a_off = matp * 1024 + 16 * (4 * (4 * g4 + q4) + (fg ^ (2 * matp + (g4 & 1)))) + 8 * (p4 & 1);

    f32x4 acc[10][4];
#pragma unroll
    for (int m = 0; m < 10; ++m)
#pragma unroll
        for (int n = 0; n < 4; ++n) acc[m][n] = (f32x4){0.f, 0.f, 0.f, 0.f};
    bf16x8 ca[2], bA[4], bB[4];

    STAMP_DECL;
    STAMP(1, 0);
#pragma unroll
    for (int i = 0; i < 6; ++i) piece1(0, 0, i);
#pragma unroll
    for (int i = 0; i < 6; ++i) piece1(1, ST1, i);
    {
        const unsigned bo = 40960 + tg * 4096 + l * 16;
        WAIT_BAR(6);
        STAMP(1, 1);
        gemm_step<10, true, 6, 1>(acc, ca, bB, bA, L + a_off, L + bo, [&](int i) { piece1(2, 2 * ST1, i); });
        unsigned slot = ST1, slot2 = 0;
        for (int j = 1; j < 15; j += 2) {
            WAIT_BAR(6);
            gemm_step<10, false, 6, 1>(acc, ca, bA, bB, L + slot + a_off, L + slot + bo, [&](int i) { piece1(j + 2, slot2, i); });
            slot = (slot == 2 * ST1) ? 0 : slot + ST1; slot2 = (slot2 == 2 * ST1) ? 0 : slot2 + ST1;
            WAIT_BAR(6);
            gemm_step<10, false, 6, 1>(acc, ca, bB, bA, L + slot + a_off, L + slot + bo, [&](int i) { piece1(j + 3, slot2, i); });
            slot = (slot == 2 * ST1) ? 0 : slot + ST1; slot2 = (slot2 == 2 * ST1) ? 0 : slot2 + ST1;
        }
        WAIT_BAR(6);
        gemm_step<10, false, 6, 1>(acc, ca, bA, bB, L + a_off, L + bo, [&](int i) { piece1(17, 2 * ST1, i); });
        WAIT_BAR(6);
        gemm_step<10, false, 5, 1>(acc, ca, bB, bA, L + ST1 + a_off, L + ST1 + bo, [&](int i) { piece2(0, LDS_D0, i); });
        WAIT_BAR(5);
        gemm_step<10, false, 5, 2>(acc, ca, bA, bB, L + 2 * ST1 + a_off, L + 2 * ST1 + bo, [&](int i) { piece2(1, LDS_D1, i); });
        gemm_flush<10>(acc, ca, bB);
        SB(); FENCE();
    }
    if ((fg & 1) && tg == (s >> 3)) {
        const int l = fresh_lane(), tok = l & 15, g4 = l >> 4;
        const int e = s >> 1;
        f32x4 cv = acc[9][0];
        if ((e & 3) == 1) cv = acc[9][1]; else if ((e & 3) == 2) cv = acc[9][2]; else if ((e & 3) == 3) cv = acc[9][3];
        const int c0 = (fg == 1 ? 0 : 12) + 4 * (g4 - 1);
        const bool act = g4 >= 1 && c0 < 18;
        const unsigned p0 = cvtpk(cv[0], cv[1]), p1 = cvtpk(cv[2], cv[3]);
        const int kt = 4 * r + (e >> 1);
        char* kvb = ws + WS_KV + ((kt >> 3) & 3) * KVCH; const int lt = 8 * (kt >> 5) + (kt & 7);
        if ((s & 1) == 0) {
            const int key = 16 * (e & 1) + tok;
            if (act) {
                if (c0 < 16) st_agent64(kvb + lt * 1024 + ((c0 >> 3) * 32 + key) * 16 + (c0 & 4) * 2, p0, p1);
                else __hip_atomic_store((GAS unsigned*)(kvb + KV_K2 + lt * 128 + key * 4), p0, RLX_AGENT);
            }
            float n2 = 0.f;
            if (act) { n2 = bflo(p0) * bflo(p0) + bfhi(p0) * bfhi(p0); if (c0 < 16) n2 += bflo(p1) * bflo(p1) + bfhi(p1) * bfhi(p1); }
            n2 += __shfl_xor(n2, 16); n2 += __shfl_xor(n2, 32);
#pragma unroll
            for (int o = 1; o < 16; o <<= 1) n2 = fmaxf(n2, __shfl_xor(n2, o));
            if (l == 0) __hip_atomic_fetch_max((GAS unsigned*)(flags + (fg == 1 ? FL_KMAXA : FL_KMAXB)), __float_as_uint(n2), RLX_AGENT);
        } else {
            const int hv = (tok >> 2) & 1, jj = 4 * (tok >> 3) + (tok & 3);
            GAS u16* vb = (GAS u16*)(kvb + KV_V + lt * 1216 + (e & 1) * 608 + hv * 304 + jj * 2);
            if (act) {
                __hip_atomic_store(vb + (c0 + 0) * 8, (u16)(p0 & 0xffff), RLX_AGENT);
                __hip_atomic_store(vb + (c0 + 1) * 8, (u16)(p0 >> 16), RLX_AGENT);
                if (c0 < 16) { __hip_atomic_store(vb + (c0 + 2) * 8, (u16)(p1 & 0xffff), RLX_AGENT);
                               __hip_atomic_store(vb + (c0 + 3) * 8, (u16)(p1 >> 16), RLX_AGENT); }
            }
        }
    }
    LBAR();
    STAMP(1, 2);
    {
        const int l = fresh_lane(), tok = l & 15, g4 = l >> 4;
#pragma unroll
        for (int cg = 0; cg < 9; ++cg)
#pragma unroll
            for (int pr = 0; pr < 2; ++pr) {
                float h[4];
#pragma unroll
                for (int e = 0; e < 4; ++e) {
                    auto sw = __builtin_amdgcn_permlane32_swap(__float_as_uint(acc[cg][2 * pr][e]), __float_as_uint(acc[cg][2 * pr + 1][e]), false, false);
                    h[e] = fmaxf(__uint_as_float(sw[0]), 0.f) * __uint_as_float(sw[1]);
                }
                const int n = 4 * tg + 2 * pr + (l >> 5);
                u32x2 o; o[0] = cvtpk(h[0], h[1]); o[1] = cvtpk(h[2], h[3]);
                *(LAS u32x2*)(L + LDS_H + ((8 - cg) * 8 + n) * 1024 + (fg * 16 + tok) * 16 + 8 * (g4 & 1)) = o;
            }
        const float qs = 0.23570226039551584f * 1.4426950408889634f;
        const int hh = fg >> 1;
#pragma unroll
        for (int n = 0; n < 4; ++n) {
            const int ct = 2 * tg + (n >> 1), query = 16 * (n & 1) + tok;
            const unsigned p0 = cvtpk(acc[9][n][0] * qs, acc[9][n][1] * qs), p1 = cvtpk(acc[9][n][2] * qs, acc[9][n][3] * qs);
            if ((fg & 1) == 0) *(LAS u32x2*)(L + LDS_Q1 + (hh * 4 + ct) * 1024 + ((g4 >> 1) * 32 + query) * 16 + (g4 & 1) * 8) = (u32x2){p0, p1};
            else if (g4 == 0) *(LAS unsigned*)(L + LDS_Q2 + (hh * 4 + ct) * 128 + query * 4) = p0;
        }
    }
    WAIT_BAR(0);
    if (w == 0 && fresh_lane() == 0) __hip_atomic_fetch_add((GAS unsigned*)(flags + FL_KV), 1u, RLX_AGENT);
    STAMP(1, 3);
    unsigned kmaxa, kmaxb;
    const i32x4 srd_kv = make_srd(ws + WS_KV, 4 * KVCH);
    auto piece_kv = [&](int c, int i) {
        const int p = min(w + 8 * i, 36);
        dma16_sc1(srd_kv, vo_x, c * KVCH + p * 1024, lds0 + (c == 0 ? LDS_KV0 : (c - 1) * KVCH) + p * 1024);
    };
#pragma unroll
    for (int m = 0; m < 9; ++m)
#pragma unroll
        for (int n = 0; n < 4; ++n) acc[m][n] = (f32x4){0.f, 0.f, 0.f, 0.f};
    {
        LAS const char* hb = L + LDS_H + 8 * 8192 + 4 * tg * 1024 + l * 16;
        gemm_step<9, true, 0, 1>(acc, ca, bB, bA, L + LDS_D0 + a_off, hb, [](int) {});
        LBAR();
        gemm_step<9, false, 5, 2>(acc, ca, bA, bB, L + LDS_D1 + a_off, hb - 1 * 8192, [&](int i) { piece2(2, LDS_D0, i); });
        STAMP(1, 4);
        WAIT_BAR(0);
        gemm_step<9, false, 5, 2>(acc, ca, bB, bA, L + LDS_D0 + a_off, hb - 2 * 8192, [&](int i) { piece2(3, LDS_D1, i); });
        WAIT_BAR(0);
        gemm_step<9, false, 5, 2>(acc, ca, bA, bB, L + LDS_D1 + a_off, hb - 3 * 8192, [&](int i) { piece2(4, LDS_D0, i); });
        STAMP(1, 5);
        WAIT_BAR(0);
        gemm_step<9, false, 10, 2>(acc, ca, bB, bA, L + LDS_D0 + a_off, hb - 4 * 8192, [&](int i) { if (i < 5) piece2(5, LDS_D1, i); else piece2(6, LDS_D2, i - 5); });
        WAIT_BAR(5);
        gemm_step<9, false, 5, 1>(acc, ca, bA, bB, L + LDS_D1 + a_off, hb - 5 * 8192, [&](int i) { piece2(7, LDS_D0, i); });
        STAMP(1, 6);
        WAIT_BAR(5);
        gemm_step<9, false, 5, 1>(acc, ca, bB, bA, L + LDS_D2 + a_off, hb - 6 * 8192, [&](int i) { piece2(8, LDS_D1, i); });
        if (w == 0) wait_count(flags + FL_KV, 256u, flags + FL_TMO);
        WAIT_BAR(5);
        kmaxa = __hip_atomic_load((GAS unsigned*)(flags + FL_KMAXA), RLX_AGENT); kmaxb = __hip_atomic_load((GAS unsigned*)(flags + FL_KMAXB), RLX_AGENT);
        FENCE();
        gemm_step<9, false, 5, 1>(acc, ca, bA, bB, L + LDS_D0 + a_off, hb - 7 * 8192, [&](int i) { piece_kv(0, i); });
        STAMP(1, 7);
        WAIT_BAR(5);
        gemm_step<9, false, 0, 1>(acc, ca, bB, bA, L + LDS_D1 + a_off, hb - 8 * 8192, [](int) {});
        gemm_flush<9>(acc, ca, bA);
        SB(); FENCE();
    }
    STAMP(1, 8);
    const int la = fresh_lane();
#define l la
    const int hh = w >> 2, kh = (w >> 1) & 1, cp = w & 1;
#define h5 (l >> 5)
    bf16x8 q1[2]; u32x2 q2[2];
#pragma unroll
    for (int c = 0; c < 2; ++c) {
        q1[c] = lds_b128(L + LDS_Q1 + (hh * 4 + 2 * cp + c) * 1024 + l * 16);
        q2[c][0] = *(LAS const unsigned*)(L + LDS_Q2 + (hh * 4 + 2 * cp + c) * 128 + (l & 31) * 4);
    }
    u32x4 pk[18];
#pragma unroll
    for (int m = 0; m < 9; ++m)
#pragma unroll
        for (int pr = 0; pr < 2; ++pr) {
            u32x4 o; o[0] = cvtpk(acc[m][2 * pr][0], acc[m][2 * pr][1]); o[1] = cvtpk(acc[m][2 * pr][2], acc[m][2 * pr][3]);
            o[2] = cvtpk(acc[m][2 * pr + 1][0], acc[m][2 * pr + 1][1]); o[3] = cvtpk(acc[m][2 * pr + 1][2], acc[m][2 * pr + 1][3]);
            pk[m * 2 + pr] = o;
        }
    {
        const float kmax2 = __uint_as_float(kmaxa) + __uint_as_float(kmaxb);
#pragma unroll
        for (int c = 0; c < 2; ++c) {
            const u32x4 a = __builtin_bit_cast(u32x4, q1[c]); const unsigned t = h5 ? 0u : q2[c][0];
            float n2 = bflo(t) * bflo(t) + bfhi(t) * bfhi(t);
#pragma unroll
            for (int e = 0; e < 4; ++e) n2 += bflo(a[e]) * bflo(a[e]) + bfhi(a[e]) * bfhi(a[e]);
            n2 += __shfl_xor(n2, 32);
            const float cb = sqrtf(n2 * kmax2) * 1.001f + 0.01f;
            const float chi = rbf(cb), clo = cb - chi;
            q2[c][0] = t; q2[c][1] = h5 ? 0u : cvtpk(-chi, -clo);
        }
    }
    if (w == 0 && l < 40) *(LAS u32x4*)(L + LDS_ZERO + l * 16) = (u32x4){0u, 0u, 0u, 0u};
    if (w == 0 && l == 40) *(LAS unsigned*)(L + LDS_CTL) = 0u;
    WAIT_BAR(0);
    STAMP(1, 9);
#undef l
    const int lb = fresh_lane();
#define l lb
    const unsigned kone = h5 ? 0u : 0x3f803f80u;
    const bool vrow = (l & 31) < 19;
    const unsigned k1_base = 8 * kh * 1024 + l * 16, k2_base = KV_K2 + 8 * kh * 128 + (l & 31) * 4;
    const unsigned v_base = vrow ? KV_V + 8 * kh * 1216 + h5 * 304 + (l & 31) * 16 : LDS_ZERO, v_mul = vrow ? 1u : 0u;
    const int tgf = s >> 3, cgp = s & 7, nmt = cgp < 4 ? 5 : 4, m0 = cgp < 4 ? 5 * cgp : 20 + 4 * (cgp - 4);
    const int pr = w & 1, qk = w >> 1;
    const i32x4 srd_wo = make_srd(ws + WS_WO + (size_t)cgp * WOIMG, WOIMG);
    auto piece_wo = [&](int p) { dma16(srd_wo, vo_x, p * 1024, lds0 + wo_lds(p)); };
    const __amdgpu_buffer_rsrc_t rpart = __builtin_amdgcn_make_buffer_rsrc(ws + WS_PART + (size_t)r * 16 * 8 * 18 * 1024, 0, 16 * 8 * 18 * 1024, 0x00020000);
    float own[11];
    u32x4 pl[5][4]; u32x2 xv[3];

    f32x16 o0, o1, sX, sY; bf16x8 kA, kB, v0A, v1A, v0B, v1B; unsigned k2A, k2B; u32x4 p0, p1; float e3[4];
    unsigned aK, aK2, aV;
#define SLOT(SC, SN, KN, K2N, CN, OP, V1P, OC, V0C) do { float ea[4], eb[4]; \
        SN = MFMA32(KN, q1[CN], (f32x16){}); SB(); \
        ea[0] = EXP2(SC[0]); ea[1] = EXP2(SC[1]); ea[2] = EXP2(SC[2]); ea[3] = EXP2(SC[3]); \
        p1[2] = cvtpk(e3[0], e3[1]); p1[3] = cvtpk(e3[2], e3[3]); SB(); \
        OP = MFMA32(V1P, __builtin_bit_cast(bf16x8, p1), OP); SB(); \
        eb[0] = EXP2(SC[4]); eb[1] = EXP2(SC[5]); eb[2] = EXP2(SC[6]); eb[3] = EXP2(SC[7]); \
        p0[0] = cvtpk(ea[0], ea[1]); p0[1] = cvtpk(ea[2], ea[3]); SB(); \
        SN = MFMA32K8(__builtin_bit_cast(s16x4, ((u32x2){K2N, kone})), __builtin_bit_cast(s16x4, q2[CN]), SN); SB(); \
        ea[0] = EXP2(SC[8]); ea[1] = EXP2(SC[9]); ea[2] = EXP2(SC[10]); ea[3] = EXP2(SC[11]); \
        p0[2] = cvtpk(eb[0], eb[1]); p0[3] = cvtpk(eb[2], eb[3]); SB(); \
        OC = MFMA32(V0C, __builtin_bit_cast(bf16x8, p0), OC); SB(); \
        e3[0] = EXP2(SC[12]); e3[1] = EXP2(SC[13]); e3[2] = EXP2(SC[14]); e3[3] = EXP2(SC[15]); \
        p1[0] = cvtpk(ea[0], ea[1]); p1[1] = cvtpk(ea[2], ea[3]); SB(); } while (0)
#define PAIR(DK, DK2, DV, H0, H1, H2, H3) do { \
        kB = lds_b128(L + aK + 1024); k2B = *(LAS const unsigned*)(L + aK2 + 128); v0B = lds_b128(L + aV + v_mul * 1216); SB(); \
        H0; SLOT(sX, sY, kA, k2A, 1, o1, v1B, o0, v0A); \
        v1B = lds_b128(L + aV + v_mul * 1216 + 608); SB(); \
        H1; SLOT(sY, sX, kB, k2B, 0, o0, v1A, o1, v0A); \
        aK += (DK); aK2 += (DK2); aV += v_mul * (DV); \
        kA = lds_b128(L + aK); k2A = *(LAS const unsigned*)(L + aK2); v0A = lds_b128(L + aV); SB(); \
        H2; SLOT(sX, sY, kB, k2B, 1, o1, v1A, o0, v0B); \
        v1A = lds_b128(L + aV + 608); SB(); \
        H3; SLOT(sY, sX, kA, k2A, 0, o0, v1B, o1, v0B); } while (0)
#define PAIR_IN(H0, H1, H2, H3) PAIR(2048, 256, 2432, H0, H1, H2, H3)
#define PAIR_JUMP(J, H0, H1, H2, H3) PAIR((J) - 6 * 1024, (J) - 6 * 128, (J) - 6 * 1216, H0, H1, H2, H3)
#define NOP_ ((void)0)
#define ATT_INIT() do { o0 = (f32x16){}; o1 = (f32x16){}; aK = k1_base + LDS_KV0; aK2 = k2_base + LDS_KV0; aV = v_base + v_mul * LDS_KV0; \
        kA = lds_b128(L + aK); k2A = *(LAS const unsigned*)(L + aK2); v0A = lds_b128(L + aV); v1A = lds_b128(L + aV + 608); v1B = v1A; \
        sX = MFMA32(kA, q1[0], (f32x16){}); sX = MFMA32K8(__builtin_bit_cast(s16x4, ((u32x2){k2A, kone})), __builtin_bit_cast(s16x4, q2[0]), sX); \
        p1 = (u32x4){0u, 0u, 0u, 0u}; e3[0] = e3[1] = e3[2] = e3[3] = 0.f; } while (0)
#define ATT_FIN() do { p1[2] = cvtpk(e3[0], e3[1]); p1[3] = cvtpk(e3[2], e3[3]); o1 = MFMA32(v1B, __builtin_bit_cast(bf16x8, p1), o1); } while (0)
    constexpr int J01 = 0 - LDS_KV0, J12 = KVCH;
    {
        const __amdgpu_buffer_rsrc_t rp = __builtin_amdgcn_make_buffer_rsrc(ws + WS_PART + (size_t)(r * 16 + s) * 8 * 18 * 1024, 0, 8 * 18 * 1024, 0x00020000);
#define ST6(B) do { _Pragma("unroll") for (int q_ = 0; q_ < 6; ++q_) __builtin_amdgcn_raw_buffer_store_b128(pk[(B) + q_], rp, vo_x, (w * 18 + (B) + q_) * 1024, 16); SB(); } while (0)
#define KV5(C) do { _Pragma("unroll") for (int q_ = 0; q_ < 5; ++q_) piece_kv(C, q_); SB(); } while (0)
        ATT_INIT();
#define LO(X) do { if (w < 4) { X; } } while (0)
#define HI(X) do { if (w >= 4) { X; } } while (0)
        PAIR_IN(LO(KV5(1)), LO(ST6(0)), HI(KV5(1)), HI(ST6(0)));
        PAIR_IN(LO(KV5(2)), LO(ST6(6)), HI(KV5(2)), HI(ST6(6)));
        PAIR_IN(LO(KV5(3)), LO(ST6(12)), HI(KV5(3)), HI(ST6(12)));
        WAIT_BAR(28);
        PAIR_JUMP(J01, NOP_, NOP_, NOP_, NOP_);
        for (int i = 8; i < 14; i += 2) PAIR_IN(NOP_, NOP_, NOP_, NOP_);
        WAIT_BAR(0);
        if (w == 0 && fresh_lane() == 0) __hip_atomic_fetch_add((GAS unsigned*)(flags + FL_P + r * 64), 1u, RLX_AGENT);
#define WO5(P0) do { _Pragma("unroll") for (int q_ = 0; q_ < 5; ++q_) piece_wo(min((P0) + w * 5 + q_, (P0) + 36)); SB(); } while (0)
        PAIR_JUMP(J12, LO(WO5(0)), NOP_, HI(WO5(0)), NOP_);
        for (int i = 16; i < 22; i += 2) PAIR_IN(NOP_, NOP_, NOP_, NOP_);
        LBAR();
        const unsigned pflag = __hip_atomic_load((GAS unsigned*)(flags + FL_P + r * 64), RLX_AGENT);
        PAIR_JUMP(J12, LO(WO5(37)), NOP_, HI(WO5(37)), NOP_);
        PAIR_IN(NOP_, NOP_, NOP_, NOP_);
        if ((unsigned)__builtin_amdgcn_readfirstlane(pflag) < 16u) wait_count(flags + FL_P + r * 64, 16u, flags + FL_TMO);
#define PLD(T) do { const int m_ = m0 + ((T) < nmt ? (T) : 0); _Pragma("unroll") for (int sp = 0; sp < 4; ++sp) \
            pl[T][sp] = __builtin_amdgcn_raw_buffer_load_b128(rpart, vo_x, ((((4 * qk + sp) * 8 + tgf * 4 + (m_ & 3)) * 9 + (m_ >> 2)) * 2 + pr) * 1024, 16); SB(); } while (0)
        LBAR();
        PAIR_IN(LO(WO5(74)), PLD(0), HI(WO5(74)), PLD(1));
        PAIR_IN(PLD(2), PLD(3), PLD(4), NOP_);
        {
            const int lx = fresh_lane(), tok = lx & 15, g4 = lx >> 4;
#pragma unroll
            for (int u = 0; u < 3; ++u) {
                const int tt = min(qk + 4 * u, 2 * nmt - 1);
                xv[u] = *(const u32x2*)(x + (size_t)(128 * r + 64 * tgf + 32 * pr + 16 * (tt & 1) + tok) * DM + 16 * (m0 + (tt >> 1)) + 4 * g4);
            }
            SB();
        }
        PAIR(0, 0, 0, NOP_, NOP_, NOP_, NOP_);
        ATT_FIN();
    }
    STAMP(1, 10);
#undef l
    const int lc = fresh_lane();
#define l lc
    for (int attempt = 0;; ++attempt) {
        if (attempt == 1) {
            const unsigned kone = h5 ? 0u : 0x3f803f80u;
            const bool vrow = (l & 31) < 19;
            const unsigned k1_base = 8 * kh * 1024 + l * 16, k2_base = KV_K2 + 8 * kh * 128 + (l & 31) * 4;
            const unsigned v_base = vrow ? KV_V + 8 * kh * 1216 + h5 * 304 + (l & 31) * 16 : LDS_ZERO, v_mul = vrow ? 1u : 0u;
            WAIT_BAR(0);
#pragma unroll
            for (int c = 0; c < 4; ++c)
#pragma unroll
                for (int i = 0; i < 5; ++i) piece_kv(c, i);
            if (w == 0 && l < 40) *(LAS u32x4*)(L + LDS_ZERO + l * 16) = (u32x4){0u, 0u, 0u, 0u};
            WAIT_BAR(0);
            float m[2] = {-INFINITY, -INFINITY};
            for (int i = 0; i < 32; ++i) {
                const unsigned to = (i >> 3) == 0 ? LDS_KV0 : ((i >> 3) - 1) * KVCH;
                const bf16x8 k1 = lds_b128(L + k1_base + to + (i & 7) * 1024);
                const u32x2 k2 = {*(LAS const unsigned*)(L + k2_base + to + (i & 7) * 128), kone};
#pragma unroll
                for (int c = 0; c < 2; ++c) {
                    const u32x2 qz = {q2[c][0], 0u};
                    f32x16 sc = MFMA32(k1, q1[c], (f32x16){});
                    sc = MFMA32K8(__builtin_bit_cast(s16x4, k2), __builtin_bit_cast(s16x4, qz), sc);
#pragma unroll
                    for (int e = 0; e < 16; ++e) m[c] = fmaxf(m[c], sc[e]);
                }
            }
            LAS float* mx = (LAS float*)(L + LDS_MX);
#pragma unroll
            for (int c = 0; c < 2; ++c) { m[c] = fmaxf(m[c], __shfl_xor(m[c], 32)); if (l < 32) mx[(w * 2 + c) * 32 + l] = m[c]; }
            LBAR();
#pragma unroll
            for (int c = 0; c < 2; ++c) {
                const float mm = fmaxf(m[c], mx[((w ^ 2) * 2 + c) * 32 + (l & 31)]);
                const float chi = rbf(mm), clo = mm - chi;
                q2[c][1] = h5 ? 0u : cvtpk(-chi, -clo);
            }
            o0 = (f32x16){}; o1 = (f32x16){};
            for (int i = 0; i < 32; ++i) {
                const unsigned to = (i >> 3) == 0 ? LDS_KV0 : ((i >> 3) - 1) * KVCH;
                const bf16x8 k1 = lds_b128(L + k1_base + to + (i & 7) * 1024);
                const u32x2 k2 = {*(LAS const unsigned*)(L + k2_base + to + (i & 7) * 128), kone};
                const unsigned va = v_base + v_mul * (to + (i & 7) * 1216);
                const bf16x8 v0 = lds_b128(L + va), v1 = lds_b128(L + va + 608);
#pragma unroll
                for (int c = 0; c < 2; ++c) {
                    f32x16 sc = MFMA32(k1, q1[c], (f32x16){});
                    sc = MFMA32K8(__builtin_bit_cast(s16x4, k2), __builtin_bit_cast(s16x4, q2[c]), sc);
                    u32x4 pa, pb;
#pragma unroll
                    for (int e = 0; e < 4; ++e) { pa[e] = cvtpk(EXP2(sc[2 * e]), EXP2(sc[2 * e + 1])); pb[e] = cvtpk(EXP2(sc[8 + 2 * e]), EXP2(sc[9 + 2 * e])); }
                    if (c == 0) { o0 = MFMA32(v0, __builtin_bit_cast(bf16x8, pa), o0); o0 = MFMA32(v1, __builtin_bit_cast(bf16x8, pb), o0); }
                    else { o1 = MFMA32(v0, __builtin_bit_cast(bf16x8, pa), o1); o1 = MFMA32(v1, __builtin_bit_cast(bf16x8, pb), o1); }
                }
            }
        }
        LBAR();
        if (attempt == 0) {
            piece_wo(111 + w); if (w == 0) piece_wo(119);
        } else {
#pragma unroll
            for (int q = 0; q < 15; ++q) piece_wo(w * 15 + q);
        }
        LAS float* red = (LAS float*)(L + LDS_RED);
#pragma unroll
        for (int e = 0; e < 11; ++e) { red[(w * 11 + e) * 64 + l] = kh ? o0[e] : o1[e]; own[e] = kh ? o1[e] : o0[e]; }
        LBAR();
#pragma unroll
        for (int e = 0; e < 11; ++e) own[e] += red[((w ^ 2) * 11 + e) * 64 + l];
        const float lsum = __shfl(own[10], l & 31);
        own[10] = lsum;
        if (!(lsum >= 8.67e-19f && lsum < 3.0e38f) && attempt == 0) *(LAS unsigned*)(L + LDS_CTL) = 1u;
        LBAR();
        const unsigned isbad = *(LAS const unsigned*)(L + LDS_CTL);
        if (!(isbad && attempt == 0)) break;
    }
    STAMP(1, 11);
    {
        const float rl = 1.0f / own[10];
        LAS char* ot = L + LDS_RED + (32 * (2 * cp + kh) + (l & 31)) * 80 + 40 * hh;
        *(LAS u32x2*)(ot + 8 * h5) = (u32x2){cvtpk(own[0] * rl, own[1] * rl), cvtpk(own[2] * rl, own[3] * rl)};
        *(LAS u32x2*)(ot + 16 + 8 * h5) = (u32x2){cvtpk(own[4] * rl, own[5] * rl), cvtpk(own[6] * rl, own[7] * rl)};
        if (h5 == 0) *(LAS u32x2*)(ot + 32) = (u32x2){cvtpk(own[8] * rl, own[9] * rl), 0u};
        LBAR();
        const __amdgpu_buffer_rsrc_t rw = __builtin_amdgcn_make_buffer_rsrc(ws + WS_O + (size_t)(r * 16 + s) * 128 * 80, 0, 128 * 80, 0x00020000);
        __builtin_amdgcn_raw_buffer_store_b128(*(LAS const u32x4*)(L + LDS_RED + w * 1024 + l * 16), rw, l * 16, w * 1024, 16);
        if (w < 2) __builtin_amdgcn_raw_buffer_store_b128(*(LAS const u32x4*)(L + LDS_RED + (8 + w) * 1024 + l * 16), rw, l * 16, (8 + w) * 1024, 16);
    }
    {
        const int tok = l & 15, g4 = l >> 4, q4 = (l >> 2) & 3, p4 = l & 3;
        f32x4 fa[5][2];
#pragma unroll
        for (int t = 0; t < 5; ++t) {
            fa[t][0] = (f32x4){0.f, 0.f, 0.f, 0.f}; fa[t][1] = (f32x4){0.f, 0.f, 0.f, 0.f};
#pragma unroll
            for (int sp = 0; sp < 4; ++sp) {
                const u32x4 p = pl[t][sp];
                fa[t][0][0] += bflo(p[0]); fa[t][0][1] += bfhi(p[0]); fa[t][0][2] += bflo(p[1]); fa[t][0][3] += bfhi(p[1]);
                fa[t][1][0] += bflo(p[2]); fa[t][1][1] += bfhi(p[2]); fa[t][1][2] += bflo(p[3]); fa[t][1][3] += bfhi(p[3]);
            }
        }
        STAMP(1, 12);
        const __amdgpu_buffer_rsrc_t ro = __builtin_amdgcn_make_buffer_rsrc(ws + WS_O + (size_t)r * 16 * 128 * 80, 0, 16 * 128 * 80, 0x00020000);
        u32x4 bf[5][2];
        unsigned pend = 0x3ffu;
        for (unsigned spins = 0;; ++spins) {
#pragma unroll
            for (int i = 0; i < 5; ++i) {
                const int kp = 32 * (5 * qk + i) + 8 * g4, sp = kp / 40, d = kp - 40 * sp;
#pragma unroll
                for (int nn = 0; nn < 2; ++nn)
                    if (pend & (1u << (2 * i + nn))) bf[i][nn] = __builtin_amdgcn_raw_buffer_load_b128(ro, (sp * 128 + 64 * tgf + 32 * pr + 16 * nn + tok) * 80 + d * 2, 0, 16);
            }
#pragma unroll
            for (int i = 0; i < 5; ++i)
#pragma unroll
                for (int nn = 0; nn < 2; ++nn)
                    if ((pend & (1u << (2 * i + nn))) && __all(bf[i][nn][0] != O_EMPTY && bf[i][nn][1] != O_EMPTY && bf[i][nn][2] != O_EMPTY && bf[i][nn][3] != O_EMPTY)) pend &= ~(1u << (2 * i + nn));
            if (pend == 0u) break;
            if (spins > (1u << 17)) { __hip_atomic_store((GAS unsigned*)(flags + FL_TMO), 1u, RLX_AGENT); break; }
            __builtin_amdgcn_s_sleep(8);
        }
        WAIT_BAR(0);
        STAMP(1, 13);
        const unsigned ao = (4 * g4 + q4) * 64 + 8 * (p4 & 1);
#pragma unroll
        for (int t = 0; t < 5; ++t)
            if (t < nmt) {
#pragma unroll
                for (int i = 0; i < 5; ++i) {
                    const int p = (t >> 1) * 40 + (5 * qk + i) * 2;
                    const unsigned sw = ao + 16 * ((2 * (t & 1) + (p4 >> 1)) ^ (2 * (g4 & 1)));
                    const s16x4 lo = lds_tr(L + wo_lds(p) + sw), hi = lds_tr(L + wo_lds(p + 1) + sw);
                    const bf16x8 a = __builtin_shufflevector(lo, hi, 0, 1, 2, 3, 4, 5, 6, 7);
                    fa[t][0] = MFMA16(a, __builtin_bit_cast(bf16x8, bf[i][0]), fa[t][0]); fa[t][1] = MFMA16(a, __builtin_bit_cast(bf16x8, bf[i][1]), fa[t][1]);
                }
            }
        STAMP(1, 14);
        LBAR();
        LAS f32x4* ex = (LAS f32x4*)L;
#pragma unroll
        for (int t = 0; t < 5; ++t) { ex[(w * 10 + 2 * t) * 64 + l] = fa[t][0]; ex[(w * 10 + 2 * t + 1) * 64 + l] = fa[t][1]; }
        LBAR();
#pragma unroll
        for (int u = 0; u < 3; ++u) {
            const int tt = qk + 4 * u;
            if (tt < 2 * nmt) {
                f32x4 v = ex[((0 * 2 + pr) * 10 + tt) * 64 + l];
#pragma unroll
                for (int k = 1; k < 4; ++k) v += ex[((k * 2 + pr) * 10 + tt) * 64 + l];
                const size_t off = (size_t)(128 * r + 64 * tgf + 32 * pr + 16 * (tt & 1) + tok) * DM + 16 * (m0 + (tt >> 1)) + 4 * g4;
                *(u32x2*)(out + off) = (u32x2){cvtpk(v[0] + bflo(xv[u][0]), v[1] + bfhi(xv[u][0])), cvtpk(v[2] + bflo(xv[u][1]), v[3] + bfhi(xv[u][1]))};
            }
        }
    }
#undef l
#undef h5
    STAMP(1, 15); STAMP_END(1);
}

extern "C" void kernel_launch(void* const* d_in, const int* in_sizes, int n_in, void* d_out, int out_size, void* d_ws, size_t ws_size, hipStream_t stream) {
    const u16* x = (const u16*)d_in[0]; const u16* wq = (const u16*)d_in[1]; const u16* wk = (const u16*)d_in[2]; const u16* wv = (const u16*)d_in[3];
    const u16* wo = (const u16*)d_in[4]; const u16* wg = (const u16*)d_in[6]; const u16* wu = (const u16*)d_in[7]; const u16* wd = (const u16*)d_in[8];
    char* ws = (char*)d_ws;
    k1_prep<<<K1_BLOCKS, 256, 0, stream>>>(x, wq, wk, wv, wo, wg, wu, wd, ws);
    k2_main<<<256, 512, 0, stream>>>(x, wg, wu, wd, ws, (u16*)d_out);
}
```

```cpp
#include <hip/hip_runtime.h>
#include <hip/hip_bf16.h>
#include <stdint.h>

typedef unsigned short u16;
typedef short bf16x8 __attribute__((ext_vector_type(8)));
typedef short s16x4 __attribute__((ext_vector_type(4)));
typedef float f32x4 __attribute__((ext_vector_type(4)));
typedef float f32x16 __attribute__((ext_vector_type(16)));
typedef int i32x4 __attribute__((ext_vector_type(4)));
typedef unsigned u32x4 __attribute__((ext_vector_type(4)));
typedef unsigned u32x2 __attribute__((ext_vector_type(2)));
typedef __bf16 bf16x2_t __attribute__((ext_vector_type(2)));
typedef float f32x2 __attribute__((ext_vector_type(2)));
typedef short v4i16_t __attribute__((ext_vector_type(4)));
#define LAS __attribute__((address_space(3)))
#define GAS __attribute__((address_space(1)))
#define DI __device__ __forceinline__

constexpr int SEQ = 2048, DM = 576, NH = 32, DH = 18, FF = 4608;

constexpr int KVCH = 37888;
constexpr int KV_V = 16384, KV_K2 = 35840;
constexpr int WOIMG = 120 * 1024;
constexpr size_t WS_FLAGS = 0;
constexpr size_t WS_XN    = 16384;
constexpr size_t WS_WQX   = WS_XN + (size_t)SEQ * DM * 2;
constexpr size_t WS_WO    = WS_WQX + (size_t)DM * 1024 * 2;
constexpr size_t WS_KV    = WS_WO + (size_t)8 * WOIMG;
constexpr size_t WS_O     = WS_KV + 4 * KVCH;
constexpr size_t WS_PART  = WS_O + (size_t)256 * 128 * 80;
constexpr size_t WS_END   = WS_PART + (size_t)256 * 8 * 18 * 1024;
constexpr int FL_KV = 0, FL_KMAXA = 64, FL_KMAXB = 128, FL_TMO = 192, FL_P = 256;
constexpr unsigned O_EMPTY = 0xffffffffu;

DI float bflo(unsigned v) { return __uint_as_float(v << 16); }
DI float bfhi(unsigned v) { return __uint_as_float(v & 0xffff0000u); }
DI unsigned cvtpk(float lo, float hi) { return __builtin_bit_cast(unsigned, __builtin_convertvector((f32x2){lo, hi}, bf16x2_t)); }
DI float rbf(float f) { return bflo(cvtpk(f, f)); }
DI i32x4 make_srd(const void* p, unsigned bytes) {
    unsigned long long a = (unsigned long long)p; i32x4 s;
    s.x = __builtin_amdgcn_readfirstlane((unsigned)a); s.y = __builtin_amdgcn_readfirstlane((unsigned)(a >> 32) & 0xffffu);
    s.z = (int)bytes; s.w = 0x00020000; return s;
}
DI void dma16(i32x4 srd, unsigned voff, unsigned soff, unsigned lds_addr) {
    unsigned keep;
    asm volatile("s_mov_b32 %0, m0\n\ts_mov_b32 m0, %4\n\ts_nop 0\n\tbuffer_load_dwordx4 %1, %2, %3 offen lds\n\ts_mov_b32 m0, %0"
                 : "=&s"(keep) : "v"(voff), "s"(srd), "s"(soff), "s"(lds_addr) : "memory");
}
DI void dma16_sc1(i32x4 srd, unsigned voff, unsigned soff, unsigned lds_addr) {
    unsigned keep;
    asm volatile("s_mov_b32 %0, m0\n\ts_mov_b32 m0, %4\n\ts_nop 0\n\tbuffer_load_dwordx4 %1, %2, %3 offen sc1 lds\n\ts_mov_b32 m0, %0"
                 : "=&s"(keep) : "v"(voff), "s"(srd), "s"(soff), "s"(lds_addr) : "memory");
}
DI void dma4(i32x4 srd, unsigned voff, unsigned lds_addr) {
    unsigned keep;
    asm volatile("s_mov_b32 %0, m0\n\ts_mov_b32 m0, %3\n\ts_nop 0\n\tbuffer_load_dword %1, %2, 0 offen lds\n\ts_mov_b32 m0, %0"
                 : "=&s"(keep) : "v"(voff), "s"(srd), "s"(lds_addr) : "memory");
}
#define WAIT_BAR(N) asm volatile("s_waitcnt vmcnt(" #N ") lgkmcnt(0)\n\ts_barrier" ::: "memory")
#define LBAR() asm volatile("s_waitcnt lgkmcnt(0)\n\ts_barrier" ::: "memory")
#define SB() __builtin_amdgcn_sched_barrier(0)
#define FENCE() asm volatile("" ::: "memory")
#define MFMA16(a, b, c) __builtin_amdgcn_mfma_f32_16x16x32_bf16(a, b, c, 0, 0, 0)
#define MFMA32(a, b, c) __builtin_amdgcn_mfma_f32_32x32x16_bf16(a, b, c, 0, 0, 0)
#define MFMA32K8(a, b, c) __builtin_amdgcn_mfma_f32_32x32x8bf16_1k(a, b, c, 0, 0, 0)
#define EXP2(x) __builtin_amdgcn_exp2f(x)
DI s16x4 lds_tr(LAS const char* p) { return __builtin_bit_cast(s16x4, __builtin_amdgcn_ds_read_tr16_b64_v4i16((LAS v4i16_t*)p)); }
DI bf16x8 lds_b128(LAS const char* p) { return *(LAS const bf16x8*)p; }
DI bf16x8 lds_afrag(LAS const char* p) { const s16x4 lo = lds_tr(p), hi = lds_tr(p + 2048); return __builtin_shufflevector(lo, hi, 0, 1, 2, 3, 4, 5, 6, 7); }
#define RLX_AGENT __ATOMIC_RELAXED, __HIP_MEMORY_SCOPE_AGENT
DI int fresh_lane() { int t = __builtin_amdgcn_mbcnt_hi(~0u, __builtin_amdgcn_mbcnt_lo(~0u, 0u)); asm volatile("" : "+v"(t)); return t; }
DI void st_agent64(void* p, unsigned lo, unsigned hi) { __hip_atomic_store((GAS unsigned long long*)p, ((unsigned long long)hi << 32) | lo, RLX_AGENT); }
DI void wait_count(unsigned* word, unsigned want, unsigned* tmo) {
    unsigned spins = 0;
    while ((unsigned)__builtin_amdgcn_readfirstlane(__hip_atomic_load((GAS unsigned*)word, RLX_AGENT)) < want) {
        __builtin_amdgcn_s_sleep(1);
        if (++spins > (1u << 20)) { __hip_atomic_store((GAS unsigned*)tmo, 1u, RLX_AGENT); break; }
    }
    FENCE();
}
#define STAMP_DECL
#define STAMP(K, i)
#define STAMP_END(K)

constexpr int K1_BLOCKS = 945;
__global__ __launch_bounds__(256) void k1_prep(const u16* __restrict__ x, const u16* __restrict__ wq, const u16* __restrict__ wk, const u16* __restrict__ wv,
                                                const u16* __restrict__ wo, const u16* __restrict__ wg, const u16* __restrict__ wu, const u16* __restrict__ wd,
                                                char* __restrict__ ws) {
    const int b = blockIdx.x, tid = threadIdx.x, l = tid & 63;
    STAMP_DECL;
    STAMP(0, 0);
    if (b < 128) {
        const int tl = tid >> 4, tok = b * 16 + tl, sub = l & 15;
        const u32x4* src = (const u32x4*)(x + (size_t)tok * DM);
        u32x4 v[5];
#pragma unroll
        for (int i = 0; i < 4; ++i) v[i] = src[sub + 16 * i];
        v[4] = sub < 8 ? src[64 + sub] : (u32x4){0u, 0u, 0u, 0u};
        float s = 0.f;
#pragma unroll
        for (int i = 0; i < 5; ++i)
#pragma unroll
            for (int e = 0; e < 4; ++e) s += bflo(v[i][e]) + bfhi(v[i][e]);
#pragma unroll
        for (int o = 1; o < 16; o <<= 1) s += __shfl_xor(s, o);
        const float mean = s * (1.0f / DM);
        float q = 0.f;
#pragma unroll
        for (int i = 0; i < 5; ++i)
            if (i < 4 || sub < 8) {
#pragma unroll
                for (int e = 0; e < 4; ++e) { const float a = bflo(v[i][e]) - mean, bb = bfhi(v[i][e]) - mean; q += a * a + bb * bb; }
            }
#pragma unroll
        for (int o = 1; o < 16; o <<= 1) q += __shfl_xor(q, o);
        const float m = rbf(mean), var = rbf(q * (1.0f / DM));
        const float sd = rbf(sqrtf(rbf(var + 1e-5f)));
        const float rs = 1.0f / sd;
        char* dst = ws + WS_XN + ((size_t)(b >> 3) * 18 * 8 + (b & 7)) * 1024 + tl * 16;
#pragma unroll
        for (int i = 0; i < 5; ++i)
            if (i < 4 || sub < 8) {
                u32x4 o;
#pragma unroll
                for (int e = 0; e < 4; ++e) o[e] = cvtpk(rbf(bflo(v[i][e]) - m) * rs, rbf(bfhi(v[i][e]) - m) * rs);
                const int c = sub + 16 * i;
                *(u32x4*)(dst + (size_t)(c >> 2) * 8192 + (c & 3) * 256) = o;
            }
    } else if (b < 256) {
        __shared__ __attribute__((aligned(1024))) char sink[4096];
        const int g = b & 7, wv = ((b - 128) >> 3) * 4 + __builtin_amdgcn_readfirstlane(tid >> 6);
        const unsigned dst = (unsigned)(uintptr_t)sink + (wv & 3) * 1024;
        const i32x4 sg = make_srd(wg, DM * FF * 2), su = make_srd(wu, DM * FF * 2), sd = make_srd(wd, FF * DM * 2);
        for (int i = wv; i < 648; i += 64) {
            const int c = i * 64 + l, row = c / 72, off = (c - row * 72) * 16;
            dma16(sg, row * (FF * 2) + g * 1152 + off, 0, dst);
            dma16(su, row * (FF * 2) + g * 1152 + off, 0, dst);
            dma16(sd, g * 576 * (DM * 2) + c * 16, 0, dst);
        }
        asm volatile("s_waitcnt vmcnt(0)" ::: "memory");
    } else if (b < 544) {
        const int idx = (b - 256) * 256 + tid;
        const int k = idx >> 7, s = (idx >> 3) & 15, mat = (idx >> 2) & 1, t = idx & 3;
        const u16* wc = (s & 1) ? wv : wk;
        u16 o[8];
#pragma unroll
        for (int e = 0; e < 8; ++e) {
            const int rho = mat * 8 + e; const int head = 2 * s + (t >> 1);
            u16 val = 0;
            if ((t & 1) == 0) val = wq[(size_t)k * DM + head * DH + rho];
            else if (rho < 2) val = wq[(size_t)k * DM + head * DH + 16 + rho];
            else if (rho >= 4) { const int cc = (t == 1 ? 0 : 12) + rho - 4; if (cc < (t == 1 ? 12 : 18)) val = wc[k * DH + cc]; }
            o[e] = val;
        }
        u32x4 w; w[0] = o[0] | ((unsigned)o[1] << 16); w[1] = o[2] | ((unsigned)o[3] << 16); w[2] = o[4] | ((unsigned)o[5] << 16); w[3] = o[6] | ((unsigned)o[7] << 16);
        *(u32x4*)(ws + WS_WQX + (size_t)idx * 16) = w;
    } else if (b < 784) {
        const int idx = (b - 544) * 256 + tid, cgp = idx / 7680, rem = idx - cgp * 7680, p = rem >> 6, pos = rem & 63;
        const int m0 = cgp < 4 ? 5 * cgp : 20 + 4 * (cgp - 4);
        const int pp = p / 40, js = (p % 40) >> 1, R = p & 1, i = pos >> 2, ch = (pos & 3) ^ (2 * ((i >> 2) & 1));
        const int kp = 32 * js + 8 * (i >> 2) + (i & 3) + 4 * R, sp = kp / 40, d = kp - 40 * sp, hd = d >= 20 ? 1 : 0, dd = d - 20 * hd;
        const int row = 36 * sp + 18 * hd + (dd < 18 ? dd : 17), col = min(16 * (m0 + 2 * pp) + 8 * ch, DM - 8);
        *(u32x4*)(ws + WS_WO + (size_t)idx * 16) = *(const u32x4*)(wo + (size_t)row * DM + col);
    } else if (b < 944) {
        u32x4* p = (u32x4*)(ws + WS_O) + (b - 784) * 1024 + tid;
#pragma unroll
        for (int i = 0; i < 4; ++i) p[i * 256] = (u32x4){O_EMPTY, O_EMPTY, O_EMPTY, O_EMPTY};
    } else {
        const int c = tid >> 6, rest = tid & 63;
        *(u32x4*)(ws + WS_KV + c * KVCH + KV_V + rest * 304 + 18 * 16) = (u32x4){0x3f803f80u, 0x3f803f80u, 0x3f803f80u, 0x3f803f80u};
        u32x4* f = (u32x4*)(ws + WS_FLAGS);
#pragma unroll
        for (int i = 0; i < 4; ++i) f[i * 256 + tid] = (u32x4){0u, 0u, 0u, 0u};
    }
    STAMP(0, 1); STAMP_END(0);
}

constexpr int ST1 = 49152;
constexpr int LDS_D0 = 0, LDS_D1 = 49152, LDS_H = 86016, LDS_Q1 = 36864, LDS_Q2 = 45056;
constexpr int LDS_D2 = 126976;
constexpr int LDS_KV0 = 125952;
constexpr int LDS_ZERO = 122880;
constexpr int LDS_CTL = LDS_ZERO + 640;
constexpr int LDS_MX = LDS_CTL + 64;
constexpr int LDS_RED = 2 * KVCH + 9 * 1024;
constexpr int LDS_OT = LDS_RED + 8 * 11 * 256;
constexpr int LDS_BYTES = 163840;
DI unsigned wo_lds(int p) { return p < 37 ? LDS_KV0 + p * 1024 : p < 74 ? (p - 37) * 1024 : p < 111 ? KVCH + (p - 74) * 1024 : 2 * KVCH + (p - 111) * 1024; }

template <int NM, bool FIRST, int ND, int DPG, class Dma>
DI void gemm_step(f32x4 (&acc)[10][4], bf16x8 (&ca)[2], bf16x8 (&cb)[4], bf16x8 (&nb)[4], LAS const char* ap, LAS const char* bp, const Dma& dma) {
    bf16x8 a[10];
#pragma unroll
    for (int n = 0; n < 4; ++n) nb[n] = lds_b128(bp + n * 1024);
    a[0] = lds_afrag(ap); a[1] = lds_afrag(ap + 4096); a[2] = lds_afrag(ap + 2 * 4096);
    SB();
    int d = 0;
    if (!FIRST) {
#pragma unroll
        for (int k = 0; k < DPG; ++k) if (d < ND) dma(d++);
#pragma unroll
        for (int n = 0; n < 4; ++n) acc[NM - 2][n] = MFMA16(ca[0], cb[n], acc[NM - 2][n]);
        SB();
#pragma unroll
        for (int k = 0; k < DPG; ++k) if (d < ND) dma(d++);
#pragma unroll
        for (int n = 0; n < 4; ++n) acc[NM - 1][n] = MFMA16(ca[1], cb[n], acc[NM - 1][n]);
        SB();
    }
#pragma unroll
    for (int m = 0; m < NM - 2; ++m) {
        if (m + 3 < NM) a[m + 3] = lds_afrag(ap + (m + 3) * 4096);
#pragma unroll
        for (int k = 0; k < DPG; ++k) if (d < ND) dma(d++);
#pragma unroll
        for (int n = 0; n < 4; ++n) acc[m][n] = MFMA16(a[m], nb[n], acc[m][n]);
        SB();
    }
    ca[0] = a[NM - 2]; ca[1] = a[NM - 1];
}
template <int NM>
DI void gemm_flush(f32x4 (&acc)[10][4], const bf16x8 (&ca)[2], const bf16x8 (&cb)[4]) {
#pragma unroll
    for (int n = 0; n < 4; ++n) acc[NM - 2][n] = MFMA16(ca[0], cb[n], acc[NM - 2][n]);
#pragma unroll
    for (int n = 0; n < 4; ++n) acc[NM - 1][n] = MFMA16(ca[1], cb[n], acc[NM - 1][n]);
}

__global__ __launch_bounds__(512, 2) void k2_main(const u16* __restrict__ x, const u16* __restrict__ wg, const u16* __restrict__ wu,
                                                   const u16* __restrict__ wd, char* __restrict__ ws, u16* __restrict__ out) {
    __shared__ __attribute__((aligned(1024))) char lds[LDS_BYTES];
    const int tid = threadIdx.x, l = tid & 63;
    const int w = __builtin_amdgcn_readfirstlane(tid >> 6);
    const int b = blockIdx.x, s = 2 * (b & 7) + ((b >> 3) & 1), r = b >> 4;
    const int tg = w >> 2, fg = w & 3;
    const unsigned lds0 = (unsigned)(uintptr_t)lds;
    LAS char* L = (LAS char*)lds;
    unsigned* flags = (unsigned*)(ws + WS_FLAGS);

    const int dmat = w & 1, dR = (w >> 1) & 1, dcg0 = 5 * (w >> 2);
    const int di = l >> 2, dsig = l & 3;
    const int dmi = dsig ^ (2 * dmat + ((di >> 2) & 1)), dkrow = 8 * (di >> 2) + (di & 3);
    const i32x4 srd_w = make_srd(dmat ? wu : wg, DM * FF * 2);
    const i32x4 srd_q = make_srd(ws + WS_WQX, DM * 1024 * 2);
    const i32x4 srd_x = make_srd(ws + WS_XN, SEQ * DM * 2);
    const i32x4 srd_d = make_srd(wd, FF * DM * 2);
    const unsigned vo_w = dkrow * (FF * 2) + 16 * dmi, vo_q = dkrow * 2048 + 16 * dmi, vo_x = l * 16;
    const unsigned vo_d = dkrow * (DM * 2) + 32 * dmi;
    const unsigned dlds = lds0 + dcg0 * 4096 + dR * 2048 + dmat * 1024;
    auto piece1 = [&](int j, unsigned slot, int i) {
        const unsigned so = (32 * j + 4 * dR) * (FF * 2) + 288 * s * 2 + dcg0 * 64;
        if (i < 4) dma16(srd_w, vo_w, so + i * 64, dlds + slot + i * 4096);
        else if (i == 4) {
            if (w < 4) dma16(srd_w, vo_w, so + 4 * 64, dlds + slot + 4 * 4096);
            else dma16(srd_q, vo_q, (32 * j + 4 * dR) * 2048 + (64 * s + 32 * dmat) * 2, dlds + slot + 4 * 4096);
        } else dma16(srd_x, vo_x, ((r * 18 + j) * 8 + w) * 1024, lds0 + slot + 40960 + w * 1024);
    };
    auto piece2 = [&](int j, unsigned slot, int i) {
        const int ii = (w >= 4 && i == 4) ? 3 : i;
        dma16(srd_d, vo_d, (288 * s + 32 * j + 4 * dR) * (DM * 2) + 16 * dmat + (dcg0 + ii) * 128, dlds + slot + ii * 4096);
    };
    const int g4 = l >> 4, q4 = (l >> 2) & 3, p4 = l & 3, matp = p4 >> 1;
    const unsigned a_off = matp * 1024 + 16 * (4 * (4 * g4 + q4) + (fg ^ (2 * matp + (g4 & 1)))) + 8 * (p4 & 1);

    f32x4 acc[10][4];
#pragma unroll
    for (int m = 0; m < 10; ++m)
#pragma unroll
        for (int n = 0; n < 4; ++n) acc[m][n] = (f32x4){0.f, 0.f, 0.f, 0.f};
    bf16x8 ca[2], bA[4], bB[4];

    STAMP_DECL;
    STAMP(1, 0);
#pragma unroll
    for (int i = 0; i < 6; ++i) piece1(0, 0, i);
#pragma unroll
    for (int i = 0; i < 6; ++i) piece1(1, ST1, i);
    {
        const unsigned bo = 40960 + tg * 4096 + l * 16;
        WAIT_BAR(6);
        STAMP(1, 1);
        gemm_step<10, true, 6, 1>(acc, ca, bB, bA, L + a_off, L + bo, [&](int i) { piece1(2, 2 * ST1, i); });
        unsigned slot = ST1, slot2 = 0;
        for (int j = 1; j < 15; j += 2) {
            WAIT_BAR(6);
            gemm_step<10, false, 6, 1>(acc, ca, bA, bB, L + slot + a_off, L + slot + bo, [&](int i) { piece1(j + 2, slot2, i); });
            slot = (slot == 2 * ST1) ? 0 : slot + ST1; slot2 = (slot2 == 2 * ST1) ? 0 : slot2 + ST1;
            WAIT_BAR(6);
            gemm_step<10, false, 6, 1>(acc, ca, bB, bA, L + slot + a_off, L + slot + bo, [&](int i) { piece1(j + 3, slot2, i); });
            slot = (slot == 2 * ST1) ? 0 : slot + ST1; slot2 = (slot2 == 2 * ST1) ? 0 : slot2 + ST1;
        }
        WAIT_BAR(6);
        gemm_step<10, false, 6, 1>(acc, ca, bA, bB, L + a_off, L + bo, [&](int i) { piece1(17, 2 * ST1, i); });
        WAIT_BAR(6);
        gemm_step<10, false, 5, 1>(acc, ca, bB, bA, L + ST1 + a_off, L + ST1 + bo, [&](int i) { piece2(0, LDS_D0, i); });
        WAIT_BAR(5);
        gemm_step<10, false, 5, 2>(acc, ca, bA, bB, L + 2 * ST1 + a_off, L + 2 * ST1 + bo, [&](int i) { piece2(1, LDS_D1, i); });
        gemm_flush<10>(acc, ca, bB);
        SB(); FENCE();
    }
    if ((fg & 1) && tg == (s >> 3)) {
        const int l = fresh_lane(), tok = l & 15, g4 = l >> 4;
        const int e = s >> 1;
        f32x4 cv = acc[9][0];
        if ((e & 3) == 1) cv = acc[9][1]; else if ((e & 3) == 2) cv = acc[9][2]; else if ((e & 3) == 3) cv = acc[9][3];
        const int c0 = (fg == 1 ? 0 : 12) + 4 * (g4 - 1);
        const bool act = g4 >= 1 && c0 < 18;
        const unsigned p0 = cvtpk(cv[0], cv[1]), p1 = cvtpk(cv[2], cv[3]);
        const int kt = 4 * r + (e >> 1);
        char* kvb = ws + WS_KV + ((kt >> 3) & 3) * KVCH; const int lt = 8 * (kt >> 5) + (kt & 7);
        if ((s & 1) == 0) {
            const int key = 16 * (e & 1) + tok;
            if (act) {
                if (c0 < 16) st_agent64(kvb + lt * 1024 + ((c0 >> 3) * 32 + key) * 16 + (c0 & 4) * 2, p0, p1);
                else __hip_atomic_store((GAS unsigned*)(kvb + KV_K2 + lt * 128 + key * 4), p0, RLX_AGENT);
            }
            float n2 = 0.f;
            if (act) { n2 = bflo(p0) * bflo(p0) + bfhi(p0) * bfhi(p0); if (c0 < 16) n2 += bflo(p1) * bflo(p1) + bfhi(p1) * bfhi(p1); }
            n2 += __shfl_xor(n2, 16); n2 += __shfl_xor(n2, 32);
#pragma unroll
            for (int o = 1; o < 16; o <<= 1) n2 = fmaxf(n2, __shfl_xor(n2, o));
            if (l == 0) __hip_atomic_fetch_max((GAS unsigned*)(flags + (fg == 1 ? FL_KMAXA : FL_KMAXB)), __float_as_uint(n2), RLX_AGENT);
        } else {
            const int hv = (tok >> 2) & 1, jj = 4 * (tok >> 3) + (tok & 3);
            GAS u16* vb = (GAS u16*)(kvb + KV_V + lt * 1216 + (e & 1) * 608 + hv * 304 + jj * 2);
            if (act) {
                __hip_atomic_store(vb + (c0 + 0) * 8, (u16)(p0 & 0xffff), RLX_AGENT);
                __hip_atomic_store(vb + (c0 + 1) * 8, (u16)(p0 >> 16), RLX_AGENT);
                if (c0 < 16) { __hip_atomic_store(vb + (c0 + 2) * 8, (u16)(p1 & 0xffff), RLX_AGENT);
                               __hip_atomic_store(vb + (c0 + 3) * 8, (u16)(p1 >> 16), RLX_AGENT); }
            }
        }
    }
    LBAR();
    STAMP(1, 2);
    {
        const int l = fresh_lane(), tok = l & 15, g4 = l >> 4;
#pragma unroll
        for (int cg = 0; cg < 9; ++cg)
#pragma unroll
            for (int pr = 0; pr < 2; ++pr) {
                float h[4];
#pragma unroll
                for (int e = 0; e < 4; ++e) {
                    auto sw = __builtin_amdgcn_permlane32_swap(__float_as_uint(acc[cg][2 * pr][e]), __float_as_uint(acc[cg][2 * pr + 1][e]), false, false);
                    h[e] = fmaxf(__uint_as_float(sw[0]), 0.f) * __uint_as_float(sw[1]);
                }
                const int n = 4 * tg + 2 * pr + (l >> 5);
                u32x2 o; o[0] = cvtpk(h[0], h[1]); o[1] = cvtpk(h[2], h[3]);
                *(LAS u32x2*)(L + LDS_H + ((8 - cg) * 8 + n) * 1024 + (fg * 16 + tok) * 16 + 8 * (g4 & 1)) = o;
            }
        const float qs = 0.23570226039551584f * 1.4426950408889634f;
        const int hh = fg >> 1;
#pragma unroll
        for (int n = 0; n < 4; ++n) {
            const int ct = 2 * tg + (n >> 1), query = 16 * (n & 1) + tok;
            const unsigned p0 = cvtpk(acc[9][n][0] * qs, acc[9][n][1] * qs), p1 = cvtpk(acc[9][n][2] * qs, acc[9][n][3] * qs);
            if ((fg & 1) == 0) *(LAS u32x2*)(L + LDS_Q1 + (hh * 4 + ct) * 1024 + ((g4 >> 1) * 32 + query) * 16 + (g4 & 1) * 8) = (u32x2){p0, p1};
            else if (g4 == 0) *(LAS unsigned*)(L + LDS_Q2 + (hh * 4 + ct) * 128 + query * 4) = p0;
        }
    }
    WAIT_BAR(0);
    if (w == 0 && fresh_lane() == 0) __hip_atomic_fetch_add((GAS unsigned*)(flags + FL_KV), 1u, RLX_AGENT);
    STAMP(1, 3);
    unsigned kmaxa, kmaxb;
    const i32x4 srd_kv = make_srd(ws + WS_KV, 4 * KVCH);
    auto piece_kv = [&](int c, int i) {
        const int p = min(w + 8 * i, 36);
        dma16_sc1(srd_kv, vo_x, c * KVCH + p * 1024, lds0 + (c == 0 ? LDS_KV0 : (c - 1) * KVCH) + p * 1024);
    };
#pragma unroll
    for (int m = 0; m < 9; ++m)
#pragma unroll
        for (int n = 0; n < 4; ++n) acc[m][n] = (f32x4){0.f, 0.f, 0.f, 0.f};
    {
        LAS const char* hb = L + LDS_H + 8 * 8192 + 4 * tg * 1024 + l * 16;
        gemm_step<9, true, 0, 1>(acc, ca, bB, bA, L + LDS_D0 + a_off, hb, [](int) {});
        LBAR();
        gemm_step<9, false, 5, 2>(acc, ca, bA, bB, L + LDS_D1 + a_off, hb - 1 * 8192, [&](int i) { piece2(2, LDS_D0, i); });
        WAIT_BAR(0);
        gemm_step<9, false, 5, 2>(acc, ca, bB, bA, L + LDS_D0 + a_off, hb - 2 * 8192, [&](int i) { piece2(3, LDS_D1, i); });
        WAIT_BAR(0);
        gemm_step<9, false, 5, 2>(acc, ca, bA, bB, L + LDS_D1 + a_off, hb - 3 * 8192, [&](int i) { piece2(4, LDS_D0, i); });
        WAIT_BAR(0);
        gemm_step<9, false, 10, 2>(acc, ca, bB, bA, L + LDS_D0 + a_off, hb - 4 * 8192, [&](int i) { if (i < 5) piece2(5, LDS_D1, i); else piece2(6, LDS_D2, i - 5); });
        WAIT_BAR(5);
        gemm_step<9, false, 5, 1>(acc, ca, bA, bB, L + LDS_D1 + a_off, hb - 5 * 8192, [&](int i) { piece2(7, LDS_D0, i); });
        WAIT_BAR(5);
        gemm_step<9, false, 5, 1>(acc, ca, bB, bA, L + LDS_D2 + a_off, hb - 6 * 8192, [&](int i) { piece2(8, LDS_D1, i); });
        if (w == 0) wait_count(flags + FL_KV, 256u, flags + FL_TMO);
        WAIT_BAR(5);
        kmaxa = __hip_atomic_load((GAS unsigned*)(flags + FL_KMAXA), RLX_AGENT); kmaxb = __hip_atomic_load((GAS unsigned*)(flags + FL_KMAXB), RLX_AGENT);
        FENCE();
        gemm_step<9, false, 5, 1>(acc, ca, bA, bB, L + LDS_D0 + a_off, hb - 7 * 8192, [&](int i) { piece_kv(0, i); });
        WAIT_BAR(5);
        gemm_step<9, false, 0, 1>(acc, ca, bB, bA, L + LDS_D1 + a_off, hb - 8 * 8192, [](int) {});
        gemm_flush<9>(acc, ca, bA);
        SB(); FENCE();
    }
    STAMP(1, 4);
    const int la = fresh_lane();
#define l la
    const int hh = w >> 2, kh = (w >> 1) & 1, cp = w & 1;
#define h5 (l >> 5)
    bf16x8 q1[2]; u32x2 q2[2];
#pragma unroll
    for (int c = 0; c < 2; ++c) {
        q1[c] = lds_b128(L + LDS_Q1 + (hh * 4 + 2 * cp + c) * 1024 + l * 16);
        q2[c][0] = *(LAS const unsigned*)(L + LDS_Q2 + (hh * 4 + 2 * cp + c) * 128 + (l & 31) * 4);
    }
    u32x4 pk[18];
#pragma unroll
    for (int m = 0; m < 9; ++m)
#pragma unroll
        for (int pr = 0; pr < 2; ++pr) {
            u32x4 o; o[0] = cvtpk(acc[m][2 * pr][0], acc[m][2 * pr][1]); o[1] = cvtpk(acc[m][2 * pr][2], acc[m][2 * pr][3]);
            o[2] = cvtpk(acc[m][2 * pr + 1][0], acc[m][2 * pr + 1][1]); o[3] = cvtpk(acc[m][2 * pr + 1][2], acc[m][2 * pr + 1][3]);
            pk[m * 2 + pr] = o;
        }
    {
        const float kmax2 = __uint_as_float(kmaxa) + __uint_as_float(kmaxb);
#pragma unroll
        for (int c = 0; c < 2; ++c) {
            const u32x4 a = __builtin_bit_cast(u32x4, q1[c]); const unsigned t = h5 ? 0u : q2[c][0];
            float n2 = bflo(t) * bflo(t) + bfhi(t) * bfhi(t);
#pragma unroll
            for (int e = 0; e < 4; ++e) n2 += bflo(a[e]) * bflo(a[e]) + bfhi(a[e]) * bfhi(a[e]);
            n2 += __shfl_xor(n2, 32);
            const float cb = sqrtf(n2 * kmax2) * 1.001f + 0.01f;
            const float chi = rbf(cb), clo = cb - chi;
            q2[c][0] = t; q2[c][1] = h5 ? 0u : cvtpk(-chi, -clo);
        }
    }
    if (w == 0 && l < 40) *(LAS u32x4*)(L + LDS_ZERO + l * 16) = (u32x4){0u, 0u, 0u, 0u};
    if (w == 0 && l == 40) *(LAS unsigned*)(L + LDS_CTL) = 0u;
    WAIT_BAR(0);
    STAMP(1, 5);
#undef l
    const int lb = fresh_lane();
#define l lb
    const unsigned kone = h5 ? 0u : 0x3f803f80u;
    const bool vrow = (l & 31) < 19;
    const unsigned k1_base = 8 * kh * 1024 + l * 16, k2_base = KV_K2 + 8 * kh * 128 + (l & 31) * 4;
    const unsigned v_base = vrow ? KV_V + 8 * kh * 1216 + h5 * 304 + (l & 31) * 16 : LDS_ZERO, v_mul = vrow ? 1u : 0u;
    const int tgf = s >> 3, cgp = s & 7, nmt = cgp < 4 ? 5 : 4, m0 = cgp < 4 ? 5 * cgp : 20 + 4 * (cgp - 4);
    const int pr = w & 1, qk = w >> 1;
    const i32x4 srd_wo = make_srd(ws + WS_WO + (size_t)cgp * WOIMG, WOIMG);
    auto piece_wo = [&](int p) { dma16(srd_wo, vo_x, p * 1024, lds0 + wo_lds(p)); };
    const __amdgpu_buffer_rsrc_t rpart = __builtin_amdgcn_make_buffer_rsrc(ws + WS_PART + (size_t)r * 16 * 8 * 18 * 1024, 0, 16 * 8 * 18 * 1024, 0x00020000);
    float own[11];
    u32x4 pl[5][4];

    f32x16 o0, o1, sX, sY; bf16x8 kA, kB, v0A, v1A, v0B, v1B; unsigned k2A, k2B; u32x4 p0, p1; float e3[4];
    unsigned aK, aK2, aV;
#define SLOT(SC, SN, KN, K2N, CN, OP, V1P, OC, V0C) do { float ea[4], eb[4]; \
        SN = MFMA32(KN, q1[CN], (f32x16){}); SB(); \
        ea[0] = EXP2(SC[0]); ea[1] = EXP2(SC[1]); ea[2] = EXP2(SC[2]); ea[3] = EXP2(SC[3]); \
        p1[2] = cvtpk(e3[0], e3[1]); p1[3] = cvtpk(e3[2], e3[3]); SB(); \
        OP = MFMA32(V1P, __builtin_bit_cast(bf16x8, p1), OP); SB(); \
        eb[0] = EXP2(SC[4]); eb[1] = EXP2(SC[5]); eb[2] = EXP2(SC[6]); eb[3] = EXP2(SC[7]); \
        p0[0] = cvtpk(ea[0], ea[1]); p0[1] = cvtpk(ea[2], ea[3]); SB(); \
        SN = MFMA32K8(__builtin_bit_cast(s16x4, ((u32x2){K2N, kone})), __builtin_bit_cast(s16x4, q2[CN]), SN); SB(); \
        ea[0] = EXP2(SC[8]); ea[1] = EXP2(SC[9]); ea[2] = EXP2(SC[10]); ea[3] = EXP2(SC[11]); \
        p0[2] = cvtpk(eb[0], eb[1]); p0[3] = cvtpk(eb[2], eb[3]); SB(); \
        OC = MFMA32(V0C, __builtin_bit_cast(bf16x8, p0), OC); SB(); \
        e3[0] = EXP2(SC[12]); e3[1] = EXP2(SC[13]); e3[2] = EXP2(SC[14]); e3[3] = EXP2(SC[15]); \
        p1[0] = cvtpk(ea[0], ea[1]); p1[1] = cvtpk(ea[2], ea[3]); SB(); } while (0)
#define PAIR(DK, DK2, DV, H0, H1, H2, H3) do { \
        kB = lds_b128(L + aK + 1024); k2B = *(LAS const unsigned*)(L + aK2 + 128); v0B = lds_b128(L + aV + v_mul * 1216); SB(); \
        H0; SLOT(sX, sY, kA, k2A, 1, o1, v1B, o0, v0A); \
        v1B = lds_b128(L + aV + v_mul * 1216 + 608); SB(); \
        H1; SLOT(sY, sX, kB, k2B, 0, o0, v1A, o1, v0A); \
        aK += (DK); aK2 += (DK2); aV += v_mul * (DV); \
        kA = lds_b128(L + aK); k2A = *(LAS const unsigned*)(L + aK2); v0A = lds_b128(L + aV); SB(); \
        H2; SLOT(sX, sY, kB, k2B, 1, o1, v1A, o0, v0B); \
        v1A = lds_b128(L + aV + 608); SB(); \
        H3; SLOT(sY, sX, kA, k2A, 0, o0, v1B, o1, v0B); } while (0)
#define PAIR_IN(H0, H1, H2, H3) PAIR(2048, 256, 2432, H0, H1, H2, H3)
#define PAIR_JUMP(J, H0, H1, H2, H3) PAIR((J) - 6 * 1024, (J) - 6 * 128, (J) - 6 * 1216, H0, H1, H2, H3)
#define NOP_ ((void)0)
#define ATT_INIT() do { o0 = (f32x16){}; o1 = (f32x16){}; aK = k1_base + LDS_KV0; aK2 = k2_base + LDS_KV0; aV = v_base + v_mul * LDS_KV0; \
        kA = lds_b128(L + aK); k2A = *(LAS const unsigned*)(L + aK2); v0A = lds_b128(L + aV); v1A = lds_b128(L + aV + 608); v1B = v1A; \
        sX = MFMA32(kA, q1[0], (f32x16){}); sX = MFMA32K8(__builtin_bit_cast(s16x4, ((u32x2){k2A, kone})), __builtin_bit_cast(s16x4, q2[0]), sX); \
        p1 = (u32x4){0u, 0u, 0u, 0u}; e3[0] = e3[1] = e3[2] = e3[3] = 0.f; } while (0)
#define ATT_FIN() do { p1[2] = cvtpk(e3[0], e3[1]); p1[3] = cvtpk(e3[2], e3[3]); o1 = MFMA32(v1B, __builtin_bit_cast(bf16x8, p1), o1); } while (0)
    constexpr int J01 = 0 - LDS_KV0, J12 = KVCH;
    {
        const __amdgpu_buffer_rsrc_t rp = __builtin_amdgcn_make_buffer_rsrc(ws + WS_PART + (size_t)(r * 16 + s) * 8 * 18 * 1024, 0, 8 * 18 * 1024, 0x00020000);
#define KV(C, I) do { piece_kv(C, I); SB(); } while (0)
#define ST(I) do { __builtin_amdgcn_raw_buffer_store_b128(pk[I], rp, vo_x, (w * 18 + (I)) * 1024, 16); SB(); } while (0)
#define WO(P0, I) do { piece_wo(min((P0) + w + 8 * (I), (P0) + 36)); SB(); } while (0)
#define PL(T, SP) do { const int m_ = m0 + ((T) < nmt ? (T) : 0); \
            pl[T][SP] = __builtin_amdgcn_raw_buffer_load_b128(rpart, vo_x, ((((4 * qk + (SP)) * 8 + tgf * 4 + (m_ & 3)) * 9 + (m_ >> 2)) * 2 + pr) * 1024, 16); SB(); } while (0)
#define PL2(A) do { PL((A) >> 2, (A) & 3); PL(((A) + 1) >> 2, ((A) + 1) & 3); } while (0)
        ATT_INIT();
        PAIR_IN(KV(1, 0), KV(1, 1), KV(1, 2), KV(1, 3));
        PAIR_IN(KV(1, 4), ST(0), KV(2, 0), ST(1));
        PAIR_IN(KV(2, 1), ST(2), KV(2, 2), ST(3));
        WAIT_BAR(7);
        PAIR_JUMP(J01, KV(2, 3), ST(4), KV(2, 4), ST(5));
        PAIR_IN(KV(3, 0), ST(6), KV(3, 1), ST(7));
        PAIR_IN(KV(3, 2), ST(8), KV(3, 3), ST(9));
        PAIR_IN(KV(3, 4), ST(10), NOP_, ST(11));
        WAIT_BAR(12);
        PAIR_JUMP(J12, WO(0, 0), ST(12), WO(0, 1), ST(13));
        PAIR_IN(WO(0, 2), ST(14), WO(0, 3), ST(15));
        PAIR_IN(WO(0, 4), ST(16), NOP_, ST(17));
        PAIR_IN(NOP_, NOP_, NOP_, NOP_);
        WAIT_BAR(0);
        if (w == 0 && fresh_lane() == 0) __hip_atomic_fetch_add((GAS unsigned*)(flags + FL_P + r * 64), 1u, RLX_AGENT);
        PAIR_JUMP(J12, WO(37, 0), WO(37, 1), WO(37, 2), WO(37, 3));
        const unsigned pflag = __hip_atomic_load((GAS unsigned*)(flags + FL_P + r * 64), RLX_AGENT);
        PAIR_IN(WO(37, 4), NOP_, NOP_, NOP_);
        LBAR();
        if ((unsigned)__builtin_amdgcn_readfirstlane(pflag) < 16u) wait_count(flags + FL_P + r * 64, 16u, flags + FL_TMO);
#define WP(I, A) do { WO(74, I); PL((A) >> 2, (A) & 3); } while (0)
        PAIR_IN(WP(0, 0), WP(1, 1), WP(2, 2), WP(3, 3));
        PAIR_IN(WP(4, 4), PL2(5), PL2(7), PL2(9));
        PAIR(0, 0, 0, PL2(11), PL2(13), PL2(15), PL2(17));
        PL(4, 3);
        ATT_FIN();
    }
    STAMP(1, 6);
#undef l
    const int lc = fresh_lane();
#define l lc
    for (int attempt = 0;; ++attempt) {
        if (attempt == 1) {
            const unsigned kone = h5 ? 0u : 0x3f803f80u;
            const bool vrow = (l & 31) < 19;
            const unsigned k1_base = 8 * kh * 1024 + l * 16, k2_base = KV_K2 + 8 * kh * 128 + (l & 31) * 4;
            const unsigned v_base = vrow ? KV_V + 8 * kh * 1216 + h5 * 304 + (l & 31) * 16 : LDS_ZERO, v_mul = vrow ? 1u : 0u;
            WAIT_BAR(0);
#pragma unroll
            for (int c = 0; c < 4; ++c)
#pragma unroll
                for (int i = 0; i < 5; ++i) piece_kv(c, i);
            if (w == 0 && l < 40) *(LAS u32x4*)(L + LDS_ZERO + l * 16) = (u32x4){0u, 0u, 0u, 0u};
            WAIT_BAR(0);
            float m[2] = {-INFINITY, -INFINITY};
            for (int i = 0; i < 32; ++i) {
                const unsigned to = (i >> 3) == 0 ? LDS_KV0 : ((i >> 3) - 1) * KVCH;
                const bf16x8 k1 = lds_b128(L + k1_base + to + (i & 7) * 1024);
                const u32x2 k2 = {*(LAS const unsigned*)(L + k2_base + to + (i & 7) * 128), kone};
#pragma unroll
                for (int c = 0; c < 2; ++c) {
                    const u32x2 qz = {q2[c][0], 0u};
                    f32x16 sc = MFMA32(k1, q1[c], (f32x16){});
                    sc = MFMA32K8(__builtin_bit_cast(s16x4, k2), __builtin_bit_cast(s16x4, qz), sc);
#pragma unroll
                    for (int e = 0; e < 16; ++e) m[c] = fmaxf(m[c], sc[e]);
                }
            }
            LAS float* mx = (LAS float*)(L + LDS_MX);
#pragma unroll
            for (int c = 0; c < 2; ++c) { m[c] = fmaxf(m[c], __shfl_xor(m[c], 32)); if (l < 32) mx[(w * 2 + c) * 32 + l] = m[c]; }
            LBAR();
#pragma unroll
            for (int c = 0; c < 2; ++c) {
                const float mm = fmaxf(m[c], mx[((w ^ 2) * 2 + c) * 32 + (l & 31)]);
                const float chi = rbf(mm), clo = mm - chi;
                q2[c][1] = h5 ? 0u : cvtpk(-chi, -clo);
            }
            o0 = (f32x16){}; o1 = (f32x16){};
            for (int i = 0; i < 32; ++i) {
                const unsigned to = (i >> 3) == 0 ? LDS_KV0 : ((i >> 3) - 1) * KVCH;
                const bf16x8 k1 = lds_b128(L + k1_base + to + (i & 7) * 1024);
                const u32x2 k2 = {*(LAS const unsigned*)(L + k2_base + to + (i & 7) * 128), kone};
                const unsigned va = v_base + v_mul * (to + (i & 7) * 1216);
                const bf16x8 v0 = lds_b128(L + va), v1 = lds_b128(L + va + 608);
#pragma unroll
                for (int c = 0; c < 2; ++c) {
                    f32x16 sc = MFMA32(k1, q1[c], (f32x16){});
                    sc = MFMA32K8(__builtin_bit_cast(s16x4, k2), __builtin_bit_cast(s16x4, q2[c]), sc);
                    u32x4 pa, pb;
#pragma unroll
                    for (int e = 0; e < 4; ++e) { pa[e] = cvtpk(EXP2(sc[2 * e]), EXP2(sc[2 * e + 1])); pb[e] = cvtpk(EXP2(sc[8 + 2 * e]), EXP2(sc[9 + 2 * e])); }
                    if (c == 0) { o0 = MFMA32(v0, __builtin_bit_cast(bf16x8, pa), o0); o0 = MFMA32(v1, __builtin_bit_cast(bf16x8, pb), o0); }
                    else { o1 = MFMA32(v0, __builtin_bit_cast(bf16x8, pa), o1); o1 = MFMA32(v1, __builtin_bit_cast(bf16x8, pb), o1); }
                }
            }
        }
        LBAR();
        if (attempt == 0) {
            piece_wo(111 + w); if (w == 0) piece_wo(119);
        } else {
#pragma unroll
            for (int q = 0; q < 15; ++q) piece_wo(w * 15 + q);
        }
        LAS float* red = (LAS float*)(L + LDS_RED);
#pragma unroll
        for (int e = 0; e < 11; ++e) { red[(w * 11 + e) * 64 + l] = kh ? o0[e] : o1[e]; own[e] = kh ? o1[e] : o0[e]; }
        LBAR();
#pragma unroll
        for (int e = 0; e < 11; ++e) own[e] += red[((w ^ 2) * 11 + e) * 64 + l];
        const float lsum = __shfl(own[10], l & 31);
        if (!(lsum >= 8.67e-19f && lsum < 3.0e38f) && attempt == 0) *(LAS unsigned*)(L + LDS_CTL) = 1u;
        const float rl = 1.0f / lsum;
        LAS char* ot = L + LDS_OT + (32 * (2 * cp + kh) + (l & 31)) * 80 + 40 * hh;
        *(LAS u32x2*)(ot + 8 * h5) = (u32x2){cvtpk(own[0] * rl, own[1] * rl), cvtpk(own[2] * rl, own[3] * rl)};
        *(LAS u32x2*)(ot + 16 + 8 * h5) = (u32x2){cvtpk(own[4] * rl, own[5] * rl), cvtpk(own[6] * rl, own[7] * rl)};
        if (h5 == 0) *(LAS u32x2*)(ot + 32) = (u32x2){cvtpk(own[8] * rl, own[9] * rl), 0u};
        LBAR();
        const unsigned isbad = *(LAS const unsigned*)(L + LDS_CTL);
        if (!(isbad && attempt == 0)) break;
    }
    STAMP(1, 7);
    {
        const __amdgpu_buffer_rsrc_t rw = __builtin_amdgcn_make_buffer_rsrc(ws + WS_O + (size_t)(r * 16 + s) * 128 * 80, 0, 128 * 80, 0x00020000);
        __builtin_amdgcn_raw_buffer_store_b128(*(LAS const u32x4*)(L + LDS_OT + w * 1024 + l * 16), rw, l * 16, w * 1024, 16);
        if (w < 2) __builtin_amdgcn_raw_buffer_store_b128(*(LAS const u32x4*)(L + LDS_OT + (8 + w) * 1024 + l * 16), rw, l * 16, (8 + w) * 1024, 16);
    }
    {
        const int tok = l & 15, g4 = l >> 4, q4 = (l >> 2) & 3, p4 = l & 3;
        u32x2 xv[3];
#pragma unroll
        for (int u = 0; u < 3; ++u) {
            const int tt = min(qk + 4 * u, 2 * nmt - 1);
            xv[u] = *(const u32x2*)(x + (size_t)(128 * r + 64 * tgf + 32 * pr + 16 * (tt & 1) + tok) * DM + 16 * (m0 + (tt >> 1)) + 4 * g4);
        }
        SB();
        STAMP(1, 8);
        const __amdgpu_buffer_rsrc_t ro = __builtin_amdgcn_make_buffer_rsrc(ws + WS_O + (size_t)r * 16 * 128 * 80, 0, 16 * 128 * 80, 0x00020000);
        u32x4 bf[5][2];
        unsigned pend = 0x3ffu;
        for (unsigned spins = 0;; ++spins) {
#pragma unroll
            for (int i = 0; i < 5; ++i) {
                const int kp = 32 * (5 * qk + i) + 8 * g4, sp = kp / 40, d = kp - 40 * sp;
#pragma unroll
                for (int nn = 0; nn < 2; ++nn)
                    if (pend & (1u << (2 * i + nn))) bf[i][nn] = __builtin_amdgcn_raw_buffer_load_b128(ro, (sp * 128 + 64 * tgf + 32 * pr + 16 * nn + tok) * 80 + d * 2, 0, 16);
            }
#pragma unroll
            for (int i = 0; i < 5; ++i)
#pragma unroll
                for (int nn = 0; nn < 2; ++nn)
                    if ((pend & (1u << (2 * i + nn))) && __all(bf[i][nn][0] != O_EMPTY && bf[i][nn][1] != O_EMPTY && bf[i][nn][2] != O_EMPTY && bf[i][nn][3] != O_EMPTY)) pend &= ~(1u << (2 * i + nn));
            if (pend == 0u) break;
            if (spins > (1u << 17)) { __hip_atomic_store((GAS unsigned*)(flags + FL_TMO), 1u, RLX_AGENT); break; }
            __builtin_amdgcn_s_sleep(8);
        }
        f32x4 fa[5][2];
#pragma unroll
        for (int t = 0; t < 5; ++t) {
            fa[t][0] = (f32x4){0.f, 0.f, 0.f, 0.f}; fa[t][1] = (f32x4){0.f, 0.f, 0.f, 0.f};
#pragma unroll
            for (int sp = 0; sp < 4; ++sp) {
                const u32x4 p = pl[t][sp];
                fa[t][0][0] += bflo(p[0]); fa[t][0][1] += bfhi(p[0]); fa[t][0][2] += bflo(p[1]); fa[t][0][3] += bfhi(p[1]);
                fa[t][1][0] += bflo(p[2]); fa[t][1][1] += bfhi(p[2]); fa[t][1][2] += bflo(p[3]); fa[t][1][3] += bfhi(p[3]);
            }
        }
        WAIT_BAR(0);
        STAMP(1, 9);
        const unsigned ao = (4 * g4 + q4) * 64 + 8 * (p4 & 1);
#pragma unroll
        for (int t = 0; t < 5; ++t)
            if (t < nmt) {
#pragma unroll
                for (int i = 0; i < 5; ++i) {
                    const int p = (t >> 1) * 40 + (5 * qk + i) * 2;
                    const unsigned sw = ao + 16 * ((2 * (t & 1) + (p4 >> 1)) ^ (2 * (g4 & 1)));
                    const s16x4 lo = lds_tr(L + wo_lds(p) + sw), hi = lds_tr(L + wo_lds(p + 1) + sw);
                    const bf16x8 a = __builtin_shufflevector(lo, hi, 0, 1, 2, 3, 4, 5, 6, 7);
                    fa[t][0] = MFMA16(a, __builtin_bit_cast(bf16x8, bf[i][0]), fa[t][0]); fa[t][1] = MFMA16(a, __builtin_bit_cast(bf16x8, bf[i][1]), fa[t][1]);
                }
            }
        STAMP(1, 10);
        LBAR();
        LAS f32x4* ex = (LAS f32x4*)L;
#pragma unroll
        for (int t = 0; t < 5; ++t) { ex[(w * 10 + 2 * t) * 64 + l] = fa[t][0]; ex[(w * 10 + 2 * t + 1) * 64 + l] = fa[t][1]; }
        LBAR();
#pragma unroll
        for (int u = 0; u < 3; ++u) {
            const int tt = qk + 4 * u;
            if (tt < 2 * nmt) {
                f32x4 v = ex[((0 * 2 + pr) * 10 + tt) * 64 + l];
#pragma unroll
                for (int k = 1; k < 4; ++k) v += ex[((k * 2 + pr) * 10 + tt) * 64 + l];
                const size_t off = (size_t)(128 * r + 64 * tgf + 32 * pr + 16 * (tt & 1) + tok) * DM + 16 * (m0 + (tt >> 1)) + 4 * g4;
                *(u32x2*)(out + off) = (u32x2){cvtpk(v[0] + bflo(xv[u][0]), v[1] + bfhi(xv[u][0])), cvtpk(v[2] + bflo(xv[u][1]), v[3] + bfhi(xv[u][1]))};
            }
        }
    }
#undef l
#undef h5
    STAMP(1, 11); STAMP_END(1);
}

extern "C" void kernel_launch(void* const* d_in, const int* in_sizes, int n_in, void* d_out, int out_size, void* d_ws, size_t ws_size, hipStream_t stream) {
    const u16* x = (const u16*)d_in[0]; const u16* wq = (const u16*)d_in[1]; const u16* wk = (const u16*)d_in[2]; const u16* wv = (const u16*)d_in[3];
    const u16* wo = (const u16*)d_in[4]; const u16* wg = (const u16*)d_in[6]; const u16* wu = (const u16*)d_in[7]; const u16* wd = (const u16*)d_in[8];
    char* ws = (char*)d_ws;
    k1_prep<<<K1_BLOCKS, 256, 0, stream>>>(x, wq, wk, wv, wo, wg, wu, wd, ws);
    k2_main<<<256, 512, 0, stream>>>(x, wg, wu, wd, ws, (u16*)d_out);
}
```

```cpp
#include <hip/hip_runtime.h>
#include <hip/hip_bf16.h>
#include <stdint.h>

typedef unsigned short u16;
typedef short bf16x8 __attribute__((ext_vector_type(8)));
typedef short s16x4 __attribute__((ext_vector_type(4)));
typedef float f32x4 __attribute__((ext_vector_type(4)));
typedef float f32x16 __attribute__((ext_vector_type(16)));
typedef int i32x4 __attribute__((ext_vector_type(4)));
typedef unsigned u32x4 __attribute__((ext_vector_type(4)));
typedef unsigned u32x2 __attribute__((ext_vector_type(2)));
typedef __bf16 bf16x2_t __attribute__((ext_vector_type(2)));
typedef float f32x2 __attribute__((ext_vector_type(2)));
typedef short v4i16_t __attribute__((ext_vector_type(4)));
#define LAS __attribute__((address_space(3)))
#define GAS __attribute__((address_space(1)))
#define DI __device__ __forceinline__

constexpr int SEQ = 2048, DM = 576, NH = 32, DH = 18, FF = 4608;

constexpr int KVCH = 37888;
constexpr int KV_V = 16384, KV_K2 = 35840;
constexpr int WOIMG = 120 * 1024;
constexpr size_t WS_FLAGS = 0;
constexpr size_t WS_XN    = 16384;
constexpr size_t WS_WQX   = WS_XN + (size_t)SEQ * DM * 2;
constexpr size_t WS_WO    = WS_WQX + (size_t)DM * 1024 * 2;
constexpr size_t WS_KV    = WS_WO + (size_t)8 * WOIMG;
constexpr size_t WS_O     = WS_KV + 4 * KVCH;
constexpr size_t WS_PART  = WS_O + (size_t)256 * 128 * 80;
constexpr size_t WS_END   = WS_PART + (size_t)256 * 8 * 18 * 1024;
constexpr int FL_KV = 0, FL_KMAXA = 64, FL_KMAXB = 128, FL_TMO = 192, FL_P = 256;
constexpr unsigned O_EMPTY = 0xffffffffu;

DI float bflo(unsigned v) { return __uint_as_float(v << 16); }
DI float bfhi(unsigned v) { return __uint_as_float(v & 0xffff0000u); }
DI unsigned cvtpk(float lo, float hi) { return __builtin_bit_cast(unsigned, __builtin_convertvector((f32x2){lo, hi}, bf16x2_t)); }
DI float rbf(float f) { return bflo(cvtpk(f, f)); }
DI i32x4 make_srd(const void* p, unsigned bytes) {
    unsigned long long a = (unsigned long long)p; i32x4 s;
    s.x = __builtin_amdgcn_readfirstlane((unsigned)a); s.y = __builtin_amdgcn_readfirstlane((unsigned)(a >> 32) & 0xffffu);
    s.z = (int)bytes; s.w = 0x00020000; return s;
}
DI void dma16(i32x4 srd, unsigned voff, unsigned soff, unsigned lds_addr) {
    unsigned keep;
    asm volatile("s_mov_b32 %0, m0\n\ts_mov_b32 m0, %4\n\ts_nop 0\n\tbuffer_load_dwordx4 %1, %2, %3 offen lds\n\ts_mov_b32 m0, %0"
                 : "=&s"(keep) : "v"(voff), "s"(srd), "s"(soff), "s"(lds_addr) : "memory");
}
DI void dma16_sc1(i32x4 srd, unsigned voff, unsigned soff, unsigned lds_addr) {
    unsigned keep;
    asm volatile("s_mov_b32 %0, m0\n\ts_mov_b32 m0, %4\n\ts_nop 0\n\tbuffer_load_dwordx4 %1, %2, %3 offen sc1 lds\n\ts_mov_b32 m0, %0"
                 : "=&s"(keep) : "v"(voff), "s"(srd), "s"(soff), "s"(lds_addr) : "memory");
}
DI void dma4(i32x4 srd, unsigned voff, unsigned lds_addr) {
    unsigned keep;
    asm volatile("s_mov_b32 %0, m0\n\ts_mov_b32 m0, %3\n\ts_nop 0\n\tbuffer_load_dword %1, %2, 0 offen lds\n\ts_mov_b32 m0, %0"
                 : "=&s"(keep) : "v"(voff), "s"(srd), "s"(lds_addr) : "memory");
}
#define WAIT_BAR(N) asm volatile("s_waitcnt vmcnt(" #N ") lgkmcnt(0)\n\ts_barrier" ::: "memory")
#define LBAR() asm volatile("s_waitcnt lgkmcnt(0)\n\ts_barrier" ::: "memory")
#define SB() __builtin_amdgcn_sched_barrier(0)
#define FENCE() asm volatile("" ::: "memory")
#define MFMA16(a, b, c) __builtin_amdgcn_mfma_f32_16x16x32_bf16(a, b, c, 0, 0, 0)
#define MFMA32(a, b, c) __builtin_amdgcn_mfma_f32_32x32x16_bf16(a, b, c, 0, 0, 0)
#define MFMA32K8(a, b, c) __builtin_amdgcn_mfma_f32_32x32x8bf16_1k(a, b, c, 0, 0, 0)
#define EXP2(x) __builtin_amdgcn_exp2f(x)
DI s16x4 lds_tr(LAS const char* p) { return __builtin_bit_cast(s16x4, __builtin_amdgcn_ds_read_tr16_b64_v4i16((LAS v4i16_t*)p)); }
DI bf16x8 lds_b128(LAS const char* p) { return *(LAS const bf16x8*)p; }
DI bf16x8 lds_afrag(LAS const char* p) { const s16x4 lo = lds_tr(p), hi = lds_tr(p + 2048); return __builtin_shufflevector(lo, hi, 0, 1, 2, 3, 4, 5, 6, 7); }
#define RLX_AGENT __ATOMIC_RELAXED, __HIP_MEMORY_SCOPE_AGENT
DI int fresh_lane() { int t = __builtin_amdgcn_mbcnt_hi(~0u, __builtin_amdgcn_mbcnt_lo(~0u, 0u)); asm volatile("" : "+v"(t)); return t; }
DI void st_agent64(void* p, unsigned lo, unsigned hi) { __hip_atomic_store((GAS unsigned long long*)p, ((unsigned long long)hi << 32) | lo, RLX_AGENT); }
DI void wait_count(unsigned* word, unsigned want, unsigned* tmo) {
    unsigned spins = 0;
    while ((unsigned)__builtin_amdgcn_readfirstlane(__hip_atomic_load((GAS unsigned*)word, RLX_AGENT)) < want) {
        __builtin_amdgcn_s_sleep(1);
        if (++spins > (1u << 20)) { __hip_atomic_store((GAS unsigned*)tmo, 1u, RLX_AGENT); break; }
    }
    FENCE();
}
#define STAMP_DECL
#define STAMP(K, i)
#define STAMP_END(K)

constexpr int K1_BLOCKS = 945;
__global__ __launch_bounds__(256) void k1_prep(const u16* __restrict__ x, const u16* __restrict__ wq, const u16* __restrict__ wk, const u16* __restrict__ wv,
                                                const u16* __restrict__ wo, const u16* __restrict__ wg, const u16* __restrict__ wu, const u16* __restrict__ wd,
                                                char* __restrict__ ws) {
    const int b = blockIdx.x, tid = threadIdx.x, l = tid & 63;
    STAMP_DECL;
    STAMP(0, 0);
    if (b < 128) {
        const int tl = tid >> 4, tok = b * 16 + tl, sub = l & 15;
        const u32x4* src = (const u32x4*)(x + (size_t)tok * DM);
        u32x4 v[5];
#pragma unroll
        for (int i = 0; i < 4; ++i) v[i] = src[sub + 16 * i];
        v[4] = sub < 8 ? src[64 + sub] : (u32x4){0u, 0u, 0u, 0u};
        float s = 0.f;
#pragma unroll
        for (int i = 0; i < 5; ++i)
#pragma unroll
            for (int e = 0; e < 4; ++e) s += bflo(v[i][e]) + bfhi(v[i][e]);
#pragma unroll
        for (int o = 1; o < 16; o <<= 1) s += __shfl_xor(s, o);
        const float mean = s * (1.0f / DM);
        float q = 0.f;
#pragma unroll
        for (int i = 0; i < 5; ++i)
            if (i < 4 || sub < 8) {
#pragma unroll
                for (int e = 0; e < 4; ++e) { const float a = bflo(v[i][e]) - mean, bb = bfhi(v[i][e]) - mean; q += a * a + bb * bb; }
            }
#pragma unroll
        for (int o = 1; o < 16; o <<= 1) q += __shfl_xor(q, o);
        const float m = rbf(mean), var = rbf(q * (1.0f / DM));
        const float sd = rbf(sqrtf(rbf(var + 1e-5f)));
        const float rs = 1.0f / sd;
        char* dst = ws + WS_XN + ((size_t)(b >> 3) * 18 * 8 + (b & 7)) * 1024 + tl * 16;
#pragma unroll
        for (int i = 0; i < 5; ++i)
            if (i < 4 || sub < 8) {
                u32x4 o;
#pragma unroll
                for (int e = 0; e < 4; ++e) o[e] = cvtpk(rbf(bflo(v[i][e]) - m) * rs, rbf(bfhi(v[i][e]) - m) * rs);
                const int c = sub + 16 * i;
                *(u32x4*)(dst + (size_t)(c >> 2) * 8192 + (c & 3) * 256) = o;
            }
    } else if (b < 256) {
    } else if (b < 544) {
        const int idx = (b - 256) * 256 + tid;
        const int k = idx >> 7, s = (idx >> 3) & 15, mat = (idx >> 2) & 1, t = idx & 3;
        const u16* wc = (s & 1) ? wv : wk;
        u16 o[8];
#pragma unroll
        for (int e = 0; e < 8; ++e) {
            const int rho = mat * 8 + e; const int head = 2 * s + (t >> 1);
            u16 val = 0;
            if ((t & 1) == 0) val = wq[(size_t)k * DM + head * DH + rho];
            else if (rho < 2) val = wq[(size_t)k * DM + head * DH + 16 + rho];
            else if (rho >= 4) { const int cc = (t == 1 ? 0 : 12) + rho - 4; if (cc < (t == 1 ? 12 : 18)) val = wc[k * DH + cc]; }
            o[e] = val;
        }
        u32x4 w; w[0] = o[0] | ((unsigned)o[1] << 16); w[1] = o[2] | ((unsigned)o[3] << 16); w[2] = o[4] | ((unsigned)o[5] << 16); w[3] = o[6] | ((unsigned)o[7] << 16);
        *(u32x4*)(ws + WS_WQX + (size_t)idx * 16) = w;
    } else if (b < 784) {
        const int idx = (b - 544) * 256 + tid, cgp = idx / 7680, rem = idx - cgp * 7680, p = rem >> 6, pos = rem & 63;
        const int m0 = cgp < 4 ? 5 * cgp : 20 + 4 * (cgp - 4);
        const int pp = p / 40, js = (p % 40) >> 1, R = p & 1, i = pos >> 2, ch = (pos & 3) ^ (2 * ((i >> 2) & 1));
        const int kp = 32 * js + 8 * (i >> 2) + (i & 3) + 4 * R, sp = kp / 40, d = kp - 40 * sp, hd = d >= 20 ? 1 : 0, dd = d - 20 * hd;
        const int row = 36 * sp + 18 * hd + (dd < 18 ? dd : 17), col = min(16 * (m0 + 2 * pp) + 8 * ch, DM - 8);
        *(u32x4*)(ws + WS_WO + (size_t)idx * 16) = *(const u32x4*)(wo + (size_t)row * DM + col);
    } else if (b < 944) {
        u32x4* p = (u32x4*)(ws + WS_O) + (b - 784) * 1024 + tid;
#pragma unroll
        for (int i = 0; i < 4; ++i) p[i * 256] = (u32x4){O_EMPTY, O_EMPTY, O_EMPTY, O_EMPTY};
    } else {
        const int c = tid >> 6, rest = tid & 63;
        *(u32x4*)(ws + WS_KV + c * KVCH + KV_V + rest * 304 + 18 * 16) = (u32x4){0x3f803f80u, 0x3f803f80u, 0x3f803f80u, 0x3f803f80u};
        u32x4* f = (u32x4*)(ws + WS_FLAGS);
#pragma unroll
        for (int i = 0; i < 4; ++i) f[i * 256 + tid] = (u32x4){0u, 0u, 0u, 0u};
    }
    STAMP(0, 1); STAMP_END(0);
}

constexpr int ST1 = 49152;
constexpr int LDS_D0 = 0, LDS_D1 = 49152, LDS_H = 86016, LDS_Q1 = 36864, LDS_Q2 = 45056;
constexpr int LDS_D2 = 126976;
constexpr int LDS_KV0 = 125952;
constexpr int LDS_ZERO = 122880;
constexpr int LDS_CTL = LDS_ZERO + 640;
constexpr int LDS_MX = LDS_CTL + 64;
constexpr int LDS_RED = 2 * KVCH + 9 * 1024;
constexpr int LDS_OT = LDS_RED + 8 * 11 * 256;
constexpr int LDS_BYTES = 163840;
DI unsigned wo_lds(int p) { return p < 37 ? LDS_KV0 + p * 1024 : p < 74 ? (p - 37) * 1024 : p < 111 ? KVCH + (p - 74) * 1024 : 2 * KVCH + (p - 111) * 1024; }

template <int NM, bool FIRST, int ND, int DPG, class Dma>
DI void gemm_step(f32x4 (&acc)[10][4], bf16x8 (&ca)[2], bf16x8 (&cb)[4], bf16x8 (&nb)[4], LAS const char* ap, LAS const char* bp, const Dma& dma) {
    bf16x8 a[10];
#pragma unroll
    for (int n = 0; n < 4; ++n) nb[n] = lds_b128(bp + n * 1024);
    a[0] = lds_afrag(ap); a[1] = lds_afrag(ap + 4096); a[2] = lds_afrag(ap + 2 * 4096);
    SB();
    int d = 0;
    if (!FIRST) {
#pragma unroll
        for (int k = 0; k < DPG; ++k) if (d < ND) dma(d++);
#pragma unroll
        for (int n = 0; n < 4; ++n) acc[NM - 2][n] = MFMA16(ca[0], cb[n], acc[NM - 2][n]);
        SB();
#pragma unroll
        for (int k = 0; k < DPG; ++k) if (d < ND) dma(d++);
#pragma unroll
        for (int n = 0; n < 4; ++n) acc[NM - 1][n] = MFMA16(ca[1], cb[n], acc[NM - 1][n]);
        SB();
    }
#pragma unroll
    for (int m = 0; m < NM - 2; ++m) {
        if (m + 3 < NM) a[m + 3] = lds_afrag(ap + (m + 3) * 4096);
#pragma unroll
        for (int k = 0; k < DPG; ++k) if (d < ND) dma(d++);
#pragma unroll
        for (int n = 0; n < 4; ++n) acc[m][n] = MFMA16(a[m], nb[n], acc[m][n]);
        SB();
    }
    ca[0] = a[NM - 2]; ca[1] = a[NM - 1];
}
template <int NM>
DI void gemm_flush(f32x4 (&acc)[10][4], const bf16x8 (&ca)[2], const bf16x8 (&cb)[4]) {
#pragma unroll
    for (int n = 0; n < 4; ++n) acc[NM - 2][n] = MFMA16(ca[0], cb[n], acc[NM - 2][n]);
#pragma unroll
    for (int n = 0; n < 4; ++n) acc[NM - 1][n] = MFMA16(ca[1], cb[n], acc[NM - 1][n]);
}

__global__ __launch_bounds__(512, 2) void k2_main(const u16* __restrict__ x, const u16* __restrict__ wg, const u16* __restrict__ wu,
                                                   const u16* __restrict__ wd, char* __restrict__ ws, u16* __restrict__ out) {
    __shared__ __attribute__((aligned(1024))) char lds[LDS_BYTES];
    const int tid = threadIdx.x, l = tid & 63;
    const int w = __builtin_amdgcn_readfirstlane(tid >> 6);
    const int b = blockIdx.x, s = 2 * (b & 7) + ((b >> 3) & 1), r = b >> 4;
    const int tg = w >> 2, fg = w & 3;
    const unsigned lds0 = (unsigned)(uintptr_t)lds;
    LAS char* L = (LAS char*)lds;
    unsigned* flags = (unsigned*)(ws + WS_FLAGS);

    const int dmat = w & 1, dR = (w >> 1) & 1, dcg0 = 5 * (w >> 2);
    const int di = l >> 2, dsig = l & 3;
    const int dmi = dsig ^ (2 * dmat + ((di >> 2) & 1)), dkrow = 8 * (di >> 2) + (di & 3);
    const i32x4 srd_w = make_srd(dmat ? wu : wg, DM * FF * 2);
    const i32x4 srd_q = make_srd(ws + WS_WQX, DM * 1024 * 2);
    const i32x4 srd_x = make_srd(ws + WS_XN, SEQ * DM * 2);
    const i32x4 srd_d = make_srd(wd, FF * DM * 2);
    const unsigned vo_w = dkrow * (FF * 2) + 16 * dmi, vo_q = dkrow * 2048 + 16 * dmi, vo_x = l * 16;
    const unsigned vo_d = dkrow * (DM * 2) + 32 * dmi;
    const unsigned dlds = lds0 + dcg0 * 4096 + dR * 2048 + dmat * 1024;
    auto piece1 = [&](int j, unsigned slot, int i) {
        const unsigned so = (32 * j + 4 * dR) * (FF * 2) + 288 * s * 2 + dcg0 * 64;
        if (i < 4) dma16(srd_w, vo_w, so + i * 64, dlds + slot + i * 4096);
        else if (i == 4) {
            if (w < 4) dma16(srd_w, vo_w, so + 4 * 64, dlds + slot + 4 * 4096);
            else dma16(srd_q, vo_q, (32 * j + 4 * dR) * 2048 + (64 * s + 32 * dmat) * 2, dlds + slot + 4 * 4096);
        } else dma16(srd_x, vo_x, ((r * 18 + j) * 8 + w) * 1024, lds0 + slot + 40960 + w * 1024);
    };
    auto piece2 = [&](int j, unsigned slot, int i) {
        const int ii = (w >= 4 && i == 4) ? 3 : i;
        dma16(srd_d, vo_d, (288 * s + 32 * j + 4 * dR) * (DM * 2) + 16 * dmat + (dcg0 + ii) * 128, dlds + slot + ii * 4096);
    };
    const int g4 = l >> 4, q4 = (l >> 2) & 3, p4 = l & 3, matp = p4 >> 1;
    const unsigned a_off = matp * 1024 + 16 * (4 * (4 * g4 + q4) + (fg ^ (2 * matp + (g4 & 1)))) + 8 * (p4 & 1);

    f32x4 acc[10][4];
#pragma unroll
    for (int m = 0; m < 10; ++m)
#pragma unroll
        for (int n = 0; n < 4; ++n) acc[m][n] = (f32x4){0.f, 0.f, 0.f, 0.f};
    bf16x8 ca[2], bA[4], bB[4];

    STAMP_DECL;
    STAMP(1, 0);
#pragma unroll
    for (int i = 0; i < 6; ++i) piece1(0, 0, i);
#pragma unroll
    for (int i = 0; i < 6; ++i) piece1(1, ST1, i);
    {
        const unsigned bo = 40960 + tg * 4096 + l * 16;
        WAIT_BAR(6);
        STAMP(1, 1);
        gemm_step<10, true, 6, 1>(acc, ca, bB, bA, L + a_off, L + bo, [&](int i) { piece1(2, 2 * ST1, i); });
        unsigned slot = ST1, slot2 = 0;
        for (int j = 1; j < 15; j += 2) {
            WAIT_BAR(6);
            gemm_step<10, false, 6, 1>(acc, ca, bA, bB, L + slot + a_off, L + slot + bo, [&](int i) { piece1(j + 2, slot2, i); });
            slot = (slot == 2 * ST1) ? 0 : slot + ST1; slot2 = (slot2 == 2 * ST1) ? 0 : slot2 + ST1;
            WAIT_BAR(6);
            gemm_step<10, false, 6, 1>(acc, ca, bB, bA, L + slot + a_off, L + slot + bo, [&](int i) { piece1(j + 3, slot2, i); });
            slot = (slot == 2 * ST1) ? 0 : slot + ST1; slot2 = (slot2 == 2 * ST1) ? 0 : slot2 + ST1;
        }
        WAIT_BAR(6);
        gemm_step<10, false, 6, 1>(acc, ca, bA, bB, L + a_off, L + bo, [&](int i) { piece1(17, 2 * ST1, i); });
        WAIT_BAR(6);
        gemm_step<10, false, 5, 1>(acc, ca, bB, bA, L + ST1 + a_off, L + ST1 + bo, [&](int i) { piece2(0, LDS_D0, i); });
        WAIT_BAR(5);
        gemm_step<10, false, 5, 2>(acc, ca, bA, bB, L + 2 * ST1 + a_off, L + 2 * ST1 + bo, [&](int i) { piece2(1, LDS_D1, i); });
        gemm_flush<10>(acc, ca, bB);
        SB(); FENCE();
    }
    if ((fg & 1) && tg == (s >> 3)) {
        const int l = fresh_lane(), tok = l & 15, g4 = l >> 4;
        const int e = s >> 1;
        f32x4 cv = acc[9][0];
        if ((e & 3) == 1) cv = acc[9][1]; else if ((e & 3) == 2) cv = acc[9][2]; else if ((e & 3) == 3) cv = acc[9][3];
        const int c0 = (fg == 1 ? 0 : 12) + 4 * (g4 - 1);
        const bool act = g4 >= 1 && c0 < 18;
        const unsigned p0 = cvtpk(cv[0], cv[1]), p1 = cvtpk(cv[2], cv[3]);
        const int kt = 4 * r + (e >> 1);
        char* kvb = ws + WS_KV + ((kt >> 3) & 3) * KVCH; const int lt = 8 * (kt >> 5) + (kt & 7);
        if ((s & 1) == 0) {
            const int key = 16 * (e & 1) + tok;
            if (act) {
                if (c0 < 16) st_agent64(kvb + lt * 1024 + ((c0 >> 3) * 32 + key) * 16 + (c0 & 4) * 2, p0, p1);
                else __hip_atomic_store((GAS unsigned*)(kvb + KV_K2 + lt * 128 + key * 4), p0, RLX_AGENT);
            }
            float n2 = 0.f;
            if (act) { n2 = bflo(p0) * bflo(p0) + bfhi(p0) * bfhi(p0); if (c0 < 16) n2 += bflo(p1) * bflo(p1) + bfhi(p1) * bfhi(p1); }
            n2 += __shfl_xor(n2, 16); n2 += __shfl_xor(n2, 32);
#pragma unroll
            for (int o = 1; o < 16; o <<= 1) n2 = fmaxf(n2, __shfl_xor(n2, o));
            if (l == 0) __hip_atomic_fetch_max((GAS unsigned*)(flags + (fg == 1 ? FL_KMAXA : FL_KMAXB)), __float_as_uint(n2), RLX_AGENT);
        } else {
            const int hv = (tok >> 2) & 1, jj = 4 * (tok >> 3) + (tok & 3);
            GAS u16* vb = (GAS u16*)(kvb + KV_V + lt * 1216 + (e & 1) * 608 + hv * 304 + jj * 2);
            if (act) {
                __hip_atomic_store(vb + (c0 + 0) * 8, (u16)(p0 & 0xffff), RLX_AGENT);
                __hip_atomic_store(vb + (c0 + 1) * 8, (u16)(p0 >> 16), RLX_AGENT);
                if (c0 < 16) { __hip_atomic_store(vb + (c0 + 2) * 8, (u16)(p1 & 0xffff), RLX_AGENT);
                               __hip_atomic_store(vb + (c0 + 3) * 8, (u16)(p1 >> 16), RLX_AGENT); }
            }
        }
    }
    LBAR();
    STAMP(1, 2);
    {
        const int l = fresh_lane(), tok = l & 15, g4 = l >> 4;
#pragma unroll
        for (int cg = 0; cg < 9; ++cg)
#pragma unroll
            for (int pr = 0; pr < 2; ++pr) {
                float h[4];
#pragma unroll
                for (int e = 0; e < 4; ++e) {
                    auto sw = __builtin_amdgcn_permlane32_swap(__float_as_uint(acc[cg][2 * pr][e]), __float_as_uint(acc[cg][2 * pr + 1][e]), false, false);
                    h[e] = fmaxf(__uint_as_float(sw[0]), 0.f) * __uint_as_float(sw[1]);
                }
                const int n = 4 * tg + 2 * pr + (l >> 5);
                u32x2 o; o[0] = cvtpk(h[0], h[1]); o[1] = cvtpk(h[2], h[3]);
                *(LAS u32x2*)(L + LDS_H + ((8 - cg) * 8 + n) * 1024 + (fg * 16 + tok) * 16 + 8 * (g4 & 1)) = o;
            }
        const float qs = 0.23570226039551584f * 1.4426950408889634f;
        const int hh = fg >> 1;
#pragma unroll
        for (int n = 0; n < 4; ++n) {
            const int ct = 2 * tg + (n >> 1), query = 16 * (n & 1) + tok;
            const unsigned p0 = cvtpk(acc[9][n][0] * qs, acc[9][n][1] * qs), p1 = cvtpk(acc[9][n][2] * qs, acc[9][n][3] * qs);
            if ((fg & 1) == 0) *(LAS u32x2*)(L + LDS_Q1 + (hh * 4 + ct) * 1024 + ((g4 >> 1) * 32 + query) * 16 + (g4 & 1) * 8) = (u32x2){p0, p1};
            else if (g4 == 0) *(LAS unsigned*)(L + LDS_Q2 + (hh * 4 + ct) * 128 + query * 4) = p0;
        }
    }
    WAIT_BAR(0);
    if (w == 0 && fresh_lane() == 0) __hip_atomic_fetch_add((GAS unsigned*)(flags + FL_KV), 1u, RLX_AGENT);
    STAMP(1, 3);
    unsigned kmaxa, kmaxb;
    const i32x4 srd_kv = make_srd(ws + WS_KV, 4 * KVCH);
    auto piece_kv = [&](int c, int i) {
        const int p = min(w + 8 * i, 36);
        dma16_sc1(srd_kv, vo_x, c * KVCH + p * 1024, lds0 + (c == 0 ? LDS_KV0 : (c - 1) * KVCH) + p * 1024);
    };
#pragma unroll
    for (int m = 0; m < 9; ++m)
#pragma unroll
        for (int n = 0; n < 4; ++n) acc[m][n] = (f32x4){0.f, 0.f, 0.f, 0.f};
    {
        LAS const char* hb = L + LDS_H + 8 * 8192 + 4 * tg * 1024 + l * 16;
        gemm_step<9, true, 0, 1>(acc, ca, bB, bA, L + LDS_D0 + a_off, hb, [](int) {});
        LBAR();
        gemm_step<9, false, 5, 2>(acc, ca, bA, bB, L + LDS_D1 + a_off, hb - 1 * 8192, [&](int i) { piece2(2, LDS_D0, i); });
        WAIT_BAR(0);
        gemm_step<9, false, 5, 2>(acc, ca, bB, bA, L + LDS_D0 + a_off, hb - 2 * 8192, [&](int i) { piece2(3, LDS_D1, i); });
        WAIT_BAR(0);
        gemm_step<9, false, 5, 2>(acc, ca, bA, bB, L + LDS_D1 + a_off, hb - 3 * 8192, [&](int i) { piece2(4, LDS_D0, i); });
        WAIT_BAR(0);
        gemm_step<9, false, 10, 2>(acc, ca, bB, bA, L + LDS_D0 + a_off, hb - 4 * 8192, [&](int i) { if (i < 5) piece2(5, LDS_D1, i); else piece2(6, LDS_D2, i - 5); });
        WAIT_BAR(5);
        gemm_step<9, false, 5, 1>(acc, ca, bA, bB, L + LDS_D1 + a_off, hb - 5 * 8192, [&](int i) { piece2(7, LDS_D0, i); });
        WAIT_BAR(5);
        gemm_step<9, false, 5, 1>(acc, ca, bB, bA, L + LDS_D2 + a_off, hb - 6 * 8192, [&](int i) { piece2(8, LDS_D1, i); });
        if (w == 0) wait_count(flags + FL_KV, 256u, flags + FL_TMO);
        WAIT_BAR(5);
        kmaxa = __hip_atomic_load((GAS unsigned*)(flags + FL_KMAXA), RLX_AGENT); kmaxb = __hip_atomic_load((GAS unsigned*)(flags + FL_KMAXB), RLX_AGENT);
        FENCE();
        gemm_step<9, false, 5, 1>(acc, ca, bA, bB, L + LDS_D0 + a_off, hb - 7 * 8192, [&](int i) { piece_kv(0, i); });
        WAIT_BAR(5);
        gemm_step<9, false, 0, 1>(acc, ca, bB, bA, L + LDS_D1 + a_off, hb - 8 * 8192, [](int) {});
        gemm_flush<9>(acc, ca, bA);
        SB(); FENCE();
    }
    STAMP(1, 4);
    const int la = fresh_lane();
#define l la
    const int hh = w >> 2, kh = (w >> 1) & 1, cp = w & 1;
#define h5 (l >> 5)
    bf16x8 q1[2]; u32x2 q2[2];
#pragma unroll
    for (int c = 0; c < 2; ++c) {
        q1[c] = lds_b128(L + LDS_Q1 + (hh * 4 + 2 * cp + c) * 1024 + l * 16);
        q2[c][0] = *(LAS const unsigned*)(L + LDS_Q2 + (hh * 4 + 2 * cp + c) * 128 + (l & 31) * 4);
    }
    u32x4 pk[18];
#pragma unroll
    for (int m = 0; m < 9; ++m)
#pragma unroll
        for (int pr = 0; pr < 2; ++pr) {
            u32x4 o; o[0] = cvtpk(acc[m][2 * pr][0], acc[m][2 * pr][1]); o[1] = cvtpk(acc[m][2 * pr][2], acc[m][2 * pr][3]);
            o[2] = cvtpk(acc[m][2 * pr + 1][0], acc[m][2 * pr + 1][1]); o[3] = cvtpk(acc[m][2 * pr + 1][2], acc[m][2 * pr + 1][3]);
            pk[m * 2 + pr] = o;
        }
    {
        const float kmax2 = __uint_as_float(kmaxa) + __uint_as_float(kmaxb);
#pragma unroll
        for (int c = 0; c < 2; ++c) {
            const u32x4 a = __builtin_bit_cast(u32x4, q1[c]); const unsigned t = h5 ? 0u : q2[c][0];
            float n2 = bflo(t) * bflo(t) + bfhi(t) * bfhi(t);
#pragma unroll
            for (int e = 0; e < 4; ++e) n2 += bflo(a[e]) * bflo(a[e]) + bfhi(a[e]) * bfhi(a[e]);
            n2 += __shfl_xor(n2, 32);
            const float cb = sqrtf(n2 * kmax2) * 1.001f + 0.01f;
            const float chi = rbf(cb), clo = cb - chi;
            q2[c][0] = t; q2[c][1] = h5 ? 0u : cvtpk(-chi, -clo);
        }
    }
    if (w == 0 && l < 40) *(LAS u32x4*)(L + LDS_ZERO + l * 16) = (u32x4){0u, 0u, 0u, 0u};
    if (w == 0 && l == 40) *(LAS unsigned*)(L + LDS_CTL) = 0u;
    WAIT_BAR(0);
    STAMP(1, 5);
#undef l
    const int lb = fresh_lane();
#define l lb
    const unsigned kone = h5 ? 0u : 0x3f803f80u;
    const bool vrow = (l & 31) < 19;
    const unsigned k1_base = 8 * kh * 1024 + l * 16, k2_base = KV_K2 + 8 * kh * 128 + (l & 31) * 4;
    const unsigned v_base = vrow ? KV_V + 8 * kh * 1216 + h5 * 304 + (l & 31) * 16 : LDS_ZERO, v_mul = vrow ? 1u : 0u;
    const int tgf = s >> 3, cgp = s & 7, nmt = cgp < 4 ? 5 : 4, m0 = cgp < 4 ? 5 * cgp : 20 + 4 * (cgp - 4);
    const int pr = w & 1, qk = w >> 1;
    const i32x4 srd_wo = make_srd(ws + WS_WO + (size_t)cgp * WOIMG, WOIMG);
    auto piece_wo = [&](int p) { dma16(srd_wo, vo_x, p * 1024, lds0 + wo_lds(p)); };
    const __amdgpu_buffer_rsrc_t rpart = __builtin_amdgcn_make_buffer_rsrc(ws + WS_PART + (size_t)r * 16 * 8 * 18 * 1024, 0, 16 * 8 * 18 * 1024, 0x00020000);
    float own[11];
    u32x4 pl[5][4];

    f32x16 o0, o1, sX, sY; bf16x8 kA, kB, v0A, v1A, v0B, v1B; unsigned k2A, k2B; u32x4 p0, p1; float e3[4];
    unsigned aK, aK2, aV;
#define SLOT(SC, SN, KN, K2N, CN, OP, V1P, OC, V0C) do { float ea[4], eb[4]; \
        SN = MFMA32(KN, q1[CN], (f32x16){}); SB(); \
        ea[0] = EXP2(SC[0]); ea[1] = EXP2(SC[1]); ea[2] = EXP2(SC[2]); ea[3] = EXP2(SC[3]); \
        p1[2] = cvtpk(e3[0], e3[1]); p1[3] = cvtpk(e3[2], e3[3]); SB(); \
        OP = MFMA32(V1P, __builtin_bit_cast(bf16x8, p1), OP); SB(); \
        eb[0] = EXP2(SC[4]); eb[1] = EXP2(SC[5]); eb[2] = EXP2(SC[6]); eb[3] = EXP2(SC[7]); \
        p0[0] = cvtpk(ea[0], ea[1]); p0[1] = cvtpk(ea[2], ea[3]); SB(); \
        SN = MFMA32K8(__builtin_bit_cast(s16x4, ((u32x2){K2N, kone})), __builtin_bit_cast(s16x4, q2[CN]), SN); SB(); \
        ea[0] = EXP2(SC[8]); ea[1] = EXP2(SC[9]); ea[2] = EXP2(SC[10]); ea[3] = EXP2(SC[11]); \
        p0[2] = cvtpk(eb[0], eb[1]); p0[3] = cvtpk(eb[2], eb[3]); SB(); \
        OC = MFMA32(V0C, __builtin_bit_cast(bf16x8, p0), OC); SB(); \
        e3[0] = EXP2(SC[12]); e3[1] = EXP2(SC[13]); e3[2] = EXP2(SC[14]); e3[3] = EXP2(SC[15]); \
        p1[0] = cvtpk(ea[0], ea[1]); p1[1] = cvtpk(ea[2], ea[3]); SB(); } while (0)
#define PAIR(DK, DK2, DV, H0, H1, H2, H3) do { \
        kB = lds_b128(L + aK + 1024); k2B = *(LAS const unsigned*)(L + aK2 + 128); v0B = lds_b128(L + aV + v_mul * 1216); SB(); \
        H0; SLOT(sX, sY, kA, k2A, 1, o1, v1B, o0, v0A); \
        v1B = lds_b128(L + aV + v_mul * 1216 + 608); SB(); \
        H1; SLOT(sY, sX, kB, k2B, 0, o0, v1A, o1, v0A); \
        aK += (DK); aK2 += (DK2); aV += v_mul * (DV); \
        kA = lds_b128(L + aK); k2A = *(LAS const unsigned*)(L + aK2); v0A = lds_b128(L + aV); SB(); \
        H2; SLOT(sX, sY, kB, k2B, 1, o1, v1A, o0, v0B); \
        v1A = lds_b128(L + aV + 608); SB(); \
        H3; SLOT(sY, sX, kA, k2A, 0, o0, v1B, o1, v0B); } while (0)
#define PAIR_IN(H0, H1, H2, H3) PAIR(2048, 256, 2432, H0, H1, H2, H3)
#define PAIR_JUMP(J, H0, H1, H2, H3) PAIR((J) - 6 * 1024, (J) - 6 * 128, (J) - 6 * 1216, H0, H1, H2, H3)
#define NOP_ ((void)0)
#define ATT_INIT() do { o0 = (f32x16){}; o1 = (f32x16){}; aK = k1_base + LDS_KV0; aK2 = k2_base + LDS_KV0; aV = v_base + v_mul * LDS_KV0; \
        kA = lds_b128(L + aK); k2A = *(LAS const unsigned*)(L + aK2); v0A = lds_b128(L + aV); v1A = lds_b128(L + aV + 608); v1B = v1A; \
        sX = MFMA32(kA, q1[0], (f32x16){}); sX = MFMA32K8(__builtin_bit_cast(s16x4, ((u32x2){k2A, kone})), __builtin_bit_cast(s16x4, q2[0]), sX); \
        p1 = (u32x4){0u, 0u, 0u, 0u}; e3[0] = e3[1] = e3[2] = e3[3] = 0.f; } while (0)
#define ATT_FIN() do { p1[2] = cvtpk(e3[0], e3[1]); p1[3] = cvtpk(e3[2], e3[3]); o1 = MFMA32(v1B, __builtin_bit_cast(bf16x8, p1), o1); } while (0)
    constexpr int J01 = 0 - LDS_KV0, J12 = KVCH;
    {
        const __amdgpu_buffer_rsrc_t rp = __builtin_amdgcn_make_buffer_rsrc(ws + WS_PART + (size_t)(r * 16 + s) * 8 * 18 * 1024, 0, 8 * 18 * 1024, 0x00020000);
#define KV(C, I) do { piece_kv(C, I); SB(); } while (0)
#define ST(I) do { __builtin_amdgcn_raw_buffer_store_b128(pk[I], rp, vo_x, (w * 18 + (I)) * 1024, 16); SB(); } while (0)
#define WO(P0, I) do { piece_wo(min((P0) + w + 8 * (I), (P0) + 36)); SB(); } while (0)
#define PL(T, SP) do { const int m_ = m0 + ((T) < nmt ? (T) : 0); \
            pl[T][SP] = __builtin_amdgcn_raw_buffer_load_b128(rpart, vo_x, ((((4 * qk + (SP)) * 8 + tgf * 4 + (m_ & 3)) * 9 + (m_ >> 2)) * 2 + pr) * 1024, 16); SB(); } while (0)
#define PL2(A) do { PL((A) >> 2, (A) & 3); PL(((A) + 1) >> 2, ((A) + 1) & 3); } while (0)
        ATT_INIT();
        PAIR_IN(KV(1, 0), KV(1, 1), KV(1, 2), KV(1, 3));
        PAIR_IN(KV(1, 4), ST(0), KV(2, 0), ST(1));
        PAIR_IN(KV(2, 1), ST(2), KV(2, 2), ST(3));
        WAIT_BAR(7);
        PAIR_JUMP(J01, KV(2, 3), ST(4), KV(2, 4), ST(5));
        PAIR_IN(KV(3, 0), ST(6), KV(3, 1), ST(7));
        PAIR_IN(KV(3, 2), ST(8), KV(3, 3), ST(9));
        PAIR_IN(KV(3, 4), ST(10), NOP_, ST(11));
        WAIT_BAR(12);
        PAIR_JUMP(J12, WO(0, 0), ST(12), WO(0, 1), ST(13));
        PAIR_IN(WO(0, 2), ST(14), WO(0, 3), ST(15));
        PAIR_IN(WO(0, 4), ST(16), NOP_, ST(17));
        PAIR_IN(NOP_, NOP_, NOP_, NOP_);
        WAIT_BAR(0);
        if (w == 0 && fresh_lane() == 0) __hip_atomic_fetch_add((GAS unsigned*)(flags + FL_P + r * 64), 1u, RLX_AGENT);
        PAIR_JUMP(J12, WO(37, 0), WO(37, 1), WO(37, 2), WO(37, 3));
        const unsigned pflag = __hip_atomic_load((GAS unsigned*)(flags + FL_P + r * 64), RLX_AGENT);
        PAIR_IN(WO(37, 4), NOP_, NOP_, NOP_);
        LBAR();
        if ((unsigned)__builtin_amdgcn_readfirstlane(pflag) < 16u) wait_count(flags + FL_P + r * 64, 16u, flags + FL_TMO);
#define WP(I, A) do { WO(74, I); PL((A) >> 2, (A) & 3); } while (0)
        PAIR_IN(WP(0, 0), WP(1, 1), WP(2, 2), WP(3, 3));
        PAIR_IN(WP(4, 4), PL2(5), PL2(7), PL2(9));
        PAIR(0, 0, 0, PL2(11), PL2(13), PL2(15), PL2(17));
        PL(4, 3);
        ATT_FIN();
    }
    STAMP(1, 6);
#undef l
    const int lc = fresh_lane();
#define l lc
    for (int attempt = 0;; ++attempt) {
        if (attempt == 1) {
            const unsigned kone = h5 ? 0u : 0x3f803f80u;
            const bool vrow = (l & 31) < 19;
            const unsigned k1_base = 8 * kh * 1024 + l * 16, k2_base = KV_K2 + 8 * kh * 128 + (l & 31) * 4;
            const unsigned v_base = vrow ? KV_V + 8 * kh * 1216 + h5 * 304 + (l & 31) * 16 : LDS_ZERO, v_mul = vrow ? 1u : 0u;
            WAIT_BAR(0);
#pragma unroll
            for (int c = 0; c < 4; ++c)
#pragma unroll
                for (int i = 0; i < 5; ++i) piece_kv(c, i);
            if (w == 0 && l < 40) *(LAS u32x4*)(L + LDS_ZERO + l * 16) = (u32x4){0u, 0u, 0u, 0u};
            WAIT_BAR(0);
            float m[2] = {-INFINITY, -INFINITY};
            for (int i = 0; i < 32; ++i) {
                const unsigned to = (i >> 3) == 0 ? LDS_KV0 : ((i >> 3) - 1) * KVCH;
                const bf16x8 k1 = lds_b128(L + k1_base + to + (i & 7) * 1024);
                const u32x2 k2 = {*(LAS const unsigned*)(L + k2_base + to + (i & 7) * 128), kone};
#pragma unroll
                for (int c = 0; c < 2; ++c) {
                    const u32x2 qz = {q2[c][0], 0u};
                    f32x16 sc = MFMA32(k1, q1[c], (f32x16){});
                    sc = MFMA32K8(__builtin_bit_cast(s16x4, k2), __builtin_bit_cast(s16x4, qz), sc);
#pragma unroll
                    for (int e = 0; e < 16; ++e) m[c] = fmaxf(m[c], sc[e]);
                }
            }
            LAS float* mx = (LAS float*)(L + LDS_MX);
#pragma unroll
            for (int c = 0; c < 2; ++c) { m[c] = fmaxf(m[c], __shfl_xor(m[c], 32)); if (l < 32) mx[(w * 2 + c) * 32 + l] = m[c]; }
            LBAR();
#pragma unroll
            for (int c = 0; c < 2; ++c) {
                const float mm = fmaxf(m[c], mx[((w ^ 2) * 2 + c) * 32 + (l & 31)]);
                const float chi = rbf(mm), clo = mm - chi;
                q2[c][1] = h5 ? 0u : cvtpk(-chi, -clo);
            }
            o0 = (f32x16){}; o1 = (f32x16){};
            for (int i = 0; i < 32; ++i) {
                const unsigned to = (i >> 3) == 0 ? LDS_KV0 : ((i >> 3) - 1) * KVCH;
                const bf16x8 k1 = lds_b128(L + k1_base + to + (i & 7) * 1024);
                const u32x2 k2 = {*(LAS const unsigned*)(L + k2_base + to + (i & 7) * 128), kone};
                const unsigned va = v_base + v_mul * (to + (i & 7) * 1216);
                const bf16x8 v0 = lds_b128(L + va), v1 = lds_b128(L + va + 608);
#pragma unroll
                for (int c = 0; c < 2; ++c) {
                    f32x16 sc = MFMA32(k1, q1[c], (f32x16){});
                    sc = MFMA32K8(__builtin_bit_cast(s16x4, k2), __builtin_bit_cast(s16x4, q2[c]), sc);
                    u32x4 pa, pb;
#pragma unroll
                    for (int e = 0; e < 4; ++e) { pa[e] = cvtpk(EXP2(sc[2 * e]), EXP2(sc[2 * e + 1])); pb[e] = cvtpk(EXP2(sc[8 + 2 * e]), EXP2(sc[9 + 2 * e])); }
                    if (c == 0) { o0 = MFMA32(v0, __builtin_bit_cast(bf16x8, pa), o0); o0 = MFMA32(v1, __builtin_bit_cast(bf16x8, pb), o0); }
                    else { o1 = MFMA32(v0, __builtin_bit_cast(bf16x8, pa), o1); o1 = MFMA32(v1, __builtin_bit_cast(bf16x8, pb), o1); }
                }
            }
        }
        LBAR();
        if (attempt == 0) {
            piece_wo(111 + w); if (w == 0) piece_wo(119);
        } else {
#pragma unroll
            for (int q = 0; q < 15; ++q) piece_wo(w * 15 + q);
        }
        LAS float* red = (LAS float*)(L + LDS_RED);
#pragma unroll
        for (int e = 0; e < 11; ++e) { red[(w * 11 + e) * 64 + l] = kh ? o0[e] : o1[e]; own[e] = kh ? o1[e] : o0[e]; }
        LBAR();
#pragma unroll
        for (int e = 0; e < 11; ++e) own[e] += red[((w ^ 2) * 11 + e) * 64 + l];
        const float lsum = __shfl(own[10], l & 31);
        if (!(lsum >= 8.67e-19f && lsum < 3.0e38f) && attempt == 0) *(LAS unsigned*)(L + LDS_CTL) = 1u;
        const float rl = 1.0f / lsum;
        LAS char* ot = L + LDS_OT + (32 * (2 * cp + kh) + (l & 31)) * 80 + 40 * hh;
        *(LAS u32x2*)(ot + 8 * h5) = (u32x2){cvtpk(own[0] * rl, own[1] * rl), cvtpk(own[2] * rl, own[3] * rl)};
        *(LAS u32x2*)(ot + 16 + 8 * h5) = (u32x2){cvtpk(own[4] * rl, own[5] * rl), cvtpk(own[6] * rl, own[7] * rl)};
        if (h5 == 0) *(LAS u32x2*)(ot + 32) = (u32x2){cvtpk(own[8] * rl, own[9] * rl), 0u};
        LBAR();
        const unsigned isbad = *(LAS const unsigned*)(L + LDS_CTL);
        if (!(isbad && attempt == 0)) break;
    }
    STAMP(1, 7);
    {
        const __amdgpu_buffer_rsrc_t rw = __builtin_amdgcn_make_buffer_rsrc(ws + WS_O + (size_t)(r * 16 + s) * 128 * 80, 0, 128 * 80, 0x00020000);
        __builtin_amdgcn_raw_buffer_store_b128(*(LAS const u32x4*)(L + LDS_OT + w * 1024 + l * 16), rw, l * 16, w * 1024, 16);
        if (w < 2) __builtin_amdgcn_raw_buffer_store_b128(*(LAS const u32x4*)(L + LDS_OT + (8 + w) * 1024 + l * 16), rw, l * 16, (8 + w) * 1024, 16);
    }
    {
        const int tok = l & 15, g4 = l >> 4, q4 = (l >> 2) & 3, p4 = l & 3;
        u32x2 xv[3];
#pragma unroll
        for (int u = 0; u < 3; ++u) {
            const int tt = min(qk + 4 * u, 2 * nmt - 1);
            xv[u] = *(const u32x2*)(x + (size_t)(128 * r + 64 * tgf + 32 * pr + 16 * (tt & 1) + tok) * DM + 16 * (m0 + (tt >> 1)) + 4 * g4);
        }
        SB();
        STAMP(1, 8);
        const __amdgpu_buffer_rsrc_t ro = __builtin_amdgcn_make_buffer_rsrc(ws + WS_O + (size_t)r * 16 * 128 * 80, 0, 16 * 128 * 80, 0x00020000);
        u32x4 bf[5][2];
        unsigned pend = 0x3ffu;
        for (unsigned spins = 0;; ++spins) {
#pragma unroll
            for (int i = 0; i < 5; ++i) {
                const int kp = 32 * (5 * qk + i) + 8 * g4, sp = kp / 40, d = kp - 40 * sp;
#pragma unroll
                for (int nn = 0; nn < 2; ++nn)
                    if (pend & (1u << (2 * i + nn))) bf[i][nn] = __builtin_amdgcn_raw_buffer_load_b128(ro, (sp * 128 + 64 * tgf + 32 * pr + 16 * nn + tok) * 80 + d * 2, 0, 16);
            }
#pragma unroll
            for (int i = 0; i < 5; ++i)
#pragma unroll
                for (int nn = 0; nn < 2; ++nn)
                    if ((pend & (1u << (2 * i + nn))) && __all(bf[i][nn][0] != O_EMPTY && bf[i][nn][1] != O_EMPTY && bf[i][nn][2] != O_EMPTY && bf[i][nn][3] != O_EMPTY)) pend &= ~(1u << (2 * i + nn));
            if (pend == 0u) break;
            if (spins > (1u << 17)) { __hip_atomic_store((GAS unsigned*)(flags + FL_TMO), 1u, RLX_AGENT); break; }
            __builtin_amdgcn_s_sleep(8);
        }
        f32x4 fa[5][2];
#pragma unroll
        for (int t = 0; t < 5; ++t) {
            fa[t][0] = (f32x4){0.f, 0.f, 0.f, 0.f}; fa[t][1] = (f32x4){0.f, 0.f, 0.f, 0.f};
#pragma unroll
            for (int sp = 0; sp < 4; ++sp) {
                const u32x4 p = pl[t][sp];
                fa[t][0][0] += bflo(p[0]); fa[t][0][1] += bfhi(p[0]); fa[t][0][2] += bflo(p[1]); fa[t][0][3] += bfhi(p[1]);
                fa[t][1][0] += bflo(p[2]); fa[t][1][1] += bfhi(p[2]); fa[t][1][2] += bflo(p[3]); fa[t][1][3] += bfhi(p[3]);
            }
        }
        WAIT_BAR(0);
        STAMP(1, 9);
        const unsigned ao = (4 * g4 + q4) * 64 + 8 * (p4 & 1);
#pragma unroll
        for (int t = 0; t < 5; ++t)
            if (t < nmt) {
#pragma unroll
                for (int i = 0; i < 5; ++i) {
                    const int p = (t >> 1) * 40 + (5 * qk + i) * 2;
                    const unsigned sw = ao + 16 * ((2 * (t & 1) + (p4 >> 1)) ^ (2 * (g4 & 1)));
                    const s16x4 lo = lds_tr(L + wo_lds(p) + sw), hi = lds_tr(L + wo_lds(p + 1) + sw);
                    const bf16x8 a = __builtin_shufflevector(lo, hi, 0, 1, 2, 3, 4, 5, 6, 7);
                    fa[t][0] = MFMA16(a, __builtin_bit_cast(bf16x8, bf[i][0]), fa[t][0]); fa[t][1] = MFMA16(a, __builtin_bit_cast(bf16x8, bf[i][1]), fa[t][1]);
                }
            }
        STAMP(1, 10);
        LBAR();
        LAS f32x4* ex = (LAS f32x4*)L;
#pragma unroll
        for (int t = 0; t < 5; ++t) { ex[(w * 10 + 2 * t) * 64 + l] = fa[t][0]; ex[(w * 10 + 2 * t + 1) * 64 + l] = fa[t][1]; }
        LBAR();
#pragma unroll
        for (int u = 0; u < 3; ++u) {
            const int tt = qk + 4 * u;
            if (tt < 2 * nmt) {
                f32x4 v = ex[((0 * 2 + pr) * 10 + tt) * 64 + l];
#pragma unroll
                for (int k = 1; k < 4; ++k) v += ex[((k * 2 + pr) * 10 + tt) * 64 + l];
                const size_t off = (size_t)(128 * r + 64 * tgf + 32 * pr + 16 * (tt & 1) + tok) * DM + 16 * (m0 + (tt >> 1)) + 4 * g4;
                *(u32x2*)(out + off) = (u32x2){cvtpk(v[0] + bflo(xv[u][0]), v[1] + bfhi(xv[u][0])), cvtpk(v[2] + bflo(xv[u][1]), v[3] + bfhi(xv[u][1]))};
            }
        }
    }
#undef l
#undef h5
    STAMP(1, 11); STAMP_END(1);
}

extern "C" void kernel_launch(void* const* d_in, const int* in_sizes, int n_in, void* d_out, int out_size, void* d_ws, size_t ws_size, hipStream_t stream) {
    const u16* x = (const u16*)d_in[0]; const u16* wq = (const u16*)d_in[1]; const u16* wk = (const u16*)d_in[2]; const u16* wv = (const u16*)d_in[3];
    const u16* wo = (const u16*)d_in[4]; const u16* wg = (const u16*)d_in[6]; const u16* wu = (const u16*)d_in[7]; const u16* wd = (const u16*)d_in[8];
    char* ws = (char*)d_ws;
    k1_prep<<<K1_BLOCKS, 256, 0, stream>>>(x, wq, wk, wv, wo, wg, wu, wd, ws);
    k2_main<<<256, 512, 0, stream>>>(x, wg, wu, wd, ws, (u16*)d_out);
}
```

```cpp
#define EXPT 15
#include <hip/hip_runtime.h>
#include <hip/hip_bf16.h>
#include <stdint.h>

typedef unsigned short u16;
typedef short bf16x8 __attribute__((ext_vector_type(8)));
typedef short s16x4 __attribute__((ext_vector_type(4)));
typedef float f32x4 __attribute__((ext_vector_type(4)));
typedef float f32x16 __attribute__((ext_vector_type(16)));
typedef int i32x4 __attribute__((ext_vector_type(4)));
typedef unsigned u32x4 __attribute__((ext_vector_type(4)));
typedef unsigned u32x2 __attribute__((ext_vector_type(2)));
typedef __bf16 bf16x2_t __attribute__((ext_vector_type(2)));
typedef float f32x2 __attribute__((ext_vector_type(2)));
typedef short v4i16_t __attribute__((ext_vector_type(4)));
extern "C" __device__ unsigned long long amd_dispatch_id() __asm("llvm.amdgcn.dispatch.id");
#define LAS __attribute__((address_space(3)))
#define GAS __attribute__((address_space(1)))
#define DI __device__ __forceinline__

constexpr int SEQ = 2048, DM = 576, NH = 32, DH = 18, FF = 4608;

constexpr int KVCH = 37888;
constexpr int KV_V = 16384, KV_K2 = 35840;
constexpr int WOIMG = 120 * 1024;
constexpr size_t WS_FLAGS = 0;
constexpr size_t WS_XN    = 16384;
constexpr size_t WS_WQX   = WS_XN + (size_t)SEQ * DM * 2;
constexpr size_t WS_WO    = WS_WQX + (size_t)DM * 1024 * 2;
constexpr size_t WS_KV    = WS_WO + (size_t)8 * WOIMG;
constexpr size_t WS_O     = WS_KV + 4 * KVCH;
constexpr size_t WS_PART  = WS_O + (size_t)256 * 128 * 80;
constexpr size_t WS_END   = WS_PART + (size_t)256 * 8 * 18 * 1024;
constexpr int FL_PRO = 0, FL_P = 1024, FL_KV = 2048, FL_TMO = 8192;
constexpr unsigned O_EMPTY = 0xffffffffu;

DI float bflo(unsigned v) { return __uint_as_float(v << 16); }
DI float bfhi(unsigned v) { return __uint_as_float(v & 0xffff0000u); }
DI unsigned cvtpk(float lo, float hi) { return __builtin_bit_cast(unsigned, __builtin_convertvector((f32x2){lo, hi}, bf16x2_t)); }
DI float rbf(float f) { return bflo(cvtpk(f, f)); }
DI i32x4 make_srd(const void* p, unsigned bytes) {
    unsigned long long a = (unsigned long long)p; i32x4 s;
    s.x = __builtin_amdgcn_readfirstlane((unsigned)a); s.y = __builtin_amdgcn_readfirstlane((unsigned)(a >> 32) & 0xffffu);
    s.z = (int)bytes; s.w = 0x00020000; return s;
}
DI void dma16(i32x4 srd, unsigned voff, unsigned soff, unsigned lds_addr) {
    unsigned keep;
    asm volatile("s_mov_b32 %0, m0\n\ts_mov_b32 m0, %4\n\ts_nop 0\n\tbuffer_load_dwordx4 %1, %2, %3 offen lds\n\ts_mov_b32 m0, %0"
                 : "=&s"(keep) : "v"(voff), "s"(srd), "s"(soff), "s"(lds_addr) : "memory");
}
DI void dma16_sc1(i32x4 srd, unsigned voff, unsigned soff, unsigned lds_addr) {
    unsigned keep;
    asm volatile("s_mov_b32 %0, m0\n\ts_mov_b32 m0, %4\n\ts_nop 0\n\tbuffer_load_dwordx4 %1, %2, %3 offen sc1 lds\n\ts_mov_b32 m0, %0"
                 : "=&s"(keep) : "v"(voff), "s"(srd), "s"(soff), "s"(lds_addr) : "memory");
}
DI void dma4(i32x4 srd, unsigned voff, unsigned lds_addr) {
    unsigned keep;
    asm volatile("s_mov_b32 %0, m0\n\ts_mov_b32 m0, %3\n\ts_nop 0\n\tbuffer_load_dword %1, %2, 0 offen lds\n\ts_mov_b32 m0, %0"
                 : "=&s"(keep) : "v"(voff), "s"(srd), "s"(lds_addr) : "memory");
}
#define WAIT_BAR(N) asm volatile("s_waitcnt vmcnt(" #N ") lgkmcnt(0)\n\ts_barrier" ::: "memory")
#define LBAR() asm volatile("s_waitcnt lgkmcnt(0)\n\ts_barrier" ::: "memory")
#define SB() __builtin_amdgcn_sched_barrier(0)
#define FENCE() asm volatile("" ::: "memory")
#define PIN4(a) asm volatile("" : "+v"((a)[0]), "+v"((a)[1]), "+v"((a)[2]), "+v"((a)[3]))
#define MFMA16(a, b, c) __builtin_amdgcn_mfma_f32_16x16x32_bf16(a, b, c, 0, 0, 0)
#define MFMA32(a, b, c) __builtin_amdgcn_mfma_f32_32x32x16_bf16(a, b, c, 0, 0, 0)
#define MFMA32K8(a, b, c) __builtin_amdgcn_mfma_f32_32x32x8bf16_1k(a, b, c, 0, 0, 0)
#define EXP2(x) __builtin_amdgcn_exp2f(x)
DI s16x4 lds_tr(LAS const char* p) { return __builtin_bit_cast(s16x4, __builtin_amdgcn_ds_read_tr16_b64_v4i16((LAS v4i16_t*)p)); }
DI bf16x8 lds_b128(LAS const char* p) { return *(LAS const bf16x8*)p; }
DI bf16x8 lds_afrag(LAS const char* p) { const s16x4 lo = lds_tr(p), hi = lds_tr(p + 2048); return __builtin_shufflevector(lo, hi, 0, 1, 2, 3, 4, 5, 6, 7); }
#define RLX_AGENT __ATOMIC_RELAXED, __HIP_MEMORY_SCOPE_AGENT
template <int X> DI float xor_lane(float v) { return __int_as_float(__builtin_amdgcn_ds_swizzle(__float_as_int(v), (X << 10) | 0x1f)); }
DI float half_sum(float v) { auto r = __builtin_amdgcn_permlane32_swap(__float_as_uint(v), __float_as_uint(v), false, false); return __uint_as_float(r[0]) + __uint_as_float(r[1]); }
DI float half_max(float v) { auto r = __builtin_amdgcn_permlane32_swap(__float_as_uint(v), __float_as_uint(v), false, false); return fmaxf(__uint_as_float(r[0]), __uint_as_float(r[1])); }
DI float half_lo(float v) { auto r = __builtin_amdgcn_permlane32_swap(__float_as_uint(v), __float_as_uint(v), false, false); return __uint_as_float(r[0]); }
template <int CTRL, int ROWS> DI float dpp_add(float v) { return v + __int_as_float(__builtin_amdgcn_update_dpp(0, __float_as_int(v), CTRL, ROWS, 0xf, true)); }
DI float wave_sum_dpp(float v) {
    v = dpp_add<0x111, 0xf>(v); v = dpp_add<0x112, 0xf>(v); v = dpp_add<0x114, 0xf>(v); v = dpp_add<0x118, 0xf>(v);
    v = dpp_add<0x142, 0xa>(v); v = dpp_add<0x143, 0xc>(v);
    return __int_as_float(__builtin_amdgcn_readlane(__float_as_int(v), 63));
}
DI float wave_sum(float v) { v += xor_lane<1>(v); v += xor_lane<2>(v); v += xor_lane<4>(v); v += xor_lane<8>(v); v += xor_lane<16>(v); return half_sum(v); }
DI float wave_max(float v) { v = fmaxf(v, xor_lane<1>(v)); v = fmaxf(v, xor_lane<2>(v)); v = fmaxf(v, xor_lane<4>(v)); v = fmaxf(v, xor_lane<8>(v)); v = fmaxf(v, xor_lane<16>(v)); return half_max(v); }
DI int fresh_lane() { int t; asm volatile("v_mbcnt_lo_u32_b32 %0, -1, 0\n\tv_mbcnt_hi_u32_b32 %0, -1, %0" : "=v"(t)); return t; }
DI void st_agent64(void* p, unsigned lo, unsigned hi) { __hip_atomic_store((GAS unsigned long long*)p, ((unsigned long long)hi << 32) | lo, RLX_AGENT); }
DI void wait_tags(const char* flags, unsigned off, bool use, unsigned tag) {
    for (unsigned spins = 0;; ++spins) {
        const unsigned v = use ? __hip_atomic_load((GAS unsigned*)(flags + off), RLX_AGENT) : tag;
        if (__all(v == tag)) break;
        if (spins > (1u << 19)) { __hip_atomic_store((GAS unsigned*)(flags + FL_TMO), tag, RLX_AGENT); break; }
        __builtin_amdgcn_s_sleep(1);
    }
    FENCE();
}
#define STAMP_DECL unsigned long long t0_ = 0, t1_ = 0
#define STAMP(K, i) do { if ((i) == 0) t0_ = __builtin_amdgcn_s_memrealtime(); if ((i) == EXPT) t1_ = __builtin_amdgcn_s_memrealtime(); } while (0)
#define STAMP_END(K) do { const unsigned long long n_ = __builtin_amdgcn_s_memrealtime(), d_ = (t1_ - t0_) * 3; while (__builtin_amdgcn_s_memrealtime() - n_ < d_) __builtin_amdgcn_s_sleep(2); } while (0)

#define DMA345 dma16_sc1
constexpr int ST1 = 49152;
constexpr int LDS_D0 = 0, LDS_D1 = 49152, LDS_H = 86016, LDS_Q1 = 36864, LDS_Q2 = 45056;
constexpr int LDS_PF = 147456;
constexpr int LDS_KM = 46080;
constexpr int LDS_D2 = 126976;
constexpr int LDS_KV0 = 125952;
constexpr int LDS_ZERO = 122880;
constexpr int LDS_CTL = LDS_ZERO + 640;
constexpr int LDS_MX = LDS_CTL + 64;
constexpr int LDS_RED = 2 * KVCH + 9 * 1024;
constexpr int LDS_OT = LDS_RED + 8 * 11 * 256;
constexpr int LDS_BYTES = 163840;
DI unsigned wo_lds(int p) { return p < 37 ? LDS_KV0 + p * 1024 : p < 74 ? (p - 37) * 1024 : p < 111 ? KVCH + (p - 74) * 1024 : 2 * KVCH + (p - 111) * 1024; }

template <int NM, bool FIRST, int ND, int DPG, int AD = 3, class Dma>
DI void gemm_step(f32x4 (&acc)[10][4], bf16x8 (&ca)[2], bf16x8 (&cb)[4], bf16x8 (&nb)[4], LAS const char* ap, LAS const char* bp, const Dma& dma) {
    bf16x8 a[10];
#pragma unroll
    for (int n = 0; n < 4; ++n) nb[n] = lds_b128(bp + n * 1024);
    a[0] = lds_afrag(ap); a[1] = lds_afrag(ap + 4096); if (AD > 2) a[2] = lds_afrag(ap + 2 * 4096);
    SB();
    int d = 0;
    if (!FIRST) {
#pragma unroll
        for (int k = 0; k < DPG; ++k) if (d < ND) dma(d++);
#pragma unroll
        for (int n = 0; n < 4; ++n) acc[NM - 2][n] = MFMA16(ca[0], cb[n], acc[NM - 2][n]);
        PIN4(acc[NM - 2]); SB();
#pragma unroll
        for (int k = 0; k < DPG; ++k) if (d < ND) dma(d++);
#pragma unroll
        for (int n = 0; n < 4; ++n) acc[NM - 1][n] = MFMA16(ca[1], cb[n], acc[NM - 1][n]);
        PIN4(acc[NM - 1]); SB();
    }
#pragma unroll
    for (int m = 0; m < NM - 2; ++m) {
        if (m + AD < NM) a[m + AD] = lds_afrag(ap + (m + AD) * 4096);
#pragma unroll
        for (int k = 0; k < DPG; ++k) if (d < ND) dma(d++);
#pragma unroll
        for (int n = 0; n < 4; ++n) acc[m][n] = MFMA16(a[m], nb[n], acc[m][n]);
        PIN4(acc[m]); SB();
    }
    ca[0] = a[NM - 2]; ca[1] = a[NM - 1];
}
template <int NM>
DI void gemm_flush(f32x4 (&acc)[10][4], const bf16x8 (&ca)[2], const bf16x8 (&cb)[4]) {
#pragma unroll
    for (int n = 0; n < 4; ++n) acc[NM - 2][n] = MFMA16(ca[0], cb[n], acc[NM - 2][n]);
#pragma unroll
    for (int n = 0; n < 4; ++n) acc[NM - 1][n] = MFMA16(ca[1], cb[n], acc[NM - 1][n]);
}

__global__ __launch_bounds__(512, 2) void fused_block(const u16* __restrict__ x, const u16* __restrict__ wq, const u16* __restrict__ wk, const u16* __restrict__ wv,
                                                       const u16* __restrict__ wo, const u16* __restrict__ wg, const u16* __restrict__ wu, const u16* __restrict__ wd,
                                                       char* __restrict__ ws, u16* __restrict__ out) {
    __shared__ __attribute__((aligned(1024))) char lds[LDS_BYTES];
    const int tid = threadIdx.x, l = tid & 63;
    const int w = __builtin_amdgcn_readfirstlane(tid >> 6);
    const int b = blockIdx.x, s = 2 * (b & 7) + ((b >> 3) & 1), r = b >> 4, wgi = r * 16 + s;
    const int tg = w >> 2, fg = w & 3;
    constexpr unsigned lds0 = 0;
    LAS char* L = (LAS char*)lds;
    char* flags = ws + WS_FLAGS;
    const unsigned tag = (((unsigned)amd_dispatch_id() + 1u) * 0x9E3779B1u) ^ ((unsigned)((unsigned long long)__builtin_amdgcn_dispatch_ptr() >> 6) * 0x85EBCA77u) ^ 0x5bd1e995u;

    const int dmat = w & 1, dR = (w >> 1) & 1, dcg0 = 5 * (w >> 2);
    const int di = l >> 2, dsig = l & 3;
    const int dmi = dsig ^ (2 * dmat + ((di >> 2) & 1)), dkrow = 8 * (di >> 2) + (di & 3);
    const i32x4 srd_w = make_srd(dmat ? wu : wg, DM * FF * 2);
    const i32x4 srd_q = make_srd(ws + WS_WQX, DM * 1024 * 2);
    const i32x4 srd_x = make_srd(ws + WS_XN, SEQ * DM * 2);
    const unsigned vo_w = dkrow * (FF * 2) + 16 * dmi, vo_q = dkrow * 2048 + 16 * dmi, vo_x = l * 16;
    const unsigned dlds = lds0 + dR * 2048 + dmat * 1024;
    auto pieceW = [&](int j, unsigned slot, int cg) { dma16(srd_w, vo_w, (32 * j + 4 * dR) * (FF * 2) + 288 * s * 2 + cg * 64, dlds + slot + cg * 4096); };
    auto pieceQ = [&](int j, unsigned slot) { dma16_sc1(srd_q, vo_q, (32 * j + 4 * dR) * 2048 + (64 * s + 32 * dmat) * 2, dlds + slot + 9 * 4096); };
    auto pieceX = [&](int j, unsigned slot, int n) { dma16_sc1(srd_x, vo_x, ((r * 18 + j) * 8 + n) * 1024, lds0 + slot + 40960 + n * 1024); };
    auto piece1k = [&](int j, unsigned slot, int i, int kind) {
        if (i < 3) { if (kind & 1) pieceW(j, slot, dcg0 + i); }
        else if (i == 3) { if (w == 3) { if (kind & 2) pieceQ(j, slot); } else if (kind & 1) pieceW(j, slot, dcg0 + 3); }
        else if (i == 4) {
            if (w < 3) { if (kind & 1) pieceW(j, slot, 4); } else if (w == 3) { if (kind & 2) pieceX(j, slot, 7); }
            else if (w < 7) { if (kind & 2) pieceQ(j, slot); } else if (kind & 1) pieceW(j, slot, 3);
        } else { if (w == 7) { if (kind & 1) pieceW(j, slot, 4); } else if (kind & 2) pieceX(j, slot, w); }
    };
    auto piece2 = [&](int j, unsigned slot, int i) {
        const int ii = (w >= 4 && i == 4) ? 3 : i;
        const unsigned vo_d = ((vo_w & ~63u) >> 3) + ((vo_w & 48u) << 1);
        dma16(make_srd(wd, FF * DM * 2), vo_d, (288 * s + 32 * j + 4 * dR) * (DM * 2) + 16 * dmat + (dcg0 + ii) * 128, dlds + slot + (dcg0 + ii) * 4096);
    };

    auto pull = [&](int idx) {
        const int lp = fresh_lane();
        const bool gu = idx < 5;
        const int sh = gu ? 3 : 4, rg = (gu ? 8 * idx : 4 * (idx - 5)) + (lp >> sh), seg = lp & (gu ? 7 : 15);
        const bool ok = gu ? (w >= 6 && seg < 5 && rg < 36) : (w == 7 && seg < 9 && rg < 18);
        const unsigned o = (gu ? (rg * 16 + r) * (FF * 2) + ((288 * s * 2) & ~127) : (288 * s + rg * 16 + r) * (DM * 2)) + seg * 128;
        dma4(gu ? srd_w : make_srd(wd, FF * DM * 2), ok ? o : 0xfffffff0u, lds0 + LDS_PF);
    };
    STAMP_DECL;
    STAMP(1, 0);
    {
        const u32x4* xs = (const u32x4*)(x + (size_t)(128 * r + 8 * s + w) * DM);
        const u32x4 v0 = xs[l];
        u32x4 v1 = xs[64 + (l & 7)];
        unsigned qv[8];
        {
            const int tq = min(tid, 287), k = 36 * r + (tq >> 3), mat = (tq >> 2) & 1, t = tq & 3;
            const u16* wc = ((s & 1) ? wv : wk) + k * DH;
            const u16* wh = wq + (size_t)k * DM + (2 * s + (t >> 1)) * DH;
#pragma unroll
            for (int e = 0; e < 8; ++e) {
                const int rho = mat * 8 + e, cc = (t == 1 ? 0 : 12) + rho - 4;
                const bool isq = (t & 1) == 0 || rho < 2, isc = !isq && rho >= 4 && cc < (t == 1 ? 12 : 18);
                const u16* p = isc ? wc + cc : wh + ((t & 1) == 0 ? rho : 16 + (rho & 1));
                const unsigned val = *p;
                qv[e] = (isq || isc) ? val : 0u;
            }
        }
        const int cgp = s & 7, wp = 4 * (r * 2 + (s >> 3)) + ((min(max(tid, 192), 447) - 192) >> 6);
        const bool wo_act = tid >= 192 && tid < 448 && wp < 120;
        u32x4 wov;
        {
            const int m0 = cgp < 4 ? 5 * cgp : 20 + 4 * (cgp - 4), wpc = min(wp, 119);
            const int pp = wpc / 40, js = (wpc % 40) >> 1, R = wpc & 1, i = l >> 2, ch = (l & 3) ^ (2 * ((i >> 2) & 1));
            const int kp = 32 * js + 8 * (i >> 2) + (i & 3) + 4 * R, sp = kp / 40, d = kp - 40 * sp, hd = d >= 20 ? 1 : 0, dd = d - 20 * hd;
            const int row = 36 * sp + 18 * hd + (dd < 18 ? dd : 17), col = min(16 * (m0 + 2 * pp) + 8 * ch, DM - 8);
            wov = *(const u32x4*)(wo + (size_t)row * DM + col);
        }
        FENCE();
        STAMP(1, 13);
        if (l >= 8) v1 = (u32x4){0u, 0u, 0u, 0u};
        float sm = 0.f;
#pragma unroll
        for (int e = 0; e < 4; ++e) sm += bflo(v0[e]) + bfhi(v0[e]) + bflo(v1[e]) + bfhi(v1[e]);
        const float mean = wave_sum_dpp(sm) * (1.0f / DM);
        float q = 0.f, q1 = 0.f;
#pragma unroll
        for (int e = 0; e < 4; ++e) { const float a = bflo(v0[e]) - mean, c = bfhi(v0[e]) - mean; q += a * a + c * c; }
#pragma unroll
        for (int e = 0; e < 4; ++e) { const float a = bflo(v1[e]) - mean, c = bfhi(v1[e]) - mean; q1 += a * a + c * c; }
        if (l < 8) q += q1;
        const float m = rbf(mean), var = rbf(wave_sum_dpp(q) * (1.0f / DM));
        const float rs = 1.0f / rbf(sqrtf(rbf(var + 1e-5f)));
        LAS char* xst = L + 2 * ST1;
        {
            u32x4 o;
#pragma unroll
            for (int e = 0; e < 4; ++e) o[e] = cvtpk(rbf(bflo(v0[e]) - m) * rs, rbf(bfhi(v0[e]) - m) * rs);
            *(LAS u32x4*)(xst + l * 128 + w * 16) = o;
#pragma unroll
            for (int e = 0; e < 4; ++e) o[e] = cvtpk(rbf(bflo(v1[e]) - m) * rs, rbf(bfhi(v1[e]) - m) * rs);
            if (l < 8) *(LAS u32x4*)(xst + (64 + l) * 128 + w * 16) = o;
        }
        asm volatile("" : "+v"(wov));
        LBAR();
        STAMP(1, 14);
        if (w < 7) {
            const __amdgpu_buffer_rsrc_t rx = __builtin_amdgcn_make_buffer_rsrc(ws + WS_XN, 0, SEQ * DM * 2, 0x00020000);
#pragma unroll
            for (int u = 0; u < 2; ++u) {
                const int i = tid + 448 * u, c = i >> 3, t = i & 7;
                const u32x4 v = *(LAS const u32x4*)(xst + min(i, 575) * 16);
                __builtin_amdgcn_raw_buffer_store_b128(v, rx, i < 576 ? (unsigned)(((r * 18 + (c >> 2)) * 8 + (s >> 1)) * 1024 + ((c & 3) * 16 + 8 * (s & 1) + t) * 16) : 0xfffffff0u, 0, 16);
            }
            {
                u32x4 o; o[0] = qv[0] | (qv[1] << 16); o[1] = qv[2] | (qv[3] << 16); o[2] = qv[4] | (qv[5] << 16); o[3] = qv[6] | (qv[7] << 16);
                const __amdgpu_buffer_rsrc_t rq = __builtin_amdgcn_make_buffer_rsrc(ws + WS_WQX, 0, DM * 1024 * 2, 0x00020000);
                __builtin_amdgcn_raw_buffer_store_b128(o, rq, tid < 288 ? (unsigned)(((36 * r + (tid >> 3)) * 128 + s * 8 + (tid & 7)) * 16) : 0xfffffff0u, 0, 16);
            }
            WAIT_BAR(0);
        } else LBAR();
        STAMP(1, 15);
        if (tid == 0) __hip_atomic_store((GAS unsigned*)(flags + FL_PRO + wgi * 4), tag, RLX_AGENT);
#pragma unroll
        for (int i = 0; i < 6; ++i) piece1k(0, 0, i, 1);
#pragma unroll
        for (int i = 0; i < 6; ++i) piece1k(1, ST1, i, 1);
        if (w == 7) {
#pragma unroll
            for (int g = 0; g < 2; ++g) {
                const int rg = 8 * g + (l >> 3);
                const unsigned o = (l & 7) < 5 ? (unsigned)((rg * 16 + r) * (FF * 2) + ((288 * s * 2) & ~127) + (l & 7) * 128) : 0xfffffff0u;
                dma4(make_srd(wg, DM * FF * 2), o, lds0 + LDS_PF); dma4(srd_w, o, lds0 + LDS_PF);
            }
        }
        if (w < 7) {
            const __amdgpu_buffer_rsrc_t rw = __builtin_amdgcn_make_buffer_rsrc(ws + WS_WO, 0, 8 * WOIMG, 0x00020000);
            __builtin_amdgcn_raw_buffer_store_b128(wov, rw, wo_act ? (unsigned)(cgp * WOIMG + wp * 1024 + l * 16) : 0xfffffff0u, 0, 16);
            const __amdgpu_buffer_rsrc_t ro = __builtin_amdgcn_make_buffer_rsrc(ws + WS_O + (size_t)wgi * 128 * 80, 0, 128 * 80, 0x00020000);
            const u32x4 e = (u32x4){O_EMPTY, O_EMPTY, O_EMPTY, O_EMPTY};
            __builtin_amdgcn_raw_buffer_store_b128(e, ro, tid * 16, 0, 16);
            __builtin_amdgcn_raw_buffer_store_b128(e, ro, (tid + 448) * 16, 0, 16);
        }
        STAMP(1, 12);
        if (w == 0) wait_tags(flags, FL_PRO + (l < 16 ? (r * 16 + l) * 4 : ((l - 16) * 16 + s) * 4), l < 32, tag);
        asm volatile("s_barrier" ::: "memory");
#pragma unroll
        for (int i = 3; i < 6; ++i) piece1k(0, 0, i, 2);
#pragma unroll
        for (int i = 3; i < 6; ++i) piece1k(1, ST1, i, 2);
    }
    const int lg = fresh_lane();
    const unsigned a_off = ((lg & 3) >> 1) * 1024 + 16 * (4 * (4 * (lg >> 4) + ((lg >> 2) & 3)) + (fg ^ (2 * ((lg & 3) >> 1) + ((lg >> 4) & 1)))) + 8 * (lg & 1);

    f32x4 acc[10][4];
#pragma unroll
    for (int m = 0; m < 10; ++m)
#pragma unroll
        for (int n = 0; n < 4; ++n) acc[m][n] = (f32x4){0.f, 0.f, 0.f, 0.f};
    bf16x8 ca[2], bA[4], bB[4];
    {
        const unsigned bo = 40960 + tg * 4096 + lg * 16;
        WAIT_BAR(0);
        STAMP(1, 1);
        const bool q3 = w == 3, w4 = w < 3 || w == 7, x4 = w == 3, w5 = w == 7;
        const i32x4 srd3 = q3 ? srd_q : srd_w, srd4 = w4 ? srd_w : x4 ? srd_x : srd_q, srd5 = w5 ? srd_w : srd_x;
        const unsigned vo3 = q3 ? vo_q : vo_w, vo4 = w4 ? vo_w : x4 ? vo_x : vo_q, vo5 = w5 ? vo_w : vo_x;
        const unsigned soW = 4 * dR * (FF * 2) + 288 * s * 2, soQ = 4 * dR * 2048 + (64 * s + 32 * dmat) * 2, soX = r * 18 * 8192;
        const unsigned stW = 32 * FF * 2, stQ = 32 * 2048, stX = 8192;
        const unsigned so3 = q3 ? soQ : soW + (dcg0 + 3) * 64, st3 = q3 ? stQ : stW, lo3 = q3 ? dlds + 9 * 4096 : dlds + (dcg0 + 3) * 4096;
        const int cg4 = w < 3 ? 4 : 3;
        const unsigned so4 = w4 ? soW + cg4 * 64 : x4 ? soX + 7 * 1024 : soQ, st4 = w4 ? stW : x4 ? stX : stQ, lo4 = w4 ? dlds + cg4 * 4096 : x4 ? lds0 + 40960 + 7 * 1024 : dlds + 9 * 4096;
        const unsigned so5 = w5 ? soW + 4 * 64 : soX + w * 1024, st5 = w5 ? stW : stX, lo5 = w5 ? dlds + 4 * 4096 : lds0 + 40960 + w * 1024;
        auto stage1 = [&](int j, unsigned slot, int i) {
            if (i < 3) dma16(srd_w, vo_w, soW + (dcg0 + i) * 64 + j * stW, dlds + (dcg0 + i) * 4096 + slot);
            else if (i == 3) DMA345(srd3, vo3, so3 + j * st3, lo3 + slot);
            else if (i == 4) DMA345(srd4, vo4, so4 + j * st4, lo4 + slot);
            else if (i == 5) DMA345(srd5, vo5, so5 + j * st5, lo5 + slot);
            else if (w >= 6) { const int t = j - 2; pull(t < 6 ? ((t & 1) ? 10 : 2 + (t >> 1)) : t - 1); }
        };
        gemm_step<10, true, 7, 1>(acc, ca, bB, bA, L + a_off, L + bo, [&](int i) { stage1(2, 2 * ST1, i); });
        unsigned slot = ST1, slot2 = 0;
        if (w >= 6) { WAIT_BAR(7); } else { WAIT_BAR(6); }
        gemm_step<10, false, 7, 1>(acc, ca, bA, bB, L + slot + a_off, L + slot + bo, [&](int i) { stage1(3, slot2, i); });
        slot = 2 * ST1; slot2 = ST1;
#pragma unroll 1
        for (int j = 2; j < 16; j += 2) {
            if (w >= 6) { WAIT_BAR(8); } else { WAIT_BAR(6); }
            gemm_step<10, false, 7, 1>(acc, ca, bB, bA, L + slot + a_off, L + slot + bo, [&](int i) { stage1(j + 2, slot2, i); });
            slot = (slot == 2 * ST1) ? 0 : slot + ST1; slot2 = (slot2 == 2 * ST1) ? 0 : slot2 + ST1;
            if (w >= 6) { WAIT_BAR(8); } else { WAIT_BAR(6); }
            gemm_step<10, false, 7, 1>(acc, ca, bA, bB, L + slot + a_off, L + slot + bo, [&](int i) { stage1(j + 3, slot2, i); });
            slot = (slot == 2 * ST1) ? 0 : slot + ST1; slot2 = (slot2 == 2 * ST1) ? 0 : slot2 + ST1;
        }
#pragma unroll
        for (int n = 0; n < 4; ++n) bA[n] = bB[n];
#pragma unroll 1
        for (int j = 16; j < 18; ++j) {
            if (j == 16 && w >= 6) { WAIT_BAR(8); } else if (j == 16) { WAIT_BAR(6); } else { WAIT_BAR(5); }
            gemm_step<10, false, 5, 1>(acc, ca, bA, bB, L + slot + a_off, L + slot + bo, [&](int i) { piece2(j - 16, j == 16 ? LDS_D0 : LDS_D1, i); });
#pragma unroll
            for (int n = 0; n < 4; ++n) bA[n] = bB[n];
            slot += ST1;
        }
        gemm_flush<10>(acc, ca, bA);
        SB(); FENCE();
    }
    if ((fg & 1) && tg == (s >> 3)) {
        const int l = fresh_lane(), tok = l & 15, g4 = l >> 4;
        const int e = s >> 1;
        f32x4 cv = acc[9][0];
        if ((e & 3) == 1) cv = acc[9][1]; else if ((e & 3) == 2) cv = acc[9][2]; else if ((e & 3) == 3) cv = acc[9][3];
        const int c0 = (fg == 1 ? 0 : 12) + 4 * (g4 - 1);
        const bool act = g4 >= 1 && c0 < 18;
        const unsigned p0 = cvtpk(cv[0], cv[1]), p1 = cvtpk(cv[2], cv[3]);
        const int kt = 4 * r + (e >> 1);
        char* kvb = ws + WS_KV + ((kt >> 3) & 3) * KVCH; const int lt = 8 * (kt >> 5) + (kt & 7);
        if ((s & 1) == 0) {
            const int key = 16 * (e & 1) + tok;
            if (act) {
                if (c0 < 16) st_agent64(kvb + lt * 1024 + ((c0 >> 3) * 32 + key) * 16 + (c0 & 4) * 2, p0, p1);
                else __hip_atomic_store((GAS unsigned*)(kvb + KV_K2 + lt * 128 + key * 4), p0, RLX_AGENT);
            }
            float n2 = 0.f;
            if (act) { n2 = bflo(p0) * bflo(p0) + bfhi(p0) * bfhi(p0); if (c0 < 16) n2 += bflo(p1) * bflo(p1) + bfhi(p1) * bfhi(p1); }
            n2 += xor_lane<16>(n2); n2 = half_sum(n2);
            n2 = wave_max(n2);
            if (l == 0) *(LAS float*)(L + LDS_KM + (fg == 1 ? 0 : 4)) = n2;
        } else {
            if (l == 0) *(LAS float*)(L + LDS_KM + (fg == 1 ? 0 : 4)) = 0.f;
            if (fg == 1 && l < 2) {
                char* op = kvb + KV_V + lt * 1216 + (e & 1) * 608 + l * 304 + 18 * 16;
                st_agent64(op, 0x3f803f80u, 0x3f803f80u); st_agent64(op + 8, 0x3f803f80u, 0x3f803f80u);
            }
            const int hv = (tok >> 2) & 1, jj = 4 * (tok >> 3) + (tok & 3);
            GAS u16* vb = (GAS u16*)(kvb + KV_V + lt * 1216 + (e & 1) * 608 + hv * 304 + jj * 2);
            if (act) {
                __hip_atomic_store(vb + (c0 + 0) * 8, (u16)(p0 & 0xffff), RLX_AGENT);
                __hip_atomic_store(vb + (c0 + 1) * 8, (u16)(p0 >> 16), RLX_AGENT);
                if (c0 < 16) { __hip_atomic_store(vb + (c0 + 2) * 8, (u16)(p1 & 0xffff), RLX_AGENT);
                               __hip_atomic_store(vb + (c0 + 3) * 8, (u16)(p1 >> 16), RLX_AGENT); }
            }
        }
    }
    LBAR();
    STAMP(1, 2);
    {
        const int l = fresh_lane(), tok = l & 15, g4 = l >> 4;
#pragma unroll
        for (int cg = 0; cg < 9; ++cg)
#pragma unroll
            for (int pr = 0; pr < 2; ++pr) {
                float h[4];
#pragma unroll
                for (int e = 0; e < 4; ++e) {
                    auto sw = __builtin_amdgcn_permlane32_swap(__float_as_uint(acc[cg][2 * pr][e]), __float_as_uint(acc[cg][2 * pr + 1][e]), false, false);
                    h[e] = fmaxf(__uint_as_float(sw[0]), 0.f) * __uint_as_float(sw[1]);
                }
                const int n = 4 * tg + 2 * pr + (l >> 5);
                u32x2 o; o[0] = cvtpk(h[0], h[1]); o[1] = cvtpk(h[2], h[3]);
                *(LAS u32x2*)(L + LDS_H + ((8 - cg) * 8 + n) * 1024 + (fg * 16 + tok) * 16 + 8 * (g4 & 1)) = o;
            }
        const float qs = 0.23570226039551584f * 1.4426950408889634f;
        const int hh = fg >> 1;
#pragma unroll
        for (int n = 0; n < 4; ++n) {
            const int ct = 2 * tg + (n >> 1), query = 16 * (n & 1) + tok;
            const unsigned p0 = cvtpk(acc[9][n][0] * qs, acc[9][n][1] * qs), p1 = cvtpk(acc[9][n][2] * qs, acc[9][n][3] * qs);
            if ((fg & 1) == 0) *(LAS u32x2*)(L + LDS_Q1 + (hh * 4 + ct) * 1024 + ((g4 >> 1) * 32 + query) * 16 + (g4 & 1) * 8) = (u32x2){p0, p1};
            else if (g4 == 0) *(LAS unsigned*)(L + LDS_Q2 + (hh * 4 + ct) * 128 + query * 4) = p0;
        }
    }
    WAIT_BAR(0);
    if (w == 0 && fresh_lane() == 0) {
        st_agent64(flags + FL_KV + wgi * 16, tag, *(LAS const unsigned*)(L + LDS_KM));
        st_agent64(flags + FL_KV + wgi * 16 + 8, tag, *(LAS const unsigned*)(L + LDS_KM + 4));
    }
    STAMP(1, 3);
    const i32x4 srd_kv = make_srd(ws + WS_KV, 4 * KVCH);
    auto piece_kv = [&](int c, int i) {
        const int p = min(w + 8 * i, 36);
        dma16_sc1(srd_kv, vo_x, c * KVCH + p * 1024, lds0 + (c == 0 ? LDS_KV0 : (c - 1) * KVCH) + p * 1024);
    };
#pragma unroll
    for (int m = 0; m < 9; ++m)
#pragma unroll
        for (int n = 0; n < 4; ++n) acc[m][n] = (f32x4){0.f, 0.f, 0.f, 0.f};
    {
        LAS const char* hb = L + LDS_H + 8 * 8192 + 4 * tg * 1024 + l * 16;
        gemm_step<9, true, 0, 1>(acc, ca, bB, bA, L + LDS_D0 + a_off, hb, [](int) {});
        LBAR();
        gemm_step<9, false, 5, 2>(acc, ca, bA, bB, L + LDS_D1 + a_off, hb - 1 * 8192, [&](int i) { piece2(2, LDS_D0, i); });
        WAIT_BAR(0);
        gemm_step<9, false, 5, 2>(acc, ca, bB, bA, L + LDS_D0 + a_off, hb - 2 * 8192, [&](int i) { piece2(3, LDS_D1, i); });
        WAIT_BAR(0);
        gemm_step<9, false, 5, 2>(acc, ca, bA, bB, L + LDS_D1 + a_off, hb - 3 * 8192, [&](int i) { piece2(4, LDS_D0, i); });
        WAIT_BAR(0);
        gemm_step<9, false, 10, 2>(acc, ca, bB, bA, L + LDS_D0 + a_off, hb - 4 * 8192, [&](int i) { if (i < 5) piece2(5, LDS_D1, i); else piece2(6, LDS_D2, i - 5); });
        WAIT_BAR(5);
        gemm_step<9, false, 5, 1>(acc, ca, bA, bB, L + LDS_D1 + a_off, hb - 5 * 8192, [&](int i) { piece2(7, LDS_D0, i); });
        WAIT_BAR(5);
        gemm_step<9, false, 5, 1>(acc, ca, bB, bA, L + LDS_D2 + a_off, hb - 6 * 8192, [&](int i) { piece2(8, LDS_D1, i); });
        if (w == 0) {
            const int lk = fresh_lane();
            const __amdgpu_buffer_rsrc_t rk = __builtin_amdgcn_make_buffer_rsrc(flags + FL_KV, 0, 4096, 0x00020000);
            float ma, mb;
            for (unsigned spins = 0;; ++spins) {
                bool ok = true; ma = 0.f; mb = 0.f;
#pragma unroll
                for (int i = 0; i < 4; ++i) {
                    const u32x4 v = __builtin_amdgcn_raw_buffer_load_b128(rk, (i * 64 + lk) * 16, 0, 16);
                    ok = ok && v[0] == tag && v[2] == tag; ma = fmaxf(ma, __uint_as_float(v[1])); mb = fmaxf(mb, __uint_as_float(v[3]));
                }
                if (__all(ok)) break;
                if (spins > (1u << 19)) { __hip_atomic_store((GAS unsigned*)(flags + FL_TMO), tag, RLX_AGENT); break; }
                __builtin_amdgcn_s_sleep(1);
            }
            ma = wave_max(ma); mb = wave_max(mb);
            if (lk == 0) *(LAS float*)(L + LDS_KM + 8) = ma + mb;
        }
        WAIT_BAR(5);
        gemm_step<9, false, 5, 1>(acc, ca, bA, bB, L + LDS_D0 + a_off, hb - 7 * 8192, [&](int i) { piece_kv(0, i); });
        WAIT_BAR(5);
        gemm_step<9, false, 0, 1>(acc, ca, bB, bA, L + LDS_D1 + a_off, hb - 8 * 8192, [](int) {});
        gemm_flush<9>(acc, ca, bA);
        SB(); FENCE();
    }
    STAMP(1, 4);
    const int la = fresh_lane();
#define l la
    const int hh = w >> 2, kh = (w >> 1) & 1, cp = w & 1;
#define h5 (l >> 5)
    bf16x8 q1[2]; u32x2 q2[2];
#pragma unroll
    for (int c = 0; c < 2; ++c) {
        q1[c] = lds_b128(L + LDS_Q1 + (hh * 4 + 2 * cp + c) * 1024 + l * 16);
        q2[c][0] = *(LAS const unsigned*)(L + LDS_Q2 + (hh * 4 + 2 * cp + c) * 128 + (l & 31) * 4);
    }
    u32x4 pk[18];
#pragma unroll
    for (int m = 0; m < 9; ++m)
#pragma unroll
        for (int pr = 0; pr < 2; ++pr) {
            u32x4 o; o[0] = cvtpk(acc[m][2 * pr][0], acc[m][2 * pr][1]); o[1] = cvtpk(acc[m][2 * pr][2], acc[m][2 * pr][3]);
            o[2] = cvtpk(acc[m][2 * pr + 1][0], acc[m][2 * pr + 1][1]); o[3] = cvtpk(acc[m][2 * pr + 1][2], acc[m][2 * pr + 1][3]);
            pk[m * 2 + pr] = o;
        }
    {
        const float kmax2 = *(LAS const float*)(L + LDS_KM + 8);
#pragma unroll
        for (int c = 0; c < 2; ++c) {
            const u32x4 a = __builtin_bit_cast(u32x4, q1[c]); const unsigned t = h5 ? 0u : q2[c][0];
            float n2 = bflo(t) * bflo(t) + bfhi(t) * bfhi(t);
#pragma unroll
            for (int e = 0; e < 4; ++e) n2 += bflo(a[e]) * bflo(a[e]) + bfhi(a[e]) * bfhi(a[e]);
            n2 = half_sum(n2);
            const float cb = sqrtf(n2 * kmax2) * 1.001f + 0.01f;
            const float chi = rbf(cb), clo = cb - chi;
            q2[c][0] = t; q2[c][1] = h5 ? 0u : cvtpk(-chi, -clo);
        }
    }
    if (w == 0 && l < 40) *(LAS u32x4*)(L + LDS_ZERO + l * 16) = (u32x4){0u, 0u, 0u, 0u};
    if (w == 0 && l == 40) *(LAS unsigned*)(L + LDS_CTL) = 0u;
    WAIT_BAR(0);
    STAMP(1, 5);
#undef l
    const int lb = fresh_lane();
#define l lb
    const unsigned kone = h5 ? 0u : 0x3f803f80u;
    const bool vrow = (l & 31) < 19;
    const unsigned k1_base = 8 * kh * 1024 + l * 16, k2_base = KV_K2 + 8 * kh * 128 + (l & 31) * 4;
    const unsigned v_base = vrow ? KV_V + 8 * kh * 1216 + h5 * 304 + (l & 31) * 16 : LDS_ZERO, v_mul = vrow ? 1u : 0u;
    const int tgf = s >> 3, cgp = s & 7, nmt = cgp < 4 ? 5 : 4, m0 = cgp < 4 ? 5 * cgp : 20 + 4 * (cgp - 4);
    const int pr = w & 1, qk = w >> 1;
    const i32x4 srd_wo = make_srd(ws + WS_WO + (size_t)cgp * WOIMG, WOIMG);
    auto piece_wo = [&](int p) { dma16(srd_wo, vo_x, p * 1024, lds0 + wo_lds(p)); };
    const __amdgpu_buffer_rsrc_t rpart = __builtin_amdgcn_make_buffer_rsrc(ws + WS_PART + (size_t)r * 16 * 8 * 18 * 1024, 0, 16 * 8 * 18 * 1024, 0x00020000);
    float own[11];
    u32x4 pl[5][4];

    f32x16 o0, o1, sX, sY; bf16x8 kA, kB, v0A, v1A, v0B, v1B; unsigned k2A, k2B; u32x4 p0, p1; float e3[4];
    unsigned aK, aK2, aV;
#define SLOT(SC, SN, KN, K2N, CN, OP, V1P, OC, V0C) do { float ea[4], eb[4]; \
        SN = MFMA32(KN, q1[CN], (f32x16){}); SB(); \
        ea[0] = EXP2(SC[0]); ea[1] = EXP2(SC[1]); ea[2] = EXP2(SC[2]); ea[3] = EXP2(SC[3]); \
        p1[2] = cvtpk(e3[0], e3[1]); p1[3] = cvtpk(e3[2], e3[3]); SB(); \
        OP = MFMA32(V1P, __builtin_bit_cast(bf16x8, p1), OP); SB(); \
        eb[0] = EXP2(SC[4]); eb[1] = EXP2(SC[5]); eb[2] = EXP2(SC[6]); eb[3] = EXP2(SC[7]); \
        p0[0] = cvtpk(ea[0], ea[1]); p0[1] = cvtpk(ea[2], ea[3]); SB(); \
        SN = MFMA32K8(__builtin_bit_cast(s16x4, ((u32x2){K2N, kone})), __builtin_bit_cast(s16x4, q2[CN]), SN); SB(); \
        ea[0] = EXP2(SC[8]); ea[1] = EXP2(SC[9]); ea[2] = EXP2(SC[10]); ea[3] = EXP2(SC[11]); \
        p0[2] = cvtpk(eb[0], eb[1]); p0[3] = cvtpk(eb[2], eb[3]); SB(); \
        OC = MFMA32(V0C, __builtin_bit_cast(bf16x8, p0), OC); SB(); \
        e3[0] = EXP2(SC[12]); e3[1] = EXP2(SC[13]); e3[2] = EXP2(SC[14]); e3[3] = EXP2(SC[15]); \
        p1[0] = cvtpk(ea[0], ea[1]); p1[1] = cvtpk(ea[2], ea[3]); SB(); } while (0)
#define PAIR(DK, DK2, DV, H0, H1, H2, H3) do { \
        kB = lds_b128(L + aK + 1024); k2B = *(LAS const unsigned*)(L + aK2 + 128); v0B = lds_b128(L + aV + v_mul * 1216); SB(); \
        H0; SLOT(sX, sY, kA, k2A, 1, o1, v1B, o0, v0A); \
        v1B = lds_b128(L + aV + v_mul * 1216 + 608); SB(); \
        H1; SLOT(sY, sX, kB, k2B, 0, o0, v1A, o1, v0A); \
        aK += (DK); aK2 += (DK2); aV += v_mul * (DV); \
        kA = lds_b128(L + aK); k2A = *(LAS const unsigned*)(L + aK2); v0A = lds_b128(L + aV); SB(); \
        H2; SLOT(sX, sY, kB, k2B, 1, o1, v1A, o0, v0B); \
        v1A = lds_b128(L + aV + 608); SB(); \
        H3; SLOT(sY, sX, kA, k2A, 0, o0, v1B, o1, v0B); } while (0)
#define PAIR_IN(H0, H1, H2, H3) PAIR(2048, 256, 2432, H0, H1, H2, H3)
#define PAIR_JUMP(J, H0, H1, H2, H3) PAIR((J) - 6 * 1024, (J) - 6 * 128, (J) - 6 * 1216, H0, H1, H2, H3)
#define NOP_ ((void)0)
#define ATT_INIT() do { o0 = (f32x16){}; o1 = (f32x16){}; aK = k1_base + LDS_KV0; aK2 = k2_base + LDS_KV0; aV = v_base + v_mul * LDS_KV0; \
        kA = lds_b128(L + aK); k2A = *(LAS const unsigned*)(L + aK2); v0A = lds_b128(L + aV); v1A = lds_b128(L + aV + 608); v1B = v1A; \
        sX = MFMA32(kA, q1[0], (f32x16){}); sX = MFMA32K8(__builtin_bit_cast(s16x4, ((u32x2){k2A, kone})), __builtin_bit_cast(s16x4, q2[0]), sX); \
        p1 = (u32x4){0u, 0u, 0u, 0u}; e3[0] = e3[1] = e3[2] = e3[3] = 0.f; } while (0)
#define ATT_FIN() do { p1[2] = cvtpk(e3[0], e3[1]); p1[3] = cvtpk(e3[2], e3[3]); o1 = MFMA32(v1B, __builtin_bit_cast(bf16x8, p1), o1); } while (0)
    constexpr int J01 = 0 - LDS_KV0, J12 = KVCH;
    {
        const __amdgpu_buffer_rsrc_t rp = __builtin_amdgcn_make_buffer_rsrc(ws + WS_PART + (size_t)(r * 16 + s) * 8 * 18 * 1024, 0, 8 * 18 * 1024, 0x00020000);
#define KV(C, I) do { piece_kv(C, I); SB(); } while (0)
#define ST(I) do { __builtin_amdgcn_raw_buffer_store_b128(pk[I], rp, vo_x, (w * 18 + (I)) * 1024, 16); SB(); } while (0)
#define WO(P0, I) do { piece_wo(min((P0) + w + 8 * (I), (P0) + 36)); SB(); } while (0)
#define PL(T, SP) do { const int m_ = m0 + ((T) < nmt ? (T) : 0); \
            pl[T][SP] = __builtin_amdgcn_raw_buffer_load_b128(rpart, vo_x, ((((4 * qk + (SP)) * 8 + tgf * 4 + (m_ & 3)) * 9 + (m_ >> 2)) * 2 + pr) * 1024, 16); SB(); } while (0)
#define PL2(A) do { PL((A) >> 2, (A) & 3); PL(((A) + 1) >> 2, ((A) + 1) & 3); } while (0)
        ATT_INIT();
        PAIR_IN(KV(1, 0), KV(1, 1), KV(1, 2), KV(1, 3));
        PAIR_IN(KV(1, 4), ST(0), KV(2, 0), ST(1));
        PAIR_IN(KV(2, 1), ST(2), KV(2, 2), ST(3));
        WAIT_BAR(7);
        PAIR_JUMP(J01, KV(2, 3), ST(4), KV(2, 4), ST(5));
        PAIR_IN(KV(3, 0), ST(6), KV(3, 1), ST(7));
        PAIR_IN(KV(3, 2), ST(8), KV(3, 3), ST(9));
        PAIR_IN(KV(3, 4), ST(10), NOP_, ST(11));
        WAIT_BAR(12);
        PAIR_JUMP(J12, WO(0, 0), ST(12), WO(0, 1), ST(13));
        PAIR_IN(WO(0, 2), ST(14), WO(0, 3), ST(15));
        PAIR_IN(WO(0, 4), ST(16), NOP_, ST(17));
        PAIR_IN(NOP_, NOP_, NOP_, NOP_);
        WAIT_BAR(0);
        if (w == 0 && fresh_lane() == 0) __hip_atomic_store((GAS unsigned*)(flags + FL_P + wgi * 4), tag, RLX_AGENT);
        PAIR_JUMP(J12, WO(37, 0), WO(37, 1), WO(37, 2), WO(37, 3));
        const unsigned pflag = __hip_atomic_load((GAS unsigned*)(flags + FL_P + (r * 16 + (l & 15)) * 4), RLX_AGENT);
        PAIR_IN(WO(37, 4), NOP_, NOP_, NOP_);
        LBAR();
        if (!__all(pflag == tag)) wait_tags(flags, FL_P + (r * 16 + (l & 15)) * 4, true, tag);
#define WP(I, A) do { WO(74, I); PL((A) >> 2, (A) & 3); } while (0)
        PAIR_IN(WP(0, 0), WP(1, 1), WP(2, 2), WP(3, 3));
        PAIR_IN(WP(4, 4), PL2(5), PL2(7), PL2(9));
        PAIR(0, 0, 0, PL2(11), PL2(13), PL2(15), PL2(17));
        PL(4, 3);
        ATT_FIN();
    }
    STAMP(1, 6);
#undef l
    const int lc = fresh_lane();
#define l lc
    for (int attempt = 0;; ++attempt) {
        if (attempt == 1) {
            const unsigned kone = h5 ? 0u : 0x3f803f80u;
            const bool vrow = (l & 31) < 19;
            const unsigned k1_base = 8 * kh * 1024 + l * 16, k2_base = KV_K2 + 8 * kh * 128 + (l & 31) * 4;
            const unsigned v_base = vrow ? KV_V + 8 * kh * 1216 + h5 * 304 + (l & 31) * 16 : LDS_ZERO, v_mul = vrow ? 1u : 0u;
            WAIT_BAR(0);
#pragma unroll
            for (int c = 0; c < 4; ++c)
#pragma unroll
                for (int i = 0; i < 5; ++i) piece_kv(c, i);
            if (w == 0 && l < 40) *(LAS u32x4*)(L + LDS_ZERO + l * 16) = (u32x4){0u, 0u, 0u, 0u};
            WAIT_BAR(0);
            float m[2] = {-INFINITY, -INFINITY};
            for (int i = 0; i < 32; ++i) {
                const unsigned to = (i >> 3) == 0 ? LDS_KV0 : ((i >> 3) - 1) * KVCH;
                const bf16x8 k1 = lds_b128(L + k1_base + to + (i & 7) * 1024);
                const u32x2 k2 = {*(LAS const unsigned*)(L + k2_base + to + (i & 7) * 128), kone};
#pragma unroll
                for (int c = 0; c < 2; ++c) {
                    const u32x2 qz = {q2[c][0], 0u};
                    f32x16 sc = MFMA32(k1, q1[c], (f32x16){});
                    sc = MFMA32K8(__builtin_bit_cast(s16x4, k2), __builtin_bit_cast(s16x4, qz), sc);
#pragma unroll
                    for (int e = 0; e < 16; ++e) m[c] = fmaxf(m[c], sc[e]);
                }
            }
            LAS float* mx = (LAS float*)(L + LDS_MX);
#pragma unroll
            for (int c = 0; c < 2; ++c) { m[c] = half_max(m[c]); if (l < 32) mx[(w * 2 + c) * 32 + l] = m[c]; }
            LBAR();
#pragma unroll
            for (int c = 0; c < 2; ++c) {
                const float mm = fmaxf(m[c], mx[((w ^ 2) * 2 + c) * 32 + (l & 31)]);
                const float chi = rbf(mm), clo = mm - chi;
                q2[c][1] = h5 ? 0u : cvtpk(-chi, -clo);
            }
            o0 = (f32x16){}; o1 = (f32x16){};
            for (int i = 0; i < 32; ++i) {
                const unsigned to = (i >> 3) == 0 ? LDS_KV0 : ((i >> 3) - 1) * KVCH;
                const bf16x8 k1 = lds_b128(L + k1_base + to + (i & 7) * 1024);
                const u32x2 k2 = {*(LAS const unsigned*)(L + k2_base + to + (i & 7) * 128), kone};
                const unsigned va = v_base + v_mul * (to + (i & 7) * 1216);
                const bf16x8 v0 = lds_b128(L + va), v1 = lds_b128(L + va + 608);
#pragma unroll
                for (int c = 0; c < 2; ++c) {
                    f32x16 sc = MFMA32(k1, q1[c], (f32x16){});
                    sc = MFMA32K8(__builtin_bit_cast(s16x4, k2), __builtin_bit_cast(s16x4, q2[c]), sc);
                    u32x4 pa, pb;
#pragma unroll
                    for (int e = 0; e < 4; ++e) { pa[e] = cvtpk(EXP2(sc[2 * e]), EXP2(sc[2 * e + 1])); pb[e] = cvtpk(EXP2(sc[8 + 2 * e]), EXP2(sc[9 + 2 * e])); }
                    if (c == 0) { o0 = MFMA32(v0, __builtin_bit_cast(bf16x8, pa), o0); o0 = MFMA32(v1, __builtin_bit_cast(bf16x8, pb), o0); }
                    else { o1 = MFMA32(v0, __builtin_bit_cast(bf16x8, pa), o1); o1 = MFMA32(v1, __builtin_bit_cast(bf16x8, pb), o1); }
                }
            }
        }
        LBAR();
        if (attempt == 0) {
            piece_wo(111 + w); if (w == 0) piece_wo(119);
        } else {
#pragma unroll
            for (int q = 0; q < 15; ++q) piece_wo(w * 15 + q);
        }
        LAS float* red = (LAS float*)(L + LDS_RED);
#pragma unroll
        for (int e = 0; e < 11; ++e) { red[(w * 11 + e) * 64 + l] = kh ? o0[e] : o1[e]; own[e] = kh ? o1[e] : o0[e]; }
        LBAR();
#pragma unroll
        for (int e = 0; e < 11; ++e) own[e] += red[((w ^ 2) * 11 + e) * 64 + l];
        const float lsum = half_lo(own[10]);
        if (!(lsum >= 8.67e-19f && lsum < 3.0e38f) && attempt == 0) *(LAS unsigned*)(L + LDS_CTL) = 1u;
        const float rl = 1.0f / lsum;
        LAS char* ot = L + LDS_OT + (32 * (2 * cp + kh) + (l & 31)) * 80 + 40 * hh;
        *(LAS u32x2*)(ot + 8 * h5) = (u32x2){cvtpk(own[0] * rl, own[1] * rl), cvtpk(own[2] * rl, own[3] * rl)};
        *(LAS u32x2*)(ot + 16 + 8 * h5) = (u32x2){cvtpk(own[4] * rl, own[5] * rl), cvtpk(own[6] * rl, own[7] * rl)};
        if (h5 == 0) *(LAS u32x2*)(ot + 32) = (u32x2){cvtpk(own[8] * rl, own[9] * rl), 0u};
        LBAR();
        const unsigned isbad = *(LAS const unsigned*)(L + LDS_CTL);
        if (!(isbad && attempt == 0)) break;
    }
    STAMP(1, 7);
    {
        const __amdgpu_buffer_rsrc_t rw = __builtin_amdgcn_make_buffer_rsrc(ws + WS_O + (size_t)(r * 16 + s) * 128 * 80, 0, 128 * 80, 0x00020000);
        __builtin_amdgcn_raw_buffer_store_b128(*(LAS const u32x4*)(L + LDS_OT + w * 1024 + l * 16), rw, l * 16, w * 1024, 16);
        if (w < 2) __builtin_amdgcn_raw_buffer_store_b128(*(LAS const u32x4*)(L + LDS_OT + (8 + w) * 1024 + l * 16), rw, l * 16, (8 + w) * 1024, 16);
    }
    {
        const int tok = l & 15, g4 = l >> 4, q4 = (l >> 2) & 3, p4 = l & 3;
        u32x2 xv[3];
#pragma unroll
        for (int u = 0; u < 3; ++u) {
            const int tt = min(qk + 4 * u, 2 * nmt - 1);
            xv[u] = *(const u32x2*)(x + (size_t)(128 * r + 64 * tgf + 32 * pr + 16 * (tt & 1) + tok) * DM + 16 * (m0 + (tt >> 1)) + 4 * g4);
        }
        SB();
        STAMP(1, 8);
        const __amdgpu_buffer_rsrc_t ro = __builtin_amdgcn_make_buffer_rsrc(ws + WS_O + (size_t)r * 16 * 128 * 80, 0, 16 * 128 * 80, 0x00020000);
        u32x4 bf[5][2];
        unsigned pend = 0x3ffu;
        for (unsigned spins = 0;; ++spins) {
#pragma unroll
            for (int i = 0; i < 5; ++i) {
                const int kp = 32 * (5 * qk + i) + 8 * g4, sp = kp / 40, d = kp - 40 * sp;
#pragma unroll
                for (int nn = 0; nn < 2; ++nn)
                    if (pend & (1u << (2 * i + nn))) bf[i][nn] = __builtin_amdgcn_raw_buffer_load_b128(ro, (sp * 128 + 64 * tgf + 32 * pr + 16 * nn + tok) * 80 + d * 2, 0, 16);
            }
#pragma unroll
            for (int i = 0; i < 5; ++i)
#pragma unroll
                for (int nn = 0; nn < 2; ++nn)
                    if ((pend & (1u << (2 * i + nn))) && __all(bf[i][nn][0] != O_EMPTY && bf[i][nn][1] != O_EMPTY && bf[i][nn][2] != O_EMPTY && bf[i][nn][3] != O_EMPTY)) pend &= ~(1u << (2 * i + nn));
            if (pend == 0u) break;
            if (spins > (1u << 17)) { __hip_atomic_store((GAS unsigned*)(flags + FL_TMO), tag, RLX_AGENT); break; }
            __builtin_amdgcn_s_sleep(8);
        }
        f32x4 fa[5][2];
#pragma unroll
        for (int t = 0; t < 5; ++t) {
            fa[t][0] = (f32x4){0.f, 0.f, 0.f, 0.f}; fa[t][1] = (f32x4){0.f, 0.f, 0.f, 0.f};
#pragma unroll
            for (int sp = 0; sp < 4; ++sp) {
                const u32x4 p = pl[t][sp];
                fa[t][0][0] += bflo(p[0]); fa[t][0][1] += bfhi(p[0]); fa[t][0][2] += bflo(p[1]); fa[t][0][3] += bfhi(p[1]);
                fa[t][1][0] += bflo(p[2]); fa[t][1][1] += bfhi(p[2]); fa[t][1][2] += bflo(p[3]); fa[t][1][3] += bfhi(p[3]);
            }
        }
        WAIT_BAR(0);
        STAMP(1, 9);
        const unsigned ao = (4 * g4 + q4) * 64 + 8 * (p4 & 1);
#pragma unroll
        for (int t = 0; t < 5; ++t)
            if (t < nmt) {
#pragma unroll
                for (int i = 0; i < 5; ++i) {
                    const int p = (t >> 1) * 40 + (5 * qk + i) * 2;
                    const unsigned sw = ao + 16 * ((2 * (t & 1) + (p4 >> 1)) ^ (2 * (g4 & 1)));
                    const s16x4 lo = lds_tr(L + wo_lds(p) + sw), hi = lds_tr(L + wo_lds(p + 1) + sw);
                    const bf16x8 a = __builtin_shufflevector(lo, hi, 0, 1, 2, 3, 4, 5, 6, 7);
                    fa[t][0] = MFMA16(a, __builtin_bit_cast(bf16x8, bf[i][0]), fa[t][0]); fa[t][1] = MFMA16(a, __builtin_bit_cast(bf16x8, bf[i][1]), fa[t][1]);
                }
            }
        STAMP(1, 10);
        LBAR();
        LAS f32x4* ex = (LAS f32x4*)L;
#pragma unroll
        for (int t = 0; t < 5; ++t) { ex[(w * 10 + 2 * t) * 64 + l] = fa[t][0]; ex[(w * 10 + 2 * t + 1) * 64 + l] = fa[t][1]; }
        LBAR();
#pragma unroll
        for (int u = 0; u < 3; ++u) {
            const int tt = qk + 4 * u;
            if (tt < 2 * nmt) {
                f32x4 v = ex[((0 * 2 + pr) * 10 + tt) * 64 + l];
#pragma unroll
                for (int k = 1; k < 4; ++k) v += ex[((k * 2 + pr) * 10 + tt) * 64 + l];
                const size_t off = (size_t)(128 * r + 64 * tgf + 32 * pr + 16 * (tt & 1) + tok) * DM + 16 * (m0 + (tt >> 1)) + 4 * g4;
                *(u32x2*)(out + off) = (u32x2){cvtpk(v[0] + bflo(xv[u][0]), v[1] + bfhi(xv[u][0])), cvtpk(v[2] + bflo(xv[u][1]), v[3] + bfhi(xv[u][1]))};
            }
        }
    }
#undef l
#undef h5
    STAMP(1, 11); STAMP_END(1);
}

extern "C" void kernel_launch(void* const* d_in, const int* in_sizes, int n_in, void* d_out, int out_size, void* d_ws, size_t ws_size, hipStream_t stream) {
    const u16* x = (const u16*)d_in[0]; const u16* wq = (const u16*)d_in[1]; const u16* wk = (const u16*)d_in[2]; const u16* wv = (const u16*)d_in[3];
    const u16* wo = (const u16*)d_in[4]; const u16* wg = (const u16*)d_in[6]; const u16* wu = (const u16*)d_in[7]; const u16* wd = (const u16*)d_in[8];
    fused_block<<<256, 512, 0, stream>>>(x, wq, wk, wv, wo, wg, wu, wd, (char*)d_ws, (u16*)d_out);
}
```

```cpp
#include <hip/hip_runtime.h>
#include <hip/hip_bf16.h>
#include <stdint.h>

typedef unsigned short u16;
typedef short bf16x8 __attribute__((ext_vector_type(8)));
typedef short s16x4 __attribute__((ext_vector_type(4)));
typedef float f32x4 __attribute__((ext_vector_type(4)));
typedef float f32x16 __attribute__((ext_vector_type(16)));
typedef int i32x4 __attribute__((ext_vector_type(4)));
typedef unsigned u32x4 __attribute__((ext_vector_type(4)));
typedef unsigned u32x2 __attribute__((ext_vector_type(2)));
typedef __bf16 bf16x2_t __attribute__((ext_vector_type(2)));
typedef float f32x2 __attribute__((ext_vector_type(2)));
typedef short v4i16_t __attribute__((ext_vector_type(4)));
extern "C" __device__ unsigned long long amd_dispatch_id() __asm("llvm.amdgcn.dispatch.id");
#define LAS __attribute__((address_space(3)))
#define GAS __attribute__((address_space(1)))
#define DI __device__ __forceinline__

constexpr int SEQ = 2048, DM = 576, NH = 32, DH = 18, FF = 4608;

constexpr int KVCH = 37888;
constexpr int KV_V = 16384, KV_K2 = 35840;
constexpr int WOIMG = 120 * 1024;
constexpr size_t WS_FLAGS = 0;
constexpr size_t WS_XN    = 16384;
constexpr size_t WS_WQX   = WS_XN + (size_t)SEQ * DM * 2;
constexpr size_t WS_WO    = WS_WQX + (size_t)DM * 1024 * 2;
constexpr size_t WS_KV    = WS_WO + (size_t)8 * WOIMG;
constexpr size_t WS_O     = WS_KV + 4 * KVCH;
constexpr size_t WS_PART  = WS_O + (size_t)256 * 128 * 80;
constexpr size_t WS_END   = WS_PART + (size_t)256 * 8 * 18 * 1024;
constexpr int FL_PRO = 0, FL_P = 1024, FL_KV = 2048, FL_TMO = 8192;
constexpr unsigned O_EMPTY = 0xffffffffu;

DI float bflo(unsigned v) { return __uint_as_float(v << 16); }
DI float bfhi(unsigned v) { return __uint_as_float(v & 0xffff0000u); }
DI unsigned cvtpk(float lo, float hi) { return __builtin_bit_cast(unsigned, __builtin_convertvector((f32x2){lo, hi}, bf16x2_t)); }
DI float rbf(float f) { return bflo(cvtpk(f, f)); }
DI i32x4 make_srd(const void* p, unsigned bytes) {
    unsigned long long a = (unsigned long long)p; i32x4 s;
    s.x = __builtin_amdgcn_readfirstlane((unsigned)a); s.y = __builtin_amdgcn_readfirstlane((unsigned)(a >> 32) & 0xffffu);
    s.z = (int)bytes; s.w = 0x00020000; return s;
}
DI void dma16(i32x4 srd, unsigned voff, unsigned soff, unsigned lds_addr) {
    unsigned keep;
    asm volatile("s_mov_b32 %0, m0\n\ts_mov_b32 m0, %4\n\ts_nop 0\n\tbuffer_load_dwordx4 %1, %2, %3 offen lds\n\ts_mov_b32 m0, %0"
                 : "=&s"(keep) : "v"(voff), "s"(srd), "s"(soff), "s"(lds_addr) : "memory");
}
DI void dma16_sc1(i32x4 srd, unsigned voff, unsigned soff, unsigned lds_addr) {
    unsigned keep;
    asm volatile("s_mov_b32 %0, m0\n\ts_mov_b32 m0, %4\n\ts_nop 0\n\tbuffer_load_dwordx4 %1, %2, %3 offen sc1 lds\n\ts_mov_b32 m0, %0"
                 : "=&s"(keep) : "v"(voff), "s"(srd), "s"(soff), "s"(lds_addr) : "memory");
}
DI void dma4(i32x4 srd, unsigned voff, unsigned lds_addr) {
    unsigned keep;
    asm volatile("s_mov_b32 %0, m0\n\ts_mov_b32 m0, %3\n\ts_nop 0\n\tbuffer_load_dword %1, %2, 0 offen lds\n\ts_mov_b32 m0, %0"
                 : "=&s"(keep) : "v"(voff), "s"(srd), "s"(lds_addr) : "memory");
}
#define WAIT_BAR(N) asm volatile("s_waitcnt vmcnt(" #N ") lgkmcnt(0)\n\ts_barrier" ::: "memory")
#define LBAR() asm volatile("s_waitcnt lgkmcnt(0)\n\ts_barrier" ::: "memory")
#define SB() __builtin_amdgcn_sched_barrier(0)
#define FENCE() asm volatile("" ::: "memory")
#define PIN4(a) asm volatile("" : "+v"((a)[0]), "+v"((a)[1]), "+v"((a)[2]), "+v"((a)[3]))
#define MFMA16(a, b, c) __builtin_amdgcn_mfma_f32_16x16x32_bf16(a, b, c, 0, 0, 0)
#define MFMA32(a, b, c) __builtin_amdgcn_mfma_f32_32x32x16_bf16(a, b, c, 0, 0, 0)
#define MFMA32K8(a, b, c) __builtin_amdgcn_mfma_f32_32x32x8bf16_1k(a, b, c, 0, 0, 0)
#define EXP2(x) __builtin_amdgcn_exp2f(x)
DI s16x4 lds_tr(LAS const char* p) { return __builtin_bit_cast(s16x4, __builtin_amdgcn_ds_read_tr16_b64_v4i16((LAS v4i16_t*)p)); }
DI bf16x8 lds_b128(LAS const char* p) { return *(LAS const bf16x8*)p; }
DI bf16x8 lds_afrag(LAS const char* p) { const s16x4 lo = lds_tr(p), hi = lds_tr(p + 2048); return __builtin_shufflevector(lo, hi, 0, 1, 2, 3, 4, 5, 6, 7); }
#define RLX_AGENT __ATOMIC_RELAXED, __HIP_MEMORY_SCOPE_AGENT
template <int X> DI float xor_lane(float v) { return __int_as_float(__builtin_amdgcn_ds_swizzle(__float_as_int(v), (X << 10) | 0x1f)); }
DI float half_sum(float v) { auto r = __builtin_amdgcn_permlane32_swap(__float_as_uint(v), __float_as_uint(v), false, false); return __uint_as_float(r[0]) + __uint_as_float(r[1]); }
DI float half_max(float v) { auto r = __builtin_amdgcn_permlane32_swap(__float_as_uint(v), __float_as_uint(v), false, false); return fmaxf(__uint_as_float(r[0]), __uint_as_float(r[1])); }
DI float half_lo(float v) { auto r = __builtin_amdgcn_permlane32_swap(__float_as_uint(v), __float_as_uint(v), false, false); return __uint_as_float(r[0]); }
template <int CTRL, int ROWS> DI float dpp_add(float v) { return v + __int_as_float(__builtin_amdgcn_update_dpp(0, __float_as_int(v), CTRL, ROWS, 0xf, true)); }
DI float wave_sum_dpp(float v) {
    v = dpp_add<0x111, 0xf>(v); v = dpp_add<0x112, 0xf>(v); v = dpp_add<0x114, 0xf>(v); v = dpp_add<0x118, 0xf>(v);
    v = dpp_add<0x142, 0xa>(v); v = dpp_add<0x143, 0xc>(v);
    return __int_as_float(__builtin_amdgcn_readlane(__float_as_int(v), 63));
}
DI float wave_sum(float v) { v += xor_lane<1>(v); v += xor_lane<2>(v); v += xor_lane<4>(v); v += xor_lane<8>(v); v += xor_lane<16>(v); return half_sum(v); }
DI float wave_max(float v) { v = fmaxf(v, xor_lane<1>(v)); v = fmaxf(v, xor_lane<2>(v)); v = fmaxf(v, xor_lane<4>(v)); v = fmaxf(v, xor_lane<8>(v)); v = fmaxf(v, xor_lane<16>(v)); return half_max(v); }
DI int fresh_lane() { int t; asm volatile("v_mbcnt_lo_u32_b32 %0, -1, 0\n\tv_mbcnt_hi_u32_b32 %0, -1, %0" : "=v"(t)); return t; }
DI void st_agent64(void* p, unsigned lo, unsigned hi) { __hip_atomic_store((GAS unsigned long long*)p, ((unsigned long long)hi << 32) | lo, RLX_AGENT); }
DI void wait_tags(const char* flags, unsigned off, bool use, unsigned tag) {
    for (unsigned spins = 0;; ++spins) {
        const unsigned v = use ? __hip_atomic_load((GAS unsigned*)(flags + off), RLX_AGENT) : tag;
        if (__all(v == tag)) break;
        if (spins > (1u << 19)) { __hip_atomic_store((GAS unsigned*)(flags + FL_TMO), tag, RLX_AGENT); break; }
        __builtin_amdgcn_s_sleep(1);
    }
    FENCE();
}
#define STAMP_DECL
#define STAMP(K, i)
#define STAMP_END(K)

#define DMA345 dma16_sc1
constexpr int ST1 = 49152;
constexpr int LDS_D0 = 0, LDS_D1 = 49152, LDS_H = 86016, LDS_Q1 = 36864, LDS_Q2 = 45056;
constexpr int LDS_PF = 147456;
constexpr int LDS_KM = 46080;
constexpr int LDS_D2 = 126976;
constexpr int LDS_KV0 = 125952;
constexpr int LDS_ZERO = 122880;
constexpr int LDS_CTL = LDS_ZERO + 640;
constexpr int LDS_MX = LDS_CTL + 64;
constexpr int LDS_RED = 2 * KVCH + 9 * 1024;
constexpr int LDS_OT = LDS_RED + 8 * 11 * 256;
constexpr int LDS_BYTES = 163840;
DI unsigned wo_lds(int p) { return p < 37 ? LDS_KV0 + p * 1024 : p < 74 ? (p - 37) * 1024 : p < 111 ? KVCH + (p - 74) * 1024 : 2 * KVCH + (p - 111) * 1024; }

template <int NM, bool FIRST, int ND, int DPG, int AD = 3, class Dma>
DI void gemm_step(f32x4 (&acc)[10][4], bf16x8 (&ca)[2], bf16x8 (&cb)[4], bf16x8 (&nb)[4], LAS const char* ap, LAS const char* bp, const Dma& dma) {
    bf16x8 a[10];
#pragma unroll
    for (int n = 0; n < 4; ++n) nb[n] = lds_b128(bp + n * 1024);
    a[0] = lds_afrag(ap); a[1] = lds_afrag(ap + 4096); if (AD > 2) a[2] = lds_afrag(ap + 2 * 4096);
    SB();
    int d = 0;
    if (!FIRST) {
#pragma unroll
        for (int k = 0; k < DPG; ++k) if (d < ND) dma(d++);
#pragma unroll
        for (int n = 0; n < 4; ++n) acc[NM - 2][n] = MFMA16(ca[0], cb[n], acc[NM - 2][n]);
        PIN4(acc[NM - 2]); SB();
#pragma unroll
        for (int k = 0; k < DPG; ++k) if (d < ND) dma(d++);
#pragma unroll
        for (int n = 0; n < 4; ++n) acc[NM - 1][n] = MFMA16(ca[1], cb[n], acc[NM - 1][n]);
        PIN4(acc[NM - 1]); SB();
    }
#pragma unroll
    for (int m = 0; m < NM - 2; ++m) {
        if (m + AD < NM) a[m + AD] = lds_afrag(ap + (m + AD) * 4096);
#pragma unroll
        for (int k = 0; k < DPG; ++k) if (d < ND) dma(d++);
#pragma unroll
        for (int n = 0; n < 4; ++n) acc[m][n] = MFMA16(a[m], nb[n], acc[m][n]);
        PIN4(acc[m]); SB();
    }
    ca[0] = a[NM - 2]; ca[1] = a[NM - 1];
}
template <int NM>
DI void gemm_flush(f32x4 (&acc)[10][4], const bf16x8 (&ca)[2], const bf16x8 (&cb)[4]) {
#pragma unroll
    for (int n = 0; n < 4; ++n) acc[NM - 2][n] = MFMA16(ca[0], cb[n], acc[NM - 2][n]);
#pragma unroll
    for (int n = 0; n < 4; ++n) acc[NM - 1][n] = MFMA16(ca[1], cb[n], acc[NM - 1][n]);
}

__global__ __launch_bounds__(512, 2) void fused_block(const u16* __restrict__ x, const u16* __restrict__ wq, const u16* __restrict__ wk, const u16* __restrict__ wv,
                                                       const u16* __restrict__ wo, const u16* __restrict__ wg, const u16* __restrict__ wu, const u16* __restrict__ wd,
                                                       char* __restrict__ ws, u16* __restrict__ out) {
    __shared__ __attribute__((aligned(1024))) char lds[LDS_BYTES];
    const int tid = threadIdx.x, l = tid & 63;
    const int w = __builtin_amdgcn_readfirstlane(tid >> 6);
    const int b = blockIdx.x, s = 2 * (b & 7) + ((b >> 3) & 1), r = b >> 4, wgi = r * 16 + s;
    const int tg = w >> 2, fg = w & 3;
    constexpr unsigned lds0 = 0;
    LAS char* L = (LAS char*)lds;
    char* flags = ws + WS_FLAGS;
    const unsigned tag = (((unsigned)amd_dispatch_id() + 1u) * 0x9E3779B1u) ^ ((unsigned)((unsigned long long)__builtin_amdgcn_dispatch_ptr() >> 6) * 0x85EBCA77u) ^ 0x5bd1e995u;

    const int dmat = w & 1, dR = (w >> 1) & 1, dcg0 = 5 * (w >> 2);
    const int di = l >> 2, dsig = l & 3;
    const int dmi = dsig ^ (2 * dmat + ((di >> 2) & 1)), dkrow = 8 * (di >> 2) + (di & 3);
    const i32x4 srd_w = make_srd(dmat ? wu : wg, DM * FF * 2);
    const i32x4 srd_q = make_srd(ws + WS_WQX, DM * 1024 * 2);
    const i32x4 srd_x = make_srd(ws + WS_XN, SEQ * DM * 2);
    const unsigned vo_w = dkrow * (FF * 2) + 16 * dmi, vo_q = dkrow * 2048 + 16 * dmi, vo_x = l * 16;
    const unsigned dlds = lds0 + dR * 2048 + dmat * 1024;
    auto pieceW = [&](int j, unsigned slot, int cg) { dma16(srd_w, vo_w, (32 * j + 4 * dR) * (FF * 2) + 288 * s * 2 + cg * 64, dlds + slot + cg * 4096); };
    auto pieceQ = [&](int j, unsigned slot) { dma16_sc1(srd_q, vo_q, (32 * j + 4 * dR) * 2048 + (64 * s + 32 * dmat) * 2, dlds + slot + 9 * 4096); };
    auto pieceX = [&](int j, unsigned slot, int n) { dma16_sc1(srd_x, vo_x, ((r * 18 + j) * 8 + n) * 1024, lds0 + slot + 40960 + n * 1024); };
    auto piece1k = [&](int j, unsigned slot, int i, int kind) {
        if (i < 3) { if (kind & 1) pieceW(j, slot, dcg0 + i); }
        else if (i == 3) { if (w == 3) { if (kind & 2) pieceQ(j, slot); } else if (kind & 1) pieceW(j, slot, dcg0 + 3); }
        else if (i == 4) {
            if (w < 3) { if (kind & 1) pieceW(j, slot, 4); } else if (w == 3) { if (kind & 2) pieceX(j, slot, 7); }
            else if (w < 7) { if (kind & 2) pieceQ(j, slot); } else if (kind & 1) pieceW(j, slot, 3);
        } else { if (w == 7) { if (kind & 1) pieceW(j, slot, 4); } else if (kind & 2) pieceX(j, slot, w); }
    };
    auto piece2 = [&](int j, unsigned slot, int i) {
        const int ii = (w >= 4 && i == 4) ? 3 : i;
        const unsigned vo_d = ((vo_w & ~63u) >> 3) + ((vo_w & 48u) << 1);
        dma16(make_srd(wd, FF * DM * 2), vo_d, (288 * s + 32 * j + 4 * dR) * (DM * 2) + 16 * dmat + (dcg0 + ii) * 128, dlds + slot + (dcg0 + ii) * 4096);
    };

    auto pull = [&](int idx) {
        const int lp = fresh_lane();
        const bool gu = idx < 5;
        const int sh = gu ? 3 : 4, rg = (gu ? 8 * idx : 4 * (idx - 5)) + (lp >> sh), seg = lp & (gu ? 7 : 15);
        const bool ok = gu ? (w >= 6 && seg < 5 && rg < 36) : (w == 7 && seg < 9 && rg < 18);
        const unsigned o = (gu ? (rg * 16 + r) * (FF * 2) + ((288 * s * 2) & ~127) : (288 * s + rg * 16 + r) * (DM * 2)) + seg * 128;
        dma4(gu ? srd_w : make_srd(wd, FF * DM * 2), ok ? o : 0xfffffff0u, lds0 + LDS_PF);
    };
    STAMP_DECL;
    STAMP(1, 0);
    if (w == 7) {
        unsigned long long pc; unsigned len;
        asm volatile("s_getpc_b64 %0\n\tcode_here_%=:\n\ts_mov_b32 %1, fused_block_code_end-code_here_%=" : "=s"(pc), "=s"(len));
        i32x4 sc; sc.x = (int)(unsigned)pc; sc.y = (int)((unsigned)(pc >> 32) & 0xffffu); sc.z = (int)len; sc.w = 0x00020000;
        dma4(sc, l < 20 ? (unsigned)(((b >> 3) * 20 + l) * 128) : 0xfffffff0u, lds0 + LDS_PF);
    }
    {
        const u32x4* xs = (const u32x4*)(x + (size_t)(128 * r + 8 * s + w) * DM);
        const u32x4 v0 = xs[l];
        u32x4 v1 = xs[64 + (l & 7)];
        unsigned qv[8];
        {
            const int tq = min(tid, 287), k = 36 * r + (tq >> 3), mat = (tq >> 2) & 1, t = tq & 3;
            const u16* wc = ((s & 1) ? wv : wk) + k * DH;
            const u16* wh = wq + (size_t)k * DM + (2 * s + (t >> 1)) * DH;
#pragma unroll
            for (int e = 0; e < 8; ++e) {
                const int rho = mat * 8 + e, cc = (t == 1 ? 0 : 12) + rho - 4;
                const bool isq = (t & 1) == 0 || rho < 2, isc = !isq && rho >= 4 && cc < (t == 1 ? 12 : 18);
                const u16* p = isc ? wc + cc : wh + ((t & 1) == 0 ? rho : 16 + (rho & 1));
                const unsigned val = *p;
                qv[e] = (isq || isc) ? val : 0u;
            }
        }
        const int cgp = s & 7, wp = 4 * (r * 2 + (s >> 3)) + ((min(max(tid, 192), 447) - 192) >> 6);
        const bool wo_act = tid >= 192 && tid < 448 && wp < 120;
        u32x4 wov;
        {
            const int m0 = cgp < 4 ? 5 * cgp : 20 + 4 * (cgp - 4), wpc = min(wp, 119);
            const int pp = wpc / 40, js = (wpc % 40) >> 1, R = wpc & 1, i = l >> 2, ch = (l & 3) ^ (2 * ((i >> 2) & 1));
            const int kp = 32 * js + 8 * (i >> 2) + (i & 3) + 4 * R, sp = kp / 40, d = kp - 40 * sp, hd = d >= 20 ? 1 : 0, dd = d - 20 * hd;
            const int row = 36 * sp + 18 * hd + (dd < 18 ? dd : 17), col = min(16 * (m0 + 2 * pp) + 8 * ch, DM - 8);
            wov = *(const u32x4*)(wo + (size_t)row * DM + col);
        }
        FENCE();
        STAMP(1, 13);
        if (l >= 8) v1 = (u32x4){0u, 0u, 0u, 0u};
        float sm = 0.f;
#pragma unroll
        for (int e = 0; e < 4; ++e) sm += bflo(v0[e]) + bfhi(v0[e]) + bflo(v1[e]) + bfhi(v1[e]);
        const float mean = wave_sum_dpp(sm) * (1.0f / DM);
        float q = 0.f, q1 = 0.f;
#pragma unroll
        for (int e = 0; e < 4; ++e) { const float a = bflo(v0[e]) - mean, c = bfhi(v0[e]) - mean; q += a * a + c * c; }
#pragma unroll
        for (int e = 0; e < 4; ++e) { const float a = bflo(v1[e]) - mean, c = bfhi(v1[e]) - mean; q1 += a * a + c * c; }
        if (l < 8) q += q1;
        const float m = rbf(mean), var = rbf(wave_sum_dpp(q) * (1.0f / DM));
        const float rs = 1.0f / rbf(sqrtf(rbf(var + 1e-5f)));
        LAS char* xst = L + 2 * ST1;
        {
            u32x4 o;
#pragma unroll
            for (int e = 0; e < 4; ++e) o[e] = cvtpk(rbf(bflo(v0[e]) - m) * rs, rbf(bfhi(v0[e]) - m) * rs);
            *(LAS u32x4*)(xst + l * 128 + w * 16) = o;
#pragma unroll
            for (int e = 0; e < 4; ++e) o[e] = cvtpk(rbf(bflo(v1[e]) - m) * rs, rbf(bfhi(v1[e]) - m) * rs);
            if (l < 8) *(LAS u32x4*)(xst + (64 + l) * 128 + w * 16) = o;
        }
        asm volatile("" : "+v"(wov));
        LBAR();
        STAMP(1, 14);
        if (w < 7) {
            const __amdgpu_buffer_rsrc_t rx = __builtin_amdgcn_make_buffer_rsrc(ws + WS_XN, 0, SEQ * DM * 2, 0x00020000);
#pragma unroll
            for (int u = 0; u < 2; ++u) {
                const int i = tid + 448 * u, c = i >> 3, t = i & 7;
                const u32x4 v = *(LAS const u32x4*)(xst + min(i, 575) * 16);
                __builtin_amdgcn_raw_buffer_store_b128(v, rx, i < 576 ? (unsigned)(((r * 18 + (c >> 2)) * 8 + (s >> 1)) * 1024 + ((c & 3) * 16 + 8 * (s & 1) + t) * 16) : 0xfffffff0u, 0, 16);
            }
            {
                u32x4 o; o[0] = qv[0] | (qv[1] << 16); o[1] = qv[2] | (qv[3] << 16); o[2] = qv[4] | (qv[5] << 16); o[3] = qv[6] | (qv[7] << 16);
                const __amdgpu_buffer_rsrc_t rq = __builtin_amdgcn_make_buffer_rsrc(ws + WS_WQX, 0, DM * 1024 * 2, 0x00020000);
                __builtin_amdgcn_raw_buffer_store_b128(o, rq, tid < 288 ? (unsigned)(((36 * r + (tid >> 3)) * 128 + s * 8 + (tid & 7)) * 16) : 0xfffffff0u, 0, 16);
            }
            WAIT_BAR(0);
        } else LBAR();
        STAMP(1, 15);
        if (tid == 0) __hip_atomic_store((GAS unsigned*)(flags + FL_PRO + wgi * 4), tag, RLX_AGENT);
#pragma unroll
        for (int i = 0; i < 6; ++i) piece1k(0, 0, i, 1);
#pragma unroll
        for (int i = 0; i < 6; ++i) piece1k(1, ST1, i, 1);
        if (w == 7) {
#pragma unroll
            for (int g = 0; g < 2; ++g) {
                const int rg = 8 * g + (l >> 3);
                const unsigned o = (l & 7) < 5 ? (unsigned)((rg * 16 + r) * (FF * 2) + ((288 * s * 2) & ~127) + (l & 7) * 128) : 0xfffffff0u;
                dma4(make_srd(wg, DM * FF * 2), o, lds0 + LDS_PF); dma4(srd_w, o, lds0 + LDS_PF);
            }
        }
        if (w < 7) {
            const __amdgpu_buffer_rsrc_t rw = __builtin_amdgcn_make_buffer_rsrc(ws + WS_WO, 0, 8 * WOIMG, 0x00020000);
            __builtin_amdgcn_raw_buffer_store_b128(wov, rw, wo_act ? (unsigned)(cgp * WOIMG + wp * 1024 + l * 16) : 0xfffffff0u, 0, 16);
            const __amdgpu_buffer_rsrc_t ro = __builtin_amdgcn_make_buffer_rsrc(ws + WS_O + (size_t)wgi * 128 * 80, 0, 128 * 80, 0x00020000);
            const u32x4 e = (u32x4){O_EMPTY, O_EMPTY, O_EMPTY, O_EMPTY};
            __builtin_amdgcn_raw_buffer_store_b128(e, ro, tid * 16, 0, 16);
            __builtin_amdgcn_raw_buffer_store_b128(e, ro, (tid + 448) * 16, 0, 16);
        }
        STAMP(1, 12);
        if (w == 0) wait_tags(flags, FL_PRO + (l < 16 ? (r * 16 + l) * 4 : ((l - 16) * 16 + s) * 4), l < 32, tag);
        asm volatile("s_barrier" ::: "memory");
#pragma unroll
        for (int i = 3; i < 6; ++i) piece1k(0, 0, i, 2);
#pragma unroll
        for (int i = 3; i < 6; ++i) piece1k(1, ST1, i, 2);
    }
    const int lg = fresh_lane();
    const unsigned a_off = ((lg & 3) >> 1) * 1024 + 16 * (4 * (4 * (lg >> 4) + ((lg >> 2) & 3)) + (fg ^ (2 * ((lg & 3) >> 1) + ((lg >> 4) & 1)))) + 8 * (lg & 1);

    f32x4 acc[10][4];
#pragma unroll
    for (int m = 0; m < 10; ++m)
#pragma unroll
        for (int n = 0; n < 4; ++n) acc[m][n] = (f32x4){0.f, 0.f, 0.f, 0.f};
    bf16x8 ca[2], bA[4], bB[4];
    {
        const unsigned bo = 40960 + tg * 4096 + lg * 16;
        WAIT_BAR(0);
        STAMP(1, 1);
        const bool q3 = w == 3, w4 = w < 3 || w == 7, x4 = w == 3, w5 = w == 7;
        const i32x4 srd3 = q3 ? srd_q : srd_w, srd4 = w4 ? srd_w : x4 ? srd_x : srd_q, srd5 = w5 ? srd_w : srd_x;
        const unsigned vo3 = q3 ? vo_q : vo_w, vo4 = w4 ? vo_w : x4 ? vo_x : vo_q, vo5 = w5 ? vo_w : vo_x;
        const unsigned soW = 4 * dR * (FF * 2) + 288 * s * 2, soQ = 4 * dR * 2048 + (64 * s + 32 * dmat) * 2, soX = r * 18 * 8192;
        const unsigned stW = 32 * FF * 2, stQ = 32 * 2048, stX = 8192;
        const unsigned so3 = q3 ? soQ : soW + (dcg0 + 3) * 64, st3 = q3 ? stQ : stW, lo3 = q3 ? dlds + 9 * 4096 : dlds + (dcg0 + 3) * 4096;
        const int cg4 = w < 3 ? 4 : 3;
        const unsigned so4 = w4 ? soW + cg4 * 64 : x4 ? soX + 7 * 1024 : soQ, st4 = w4 ? stW : x4 ? stX : stQ, lo4 = w4 ? dlds + cg4 * 4096 : x4 ? lds0 + 40960 + 7 * 1024 : dlds + 9 * 4096;
        const unsigned so5 = w5 ? soW + 4 * 64 : soX + w * 1024, st5 = w5 ? stW : stX, lo5 = w5 ? dlds + 4 * 4096 : lds0 + 40960 + w * 1024;
        auto stage1 = [&](int j, unsigned slot, int i) {
            if (i < 3) dma16(srd_w, vo_w, soW + (dcg0 + i) * 64 + j * stW, dlds + (dcg0 + i) * 4096 + slot);
            else if (i == 3) DMA345(srd3, vo3, so3 + j * st3, lo3 + slot);
            else if (i == 4) DMA345(srd4, vo4, so4 + j * st4, lo4 + slot);
            else if (i == 5) DMA345(srd5, vo5, so5 + j * st5, lo5 + slot);
            else if (w >= 6) { const int t = j - 2; pull(t < 6 ? ((t & 1) ? 10 : 2 + (t >> 1)) : t - 1); }
        };
        gemm_step<10, true, 7, 1>(acc, ca, bB, bA, L + a_off, L + bo, [&](int i) { stage1(2, 2 * ST1, i); });
        unsigned slot = ST1, slot2 = 0;
        if (w >= 6) { WAIT_BAR(7); } else { WAIT_BAR(6); }
        gemm_step<10, false, 7, 1>(acc, ca, bA, bB, L + slot + a_off, L + slot + bo, [&](int i) { stage1(3, slot2, i); });
        slot = 2 * ST1; slot2 = ST1;
#pragma unroll 1
        for (int j = 2; j < 16; j += 2) {
            if (w >= 6) { WAIT_BAR(8); } else { WAIT_BAR(6); }
            gemm_step<10, false, 7, 1>(acc, ca, bB, bA, L + slot + a_off, L + slot + bo, [&](int i) { stage1(j + 2, slot2, i); });
            slot = (slot == 2 * ST1) ? 0 : slot + ST1; slot2 = (slot2 == 2 * ST1) ? 0 : slot2 + ST1;
            if (w >= 6) { WAIT_BAR(8); } else { WAIT_BAR(6); }
            gemm_step<10, false, 7, 1>(acc, ca, bA, bB, L + slot + a_off, L + slot + bo, [&](int i) { stage1(j + 3, slot2, i); });
            slot = (slot == 2 * ST1) ? 0 : slot + ST1; slot2 = (slot2 == 2 * ST1) ? 0 : slot2 + ST1;
        }
#pragma unroll
        for (int n = 0; n < 4; ++n) bA[n] = bB[n];
#pragma unroll 1
        for (int j = 16; j < 18; ++j) {
            if (j == 16 && w >= 6) { WAIT_BAR(8); } else if (j == 16) { WAIT_BAR(6); } else { WAIT_BAR(5); }
            gemm_step<10, false, 5, 1>(acc, ca, bA, bB, L + slot + a_off, L + slot + bo, [&](int i) { piece2(j - 16, j == 16 ? LDS_D0 : LDS_D1, i); });
#pragma unroll
            for (int n = 0; n < 4; ++n) bA[n] = bB[n];
            slot += ST1;
        }
        gemm_flush<10>(acc, ca, bA);
        SB(); FENCE();
    }
    if ((fg & 1) && tg == (s >> 3)) {
        const int l = fresh_lane(), tok = l & 15, g4 = l >> 4;
        const int e = s >> 1;
        f32x4 cv = acc[9][0];
        if ((e & 3) == 1) cv = acc[9][1]; else if ((e & 3) == 2) cv = acc[9][2]; else if ((e & 3) == 3) cv = acc[9][3];
        const int c0 = (fg == 1 ? 0 : 12) + 4 * (g4 - 1);
        const bool act = g4 >= 1 && c0 < 18;
        const unsigned p0 = cvtpk(cv[0], cv[1]), p1 = cvtpk(cv[2], cv[3]);
        const int kt = 4 * r + (e >> 1);
        char* kvb = ws + WS_KV + ((kt >> 3) & 3) * KVCH; const int lt = 8 * (kt >> 5) + (kt & 7);
        if ((s & 1) == 0) {
            const int key = 16 * (e & 1) + tok;
            if (act) {
                if (c0 < 16) st_agent64(kvb + lt * 1024 + ((c0 >> 3) * 32 + key) * 16 + (c0 & 4) * 2, p0, p1);
                else __hip_atomic_store((GAS unsigned*)(kvb + KV_K2 + lt * 128 + key * 4), p0, RLX_AGENT);
            }
            float n2 = 0.f;
            if (act) { n2 = bflo(p0) * bflo(p0) + bfhi(p0) * bfhi(p0); if (c0 < 16) n2 += bflo(p1) * bflo(p1) + bfhi(p1) * bfhi(p1); }
            n2 += xor_lane<16>(n2); n2 = half_sum(n2);
            n2 = wave_max(n2);
            if (l == 0) *(LAS float*)(L + LDS_KM + (fg == 1 ? 0 : 4)) = n2;
        } else {
            if (l == 0) *(LAS float*)(L + LDS_KM + (fg == 1 ? 0 : 4)) = 0.f;
            if (fg == 1 && l < 2) {
                char* op = kvb + KV_V + lt * 1216 + (e & 1) * 608 + l * 304 + 18 * 16;
                st_agent64(op, 0x3f803f80u, 0x3f803f80u); st_agent64(op + 8, 0x3f803f80u, 0x3f803f80u);
            }
            const int hv = (tok >> 2) & 1, jj = 4 * (tok >> 3) + (tok & 3);
            GAS u16* vb = (GAS u16*)(kvb + KV_V + lt * 1216 + (e & 1) * 608 + hv * 304 + jj * 2);
            if (act) {
                __hip_atomic_store(vb + (c0 + 0) * 8, (u16)(p0 & 0xffff), RLX_AGENT);
                __hip_atomic_store(vb + (c0 + 1) * 8, (u16)(p0 >> 16), RLX_AGENT);
                if (c0 < 16) { __hip_atomic_store(vb + (c0 + 2) * 8, (u16)(p1 & 0xffff), RLX_AGENT);
                               __hip_atomic_store(vb + (c0 + 3) * 8, (u16)(p1 >> 16), RLX_AGENT); }
            }
        }
    }
    LBAR();
    STAMP(1, 2);
    {
        const int l = fresh_lane(), tok = l & 15, g4 = l >> 4;
#pragma unroll
        for (int cg = 0; cg < 9; ++cg)
#pragma unroll
            for (int pr = 0; pr < 2; ++pr) {
                float h[4];
#pragma unroll
                for (int e = 0; e < 4; ++e) {
                    auto sw = __builtin_amdgcn_permlane32_swap(__float_as_uint(acc[cg][2 * pr][e]), __float_as_uint(acc[cg][2 * pr + 1][e]), false, false);
                    h[e] = fmaxf(__uint_as_float(sw[0]), 0.f) * __uint_as_float(sw[1]);
                }
                const int n = 4 * tg + 2 * pr + (l >> 5);
                u32x2 o; o[0] = cvtpk(h[0], h[1]); o[1] = cvtpk(h[2], h[3]);
                *(LAS u32x2*)(L + LDS_H + ((8 - cg) * 8 + n) * 1024 + (fg * 16 + tok) * 16 + 8 * (g4 & 1)) = o;
            }
        const float qs = 0.23570226039551584f * 1.4426950408889634f;
        const int hh = fg >> 1;
#pragma unroll
        for (int n = 0; n < 4; ++n) {
            const int ct = 2 * tg + (n >> 1), query = 16 * (n & 1) + tok;
            const unsigned p0 = cvtpk(acc[9][n][0] * qs, acc[9][n][1] * qs), p1 = cvtpk(acc[9][n][2] * qs, acc[9][n][3] * qs);
            if ((fg & 1) == 0) *(LAS u32x2*)(L + LDS_Q1 + (hh * 4 + ct) * 1024 + ((g4 >> 1) * 32 + query) * 16 + (g4 & 1) * 8) = (u32x2){p0, p1};
            else if (g4 == 0) *(LAS unsigned*)(L + LDS_Q2 + (hh * 4 + ct) * 128 + query * 4) = p0;
        }
    }
    WAIT_BAR(0);
    if (w == 0 && fresh_lane() == 0) {
        st_agent64(flags + FL_KV + wgi * 16, tag, *(LAS const unsigned*)(L + LDS_KM));
        st_agent64(flags + FL_KV + wgi * 16 + 8, tag, *(LAS const unsigned*)(L + LDS_KM + 4));
    }
    STAMP(1, 3);
    const i32x4 srd_kv = make_srd(ws + WS_KV, 4 * KVCH);
    auto piece_kv = [&](int c, int i) {
        const int p = min(w + 8 * i, 36);
        dma16_sc1(srd_kv, vo_x, c * KVCH + p * 1024, lds0 + (c == 0 ? LDS_KV0 : (c - 1) * KVCH) + p * 1024);
    };
#pragma unroll
    for (int m = 0; m < 9; ++m)
#pragma unroll
        for (int n = 0; n < 4; ++n) acc[m][n] = (f32x4){0.f, 0.f, 0.f, 0.f};
    {
        LAS const char* hb = L + LDS_H + 8 * 8192 + 4 * tg * 1024 + l * 16;
        gemm_step<9, true, 0, 1>(acc, ca, bB, bA, L + LDS_D0 + a_off, hb, [](int) {});
        LBAR();
        gemm_step<9, false, 5, 2>(acc, ca, bA, bB, L + LDS_D1 + a_off, hb - 1 * 8192, [&](int i) { piece2(2, LDS_D0, i); });
        WAIT_BAR(0);
        gemm_step<9, false, 5, 2>(acc, ca, bB, bA, L + LDS_D0 + a_off, hb - 2 * 8192, [&](int i) { piece2(3, LDS_D1, i); });
        WAIT_BAR(0);
        gemm_step<9, false, 5, 2>(acc, ca, bA, bB, L + LDS_D1 + a_off, hb - 3 * 8192, [&](int i) { piece2(4, LDS_D0, i); });
        WAIT_BAR(0);
        gemm_step<9, false, 10, 2>(acc, ca, bB, bA, L + LDS_D0 + a_off, hb - 4 * 8192, [&](int i) { if (i < 5) piece2(5, LDS_D1, i); else piece2(6, LDS_D2, i - 5); });
        WAIT_BAR(5);
        gemm_step<9, false, 5, 1>(acc, ca, bA, bB, L + LDS_D1 + a_off, hb - 5 * 8192, [&](int i) { piece2(7, LDS_D0, i); });
        WAIT_BAR(5);
        gemm_step<9, false, 5, 1>(acc, ca, bB, bA, L + LDS_D2 + a_off, hb - 6 * 8192, [&](int i) { piece2(8, LDS_D1, i); });
        if (w == 0) {
            const int lk = fresh_lane();
            const __amdgpu_buffer_rsrc_t rk = __builtin_amdgcn_make_buffer_rsrc(flags + FL_KV, 0, 4096, 0x00020000);
            float ma, mb;
            for (unsigned spins = 0;; ++spins) {
                bool ok = true; ma = 0.f; mb = 0.f;
#pragma unroll
                for (int i = 0; i < 4; ++i) {
                    const u32x4 v = __builtin_amdgcn_raw_buffer_load_b128(rk, (i * 64 + lk) * 16, 0, 16);
                    ok = ok && v[0] == tag && v[2] == tag; ma = fmaxf(ma, __uint_as_float(v[1])); mb = fmaxf(mb, __uint_as_float(v[3]));
                }
                if (__all(ok)) break;
                if (spins > (1u << 19)) { __hip_atomic_store((GAS unsigned*)(flags + FL_TMO), tag, RLX_AGENT); break; }
                __builtin_amdgcn_s_sleep(1);
            }
            ma = wave_max(ma); mb = wave_max(mb);
            if (lk == 0) *(LAS float*)(L + LDS_KM + 8) = ma + mb;
        }
        WAIT_BAR(5);
        gemm_step<9, false, 5, 1>(acc, ca, bA, bB, L + LDS_D0 + a_off, hb - 7 * 8192, [&](int i) { piece_kv(0, i); });
        WAIT_BAR(5);
        gemm_step<9, false, 0, 1>(acc, ca, bB, bA, L + LDS_D1 + a_off, hb - 8 * 8192, [](int) {});
        gemm_flush<9>(acc, ca, bA);
        SB(); FENCE();
    }
    STAMP(1, 4);
    const int la = fresh_lane();
#define l la
    const int hh = w >> 2, kh = (w >> 1) & 1, cp = w & 1;
#define h5 (l >> 5)
    bf16x8 q1[2]; u32x2 q2[2];
#pragma unroll
    for (int c = 0; c < 2; ++c) {
        q1[c] = lds_b128(L + LDS_Q1 + (hh * 4 + 2 * cp + c) * 1024 + l * 16);
        q2[c][0] = *(LAS const unsigned*)(L + LDS_Q2 + (hh * 4 + 2 * cp + c) * 128 + (l & 31) * 4);
    }
    u32x4 pk[18];
#pragma unroll
    for (int m = 0; m < 9; ++m)
#pragma unroll
        for (int pr = 0; pr < 2; ++pr) {
            u32x4 o; o[0] = cvtpk(acc[m][2 * pr][0], acc[m][2 * pr][1]); o[1] = cvtpk(acc[m][2 * pr][2], acc[m][2 * pr][3]);
            o[2] = cvtpk(acc[m][2 * pr + 1][0], acc[m][2 * pr + 1][1]); o[3] = cvtpk(acc[m][2 * pr + 1][2], acc[m][2 * pr + 1][3]);
            pk[m * 2 + pr] = o;
        }
    {
        const float kmax2 = *(LAS const float*)(L + LDS_KM + 8);
#pragma unroll
        for (int c = 0; c < 2; ++c) {
            const u32x4 a = __builtin_bit_cast(u32x4, q1[c]); const unsigned t = h5 ? 0u : q2[c][0];
            float n2 = bflo(t) * bflo(t) + bfhi(t) * bfhi(t);
#pragma unroll
            for (int e = 0; e < 4; ++e) n2 += bflo(a[e]) * bflo(a[e]) + bfhi(a[e]) * bfhi(a[e]);
            n2 = half_sum(n2);
            const float cb = sqrtf(n2 * kmax2) * 1.001f + 0.01f;
            const float chi = rbf(cb), clo = cb - chi;
            q2[c][0] = t; q2[c][1] = h5 ? 0u : cvtpk(-chi, -clo);
        }
    }
    if (w == 0 && l < 40) *(LAS u32x4*)(L + LDS_ZERO + l * 16) = (u32x4){0u, 0u, 0u, 0u};
    if (w == 0 && l == 40) *(LAS unsigned*)(L + LDS_CTL) = 0u;
    WAIT_BAR(0);
    STAMP(1, 5);
#undef l
    const int lb = fresh_lane();
#define l lb
    const unsigned kone = h5 ? 0u : 0x3f803f80u;
    const bool vrow = (l & 31) < 19;
    const unsigned k1_base = 8 * kh * 1024 + l * 16, k2_base = KV_K2 + 8 * kh * 128 + (l & 31) * 4;
    const unsigned v_base = vrow ? KV_V + 8 * kh * 1216 + h5 * 304 + (l & 31) * 16 : LDS_ZERO, v_mul = vrow ? 1u : 0u;
    const int tgf = s >> 3, cgp = s & 7, nmt = cgp < 4 ? 5 : 4, m0 = cgp < 4 ? 5 * cgp : 20 + 4 * (cgp - 4);
    const int pr = w & 1, qk = w >> 1;
    const i32x4 srd_wo = make_srd(ws + WS_WO + (size_t)cgp * WOIMG, WOIMG);
    auto piece_wo = [&](int p) { dma16(srd_wo, vo_x, p * 1024, lds0 + wo_lds(p)); };
    const __amdgpu_buffer_rsrc_t rpart = __builtin_amdgcn_make_buffer_rsrc(ws + WS_PART + (size_t)r * 16 * 8 * 18 * 1024, 0, 16 * 8 * 18 * 1024, 0x00020000);
    float own[11];
    u32x4 pl[5][4];

    f32x16 o0, o1, sX, sY; bf16x8 kA, kB, v0A, v1A, v0B, v1B; unsigned k2A, k2B; u32x4 p0, p1; float e3[4];
    unsigned aK, aK2, aV;
#define SLOT(SC, SN, KN, K2N, CN, OP, V1P, OC, V0C) do { float ea[4], eb[4]; \
        SN = MFMA32(KN, q1[CN], (f32x16){}); SB(); \
        ea[0] = EXP2(SC[0]); ea[1] = EXP2(SC[1]); ea[2] = EXP2(SC[2]); ea[3] = EXP2(SC[3]); \
        p1[2] = cvtpk(e3[0], e3[1]); p1[3] = cvtpk(e3[2], e3[3]); SB(); \
        OP = MFMA32(V1P, __builtin_bit_cast(bf16x8, p1), OP); SB(); \
        eb[0] = EXP2(SC[4]); eb[1] = EXP2(SC[5]); eb[2] = EXP2(SC[6]); eb[3] = EXP2(SC[7]); \
        p0[0] = cvtpk(ea[0], ea[1]); p0[1] = cvtpk(ea[2], ea[3]); SB(); \
        SN = MFMA32K8(__builtin_bit_cast(s16x4, ((u32x2){K2N, kone})), __builtin_bit_cast(s16x4, q2[CN]), SN); SB(); \
        ea[0] = EXP2(SC[8]); ea[1] = EXP2(SC[9]); ea[2] = EXP2(SC[10]); ea[3] = EXP2(SC[11]); \
        p0[2] = cvtpk(eb[0], eb[1]); p0[3] = cvtpk(eb[2], eb[3]); SB(); \
        OC = MFMA32(V0C, __builtin_bit_cast(bf16x8, p0), OC); SB(); \
        e3[0] = EXP2(SC[12]); e3[1] = EXP2(SC[13]); e3[2] = EXP2(SC[14]); e3[3] = EXP2(SC[15]); \
        p1[0] = cvtpk(ea[0], ea[1]); p1[1] = cvtpk(ea[2], ea[3]); SB(); } while (0)
#define PAIR(DK, DK2, DV, H0, H1, H2, H3) do { \
        kB = lds_b128(L + aK + 1024); k2B = *(LAS const unsigned*)(L + aK2 + 128); v0B = lds_b128(L + aV + v_mul * 1216); SB(); \
        H0; SLOT(sX, sY, kA, k2A, 1, o1, v1B, o0, v0A); \
        v1B = lds_b128(L + aV + v_mul * 1216 + 608); SB(); \
        H1; SLOT(sY, sX, kB, k2B, 0, o0, v1A, o1, v0A); \
        aK += (DK); aK2 += (DK2); aV += v_mul * (DV); \
        kA = lds_b128(L + aK); k2A = *(LAS const unsigned*)(L + aK2); v0A = lds_b128(L + aV); SB(); \
        H2; SLOT(sX, sY, kB, k2B, 1, o1, v1A, o0, v0B); \
        v1A = lds_b128(L + aV + 608); SB(); \
        H3; SLOT(sY, sX, kA, k2A, 0, o0, v1B, o1, v0B); } while (0)
#define PAIR_IN(H0, H1, H2, H3) PAIR(2048, 256, 2432, H0, H1, H2, H3)
#define PAIR_JUMP(J, H0, H1, H2, H3) PAIR((J) - 6 * 1024, (J) - 6 * 128, (J) - 6 * 1216, H0, H1, H2, H3)
#define NOP_ ((void)0)
#define ATT_INIT() do { o0 = (f32x16){}; o1 = (f32x16){}; aK = k1_base + LDS_KV0; aK2 = k2_base + LDS_KV0; aV = v_base + v_mul * LDS_KV0; \
        kA = lds_b128(L + aK); k2A = *(LAS const unsigned*)(L + aK2); v0A = lds_b128(L + aV); v1A = lds_b128(L + aV + 608); v1B = v1A; \
        sX = MFMA32(kA, q1[0], (f32x16){}); sX = MFMA32K8(__builtin_bit_cast(s16x4, ((u32x2){k2A, kone})), __builtin_bit_cast(s16x4, q2[0]), sX); \
        p1 = (u32x4){0u, 0u, 0u, 0u}; e3[0] = e3[1] = e3[2] = e3[3] = 0.f; } while (0)
#define ATT_FIN() do { p1[2] = cvtpk(e3[0], e3[1]); p1[3] = cvtpk(e3[2], e3[3]); o1 = MFMA32(v1B, __builtin_bit_cast(bf16x8, p1), o1); } while (0)
    constexpr int J01 = 0 - LDS_KV0, J12 = KVCH;
    {
        const __amdgpu_buffer_rsrc_t rp = __builtin_amdgcn_make_buffer_rsrc(ws + WS_PART + (size_t)(r * 16 + s) * 8 * 18 * 1024, 0, 8 * 18 * 1024, 0x00020000);
#define KV(C, I) do { piece_kv(C, I); SB(); } while (0)
#define ST(I) do { __builtin_amdgcn_raw_buffer_store_b128(pk[I], rp, vo_x, (w * 18 + (I)) * 1024, 16); SB(); } while (0)
#define WO(P0, I) do { piece_wo(min((P0) + w + 8 * (I), (P0) + 36)); SB(); } while (0)
#define PL(T, SP) do { const int m_ = m0 + ((T) < nmt ? (T) : 0); \
            pl[T][SP] = __builtin_amdgcn_raw_buffer_load_b128(rpart, vo_x, ((((4 * qk + (SP)) * 8 + tgf * 4 + (m_ & 3)) * 9 + (m_ >> 2)) * 2 + pr) * 1024, 16); SB(); } while (0)
#define PL2(A) do { PL((A) >> 2, (A) & 3); PL(((A) + 1) >> 2, ((A) + 1) & 3); } while (0)
        ATT_INIT();
        PAIR_IN(KV(1, 0), KV(1, 1), KV(1, 2), KV(1, 3));
        PAIR_IN(KV(1, 4), ST(0), KV(2, 0), ST(1));
        PAIR_IN(KV(2, 1), ST(2), KV(2, 2), ST(3));
        WAIT_BAR(7);
        PAIR_JUMP(J01, KV(2, 3), ST(4), KV(2, 4), ST(5));
        PAIR_IN(KV(3, 0), ST(6), KV(3, 1), ST(7));
        PAIR_IN(KV(3, 2), ST(8), KV(3, 3), ST(9));
        PAIR_IN(KV(3, 4), ST(10), NOP_, ST(11));
        WAIT_BAR(12);
        PAIR_JUMP(J12, WO(0, 0), ST(12), WO(0, 1), ST(13));
        PAIR_IN(WO(0, 2), ST(14), WO(0, 3), ST(15));
        PAIR_IN(WO(0, 4), ST(16), NOP_, ST(17));
        PAIR_IN(NOP_, NOP_, NOP_, NOP_);
        WAIT_BAR(0);
        if (w == 0 && fresh_lane() == 0) __hip_atomic_store((GAS unsigned*)(flags + FL_P + wgi * 4), tag, RLX_AGENT);
        PAIR_JUMP(J12, WO(37, 0), WO(37, 1), WO(37, 2), WO(37, 3));
        const unsigned pflag = __hip_atomic_load((GAS unsigned*)(flags + FL_P + (r * 16 + (l & 15)) * 4), RLX_AGENT);
        PAIR_IN(WO(37, 4), NOP_, NOP_, NOP_);
        LBAR();
        if (!__all(pflag == tag)) wait_tags(flags, FL_P + (r * 16 + (l & 15)) * 4, true, tag);
#define WP(I, A) do { WO(74, I); PL((A) >> 2, (A) & 3); } while (0)
        PAIR_IN(WP(0, 0), WP(1, 1), WP(2, 2), WP(3, 3));
        PAIR_IN(WP(4, 4), PL2(5), PL2(7), PL2(9));
        PAIR(0, 0, 0, PL2(11), PL2(13), PL2(15), PL2(17));
        PL(4, 3);
        ATT_FIN();
    }
    STAMP(1, 6);
#undef l
    const int lc = fresh_lane();
#define l lc
    for (int attempt = 0;; ++attempt) {
        if (attempt == 1) {
            const unsigned kone = h5 ? 0u : 0x3f803f80u;
            const bool vrow = (l & 31) < 19;
            const unsigned k1_base = 8 * kh * 1024 + l * 16, k2_base = KV_K2 + 8 * kh * 128 + (l & 31) * 4;
            const unsigned v_base = vrow ? KV_V + 8 * kh * 1216 + h5 * 304 + (l & 31) * 16 : LDS_ZERO, v_mul = vrow ? 1u : 0u;
            WAIT_BAR(0);
#pragma unroll
            for (int c = 0; c < 4; ++c)
#pragma unroll
                for (int i = 0; i < 5; ++i) piece_kv(c, i);
            if (w == 0 && l < 40) *(LAS u32x4*)(L + LDS_ZERO + l * 16) = (u32x4){0u, 0u, 0u, 0u};
            WAIT_BAR(0);
            float m[2] = {-INFINITY, -INFINITY};
            for (int i = 0; i < 32; ++i) {
                const unsigned to = (i >> 3) == 0 ? LDS_KV0 : ((i >> 3) - 1) * KVCH;
                const bf16x8 k1 = lds_b128(L + k1_base + to + (i & 7) * 1024);
                const u32x2 k2 = {*(LAS const unsigned*)(L + k2_base + to + (i & 7) * 128), kone};
#pragma unroll
                for (int c = 0; c < 2; ++c) {
                    const u32x2 qz = {q2[c][0], 0u};
                    f32x16 sc = MFMA32(k1, q1[c], (f32x16){});
                    sc = MFMA32K8(__builtin_bit_cast(s16x4, k2), __builtin_bit_cast(s16x4, qz), sc);
#pragma unroll
                    for (int e = 0; e < 16; ++e) m[c] = fmaxf(m[c], sc[e]);
                }
            }
            LAS float* mx = (LAS float*)(L + LDS_MX);
#pragma unroll
            for (int c = 0; c < 2; ++c) { m[c] = half_max(m[c]); if (l < 32) mx[(w * 2 + c) * 32 + l] = m[c]; }
            LBAR();
#pragma unroll
            for (int c = 0; c < 2; ++c) {
                const float mm = fmaxf(m[c], mx[((w ^ 2) * 2 + c) * 32 + (l & 31)]);
                const float chi = rbf(mm), clo = mm - chi;
                q2[c][1] = h5 ? 0u : cvtpk(-chi, -clo);
            }
            o0 = (f32x16){}; o1 = (f32x16){};
            for (int i = 0; i < 32; ++i) {
                const unsigned to = (i >> 3) == 0 ? LDS_KV0 : ((i >> 3) - 1) * KVCH;
                const bf16x8 k1 = lds_b128(L + k1_base + to + (i & 7) * 1024);
                const u32x2 k2 = {*(LAS const unsigned*)(L + k2_base + to + (i & 7) * 128), kone};
                const unsigned va = v_base + v_mul * (to + (i & 7) * 1216);
                const bf16x8 v0 = lds_b128(L + va), v1 = lds_b128(L + va + 608);
#pragma unroll
                for (int c = 0; c < 2; ++c) {
                    f32x16 sc = MFMA32(k1, q1[c], (f32x16){});
                    sc = MFMA32K8(__builtin_bit_cast(s16x4, k2), __builtin_bit_cast(s16x4, q2[c]), sc);
                    u32x4 pa, pb;
#pragma unroll
                    for (int e = 0; e < 4; ++e) { pa[e] = cvtpk(EXP2(sc[2 * e]), EXP2(sc[2 * e + 1])); pb[e] = cvtpk(EXP2(sc[8 + 2 * e]), EXP2(sc[9 + 2 * e])); }
                    if (c == 0) { o0 = MFMA32(v0, __builtin_bit_cast(bf16x8, pa), o0); o0 = MFMA32(v1, __builtin_bit_cast(bf16x8, pb), o0); }
                    else { o1 = MFMA32(v0, __builtin_bit_cast(bf16x8, pa), o1); o1 = MFMA32(v1, __builtin_bit_cast(bf16x8, pb), o1); }
                }
            }
        }
        LBAR();
        if (attempt == 0) {
            piece_wo(111 + w); if (w == 0) piece_wo(119);
        } else {
#pragma unroll
            for (int q = 0; q < 15; ++q) piece_wo(w * 15 + q);
        }
        LAS float* red = (LAS float*)(L + LDS_RED);
#pragma unroll
        for (int e = 0; e < 11; ++e) { red[(w * 11 + e) * 64 + l] = kh ? o0[e] : o1[e]; own[e] = kh ? o1[e] : o0[e]; }
        LBAR();
#pragma unroll
        for (int e = 0; e < 11; ++e) own[e] += red[((w ^ 2) * 11 + e) * 64 + l];
        const float lsum = half_lo(own[10]);
        if (!(lsum >= 8.67e-19f && lsum < 3.0e38f) && attempt == 0) *(LAS unsigned*)(L + LDS_CTL) = 1u;
        const float rl = 1.0f / lsum;
        LAS char* ot = L + LDS_OT + (32 * (2 * cp + kh) + (l & 31)) * 80 + 40 * hh;
        *(LAS u32x2*)(ot + 8 * h5) = (u32x2){cvtpk(own[0] * rl, own[1] * rl), cvtpk(own[2] * rl, own[3] * rl)};
        *(LAS u32x2*)(ot + 16 + 8 * h5) = (u32x2){cvtpk(own[4] * rl, own[5] * rl), cvtpk(own[6] * rl, own[7] * rl)};
        if (h5 == 0) *(LAS u32x2*)(ot + 32) = (u32x2){cvtpk(own[8] * rl, own[9] * rl), 0u};
        LBAR();
        const unsigned isbad = *(LAS const unsigned*)(L + LDS_CTL);
        if (!(isbad && attempt == 0)) break;
    }
    STAMP(1, 7);
    {
        const __amdgpu_buffer_rsrc_t rw = __builtin_amdgcn_make_buffer_rsrc(ws + WS_O + (size_t)(r * 16 + s) * 128 * 80, 0, 128 * 80, 0x00020000);
        __builtin_amdgcn_raw_buffer_store_b128(*(LAS const u32x4*)(L + LDS_OT + w * 1024 + l * 16), rw, l * 16, w * 1024, 16);
        if (w < 2) __builtin_amdgcn_raw_buffer_store_b128(*(LAS const u32x4*)(L + LDS_OT + (8 + w) * 1024 + l * 16), rw, l * 16, (8 + w) * 1024, 16);
    }
    {
        const int tok = l & 15, g4 = l >> 4, q4 = (l >> 2) & 3, p4 = l & 3;
        u32x2 xv[3];
#pragma unroll
        for (int u = 0; u < 3; ++u) {
            const int tt = min(qk + 4 * u, 2 * nmt - 1);
            xv[u] = *(const u32x2*)(x + (size_t)(128 * r + 64 * tgf + 32 * pr + 16 * (tt & 1) + tok) * DM + 16 * (m0 + (tt >> 1)) + 4 * g4);
        }
        SB();
        STAMP(1, 8);
        const __amdgpu_buffer_rsrc_t ro = __builtin_amdgcn_make_buffer_rsrc(ws + WS_O + (size_t)r * 16 * 128 * 80, 0, 16 * 128 * 80, 0x00020000);
        u32x4 bf[5][2];
        unsigned pend = 0x3ffu;
        for (unsigned spins = 0;; ++spins) {
#pragma unroll
            for (int i = 0; i < 5; ++i) {
                const int kp = 32 * (5 * qk + i) + 8 * g4, sp = kp / 40, d = kp - 40 * sp;
#pragma unroll
                for (int nn = 0; nn < 2; ++nn)
                    if (pend & (1u << (2 * i + nn))) bf[i][nn] = __builtin_amdgcn_raw_buffer_load_b128(ro, (sp * 128 + 64 * tgf + 32 * pr + 16 * nn + tok) * 80 + d * 2, 0, 16);
            }
#pragma unroll
            for (int i = 0; i < 5; ++i)
#pragma unroll
                for (int nn = 0; nn < 2; ++nn)
                    if ((pend & (1u << (2 * i + nn))) && __all(bf[i][nn][0] != O_EMPTY && bf[i][nn][1] != O_EMPTY && bf[i][nn][2] != O_EMPTY && bf[i][nn][3] != O_EMPTY)) pend &= ~(1u << (2 * i + nn));
            if (pend == 0u) break;
            if (spins > (1u << 17)) { __hip_atomic_store((GAS unsigned*)(flags + FL_TMO), tag, RLX_AGENT); break; }
            __builtin_amdgcn_s_sleep(8);
        }
        f32x4 fa[5][2];
#pragma unroll
        for (int t = 0; t < 5; ++t) {
            fa[t][0] = (f32x4){0.f, 0.f, 0.f, 0.f}; fa[t][1] = (f32x4){0.f, 0.f, 0.f, 0.f};
#pragma unroll
            for (int sp = 0; sp < 4; ++sp) {
                const u32x4 p = pl[t][sp];
                fa[t][0][0] += bflo(p[0]); fa[t][0][1] += bfhi(p[0]); fa[t][0][2] += bflo(p[1]); fa[t][0][3] += bfhi(p[1]);
                fa[t][1][0] += bflo(p[2]); fa[t][1][1] += bfhi(p[2]); fa[t][1][2] += bflo(p[3]); fa[t][1][3] += bfhi(p[3]);
            }
        }
        WAIT_BAR(0);
        STAMP(1, 9);
        const unsigned ao = (4 * g4 + q4) * 64 + 8 * (p4 & 1);
#pragma unroll
        for (int t = 0; t < 5; ++t)
            if (t < nmt) {
#pragma unroll
                for (int i = 0; i < 5; ++i) {
                    const int p = (t >> 1) * 40 + (5 * qk + i) * 2;
                    const unsigned sw = ao + 16 * ((2 * (t & 1) + (p4 >> 1)) ^ (2 * (g4 & 1)));
                    const s16x4 lo = lds_tr(L + wo_lds(p) + sw), hi = lds_tr(L + wo_lds(p + 1) + sw);
                    const bf16x8 a = __builtin_shufflevector(lo, hi, 0, 1, 2, 3, 4, 5, 6, 7);
                    fa[t][0] = MFMA16(a, __builtin_bit_cast(bf16x8, bf[i][0]), fa[t][0]); fa[t][1] = MFMA16(a, __builtin_bit_cast(bf16x8, bf[i][1]), fa[t][1]);
                }
            }
        STAMP(1, 10);
        LBAR();
        LAS f32x4* ex = (LAS f32x4*)L;
#pragma unroll
        for (int t = 0; t < 5; ++t) { ex[(w * 10 + 2 * t) * 64 + l] = fa[t][0]; ex[(w * 10 + 2 * t + 1) * 64 + l] = fa[t][1]; }
        LBAR();
#pragma unroll
        for (int u = 0; u < 3; ++u) {
            const int tt = qk + 4 * u;
            if (tt < 2 * nmt) {
                f32x4 v = ex[((0 * 2 + pr) * 10 + tt) * 64 + l];
#pragma unroll
                for (int k = 1; k < 4; ++k) v += ex[((k * 2 + pr) * 10 + tt) * 64 + l];
                const size_t off = (size_t)(128 * r + 64 * tgf + 32 * pr + 16 * (tt & 1) + tok) * DM + 16 * (m0 + (tt >> 1)) + 4 * g4;
                *(u32x2*)(out + off) = (u32x2){cvtpk(v[0] + bflo(xv[u][0]), v[1] + bfhi(xv[u][0])), cvtpk(v[2] + bflo(xv[u][1]), v[3] + bfhi(xv[u][1]))};
            }
        }
    }
#undef l
#undef h5
    STAMP(1, 11); STAMP_END(1);
    asm volatile("fused_block_code_end:");
}

extern "C" void kernel_launch(void* const* d_in, const int* in_sizes, int n_in, void* d_out, int out_size, void* d_ws, size_t ws_size, hipStream_t stream) {
    const u16* x = (const u16*)d_in[0]; const u16* wq = (const u16*)d_in[1]; const u16* wk = (const u16*)d_in[2]; const u16* wv = (const u16*)d_in[3];
    const u16* wo = (const u16*)d_in[4]; const u16* wg = (const u16*)d_in[6]; const u16* wu = (const u16*)d_in[7]; const u16* wd = (const u16*)d_in[8];
    fused_block<<<256, 512, 0, stream>>>(x, wq, wk, wv, wo, wg, wu, wd, (char*)d_ws, (u16*)d_out);
}
```

```cpp
#include <hip/hip_runtime.h>
#include <hip/hip_bf16.h>
#include <stdint.h>

typedef unsigned short u16;
typedef short bf16x8 __attribute__((ext_vector_type(8)));
typedef short s16x4 __attribute__((ext_vector_type(4)));
typedef float f32x4 __attribute__((ext_vector_type(4)));
typedef float f32x16 __attribute__((ext_vector_type(16)));
typedef int i32x4 __attribute__((ext_vector_type(4)));
typedef unsigned u32x4 __attribute__((ext_vector_type(4)));
typedef unsigned u32x2 __attribute__((ext_vector_type(2)));
typedef __bf16 bf16x2_t __attribute__((ext_vector_type(2)));
typedef float f32x2 __attribute__((ext_vector_type(2)));
typedef short v4i16_t __attribute__((ext_vector_type(4)));
extern "C" __device__ unsigned long long amd_dispatch_id() __asm("llvm.amdgcn.dispatch.id");
#define LAS __attribute__((address_space(3)))
#define GAS __attribute__((address_space(1)))
#define DI __device__ __forceinline__

constexpr int SEQ = 2048, DM = 576, NH = 32, DH = 18, FF = 4608;

constexpr int KVCH = 37888;
constexpr int KV_V = 16384, KV_K2 = 35840;
constexpr int WOIMG = 120 * 1024;
constexpr size_t WS_FLAGS = 0;
constexpr size_t WS_XN    = 16384;
constexpr size_t WS_WQX   = WS_XN + (size_t)SEQ * DM * 2;
constexpr size_t WS_WO    = WS_WQX + (size_t)DM * 1024 * 2;
constexpr size_t WS_KV    = WS_WO + (size_t)8 * WOIMG;
constexpr size_t WS_O     = WS_KV + 4 * KVCH;
constexpr size_t WS_PART  = WS_O + (size_t)256 * 128 * 80;
constexpr size_t WS_END   = WS_PART + (size_t)256 * 8 * 18 * 1024;
constexpr int FL_PRO = 0, FL_P = 1024, FL_KV = 2048, FL_TMO = 8192;
constexpr unsigned O_EMPTY = 0xffffffffu;

DI float bflo(unsigned v) { return __uint_as_float(v << 16); }
DI float bfhi(unsigned v) { return __uint_as_float(v & 0xffff0000u); }
DI unsigned cvtpk(float lo, float hi) { return __builtin_bit_cast(unsigned, __builtin_convertvector((f32x2){lo, hi}, bf16x2_t)); }
DI float rbf(float f) { return bflo(cvtpk(f, f)); }
DI i32x4 make_srd(const void* p, unsigned bytes) {
    unsigned long long a = (unsigned long long)p; i32x4 s;
    s.x = __builtin_amdgcn_readfirstlane((unsigned)a); s.y = __builtin_amdgcn_readfirstlane((unsigned)(a >> 32) & 0xffffu);
    s.z = (int)bytes; s.w = 0x00020000; return s;
}
DI void dma16(i32x4 srd, unsigned voff, unsigned soff, unsigned lds_addr) {
    unsigned keep;
    asm volatile("s_mov_b32 %0, m0\n\ts_mov_b32 m0, %4\n\ts_nop 0\n\tbuffer_load_dwordx4 %1, %2, %3 offen lds\n\ts_mov_b32 m0, %0"
                 : "=&s"(keep) : "v"(voff), "s"(srd), "s"(soff), "s"(lds_addr) : "memory");
}
DI void dma16_sc1(i32x4 srd, unsigned voff, unsigned soff, unsigned lds_addr) {
    unsigned keep;
    asm volatile("s_mov_b32 %0, m0\n\ts_mov_b32 m0, %4\n\ts_nop 0\n\tbuffer_load_dwordx4 %1, %2, %3 offen sc1 lds\n\ts_mov_b32 m0, %0"
                 : "=&s"(keep) : "v"(voff), "s"(srd), "s"(soff), "s"(lds_addr) : "memory");
}
DI void dma4(i32x4 srd, unsigned voff, unsigned soff, unsigned lds_addr) {
    unsigned keep;
    asm volatile("s_mov_b32 %0, m0\n\ts_mov_b32 m0, %4\n\ts_nop 0\n\tbuffer_load_dword %1, %2, %3 offen lds\n\ts_mov_b32 m0, %0"
                 : "=&s"(keep) : "v"(voff), "s"(srd), "s"(soff), "s"(lds_addr) : "memory");
}
#define WAIT_BAR(N) asm volatile("s_waitcnt vmcnt(" #N ") lgkmcnt(0)\n\ts_barrier" ::: "memory")
#define LBAR() asm volatile("s_waitcnt lgkmcnt(0)\n\ts_barrier" ::: "memory")
#define SB() __builtin_amdgcn_sched_barrier(0)
#define FENCE() asm volatile("" ::: "memory")
#define PIN4(a) asm volatile("" : "+v"((a)[0]), "+v"((a)[1]), "+v"((a)[2]), "+v"((a)[3]))
#define MFMA16(a, b, c) __builtin_amdgcn_mfma_f32_16x16x32_bf16(a, b, c, 0, 0, 0)
#define MFMA32(a, b, c) __builtin_amdgcn_mfma_f32_32x32x16_bf16(a, b, c, 0, 0, 0)
#define MFMA32K8(a, b, c) __builtin_amdgcn_mfma_f32_32x32x8bf16_1k(a, b, c, 0, 0, 0)
#define EXP2(x) __builtin_amdgcn_exp2f(x)
DI s16x4 lds_tr(LAS const char* p) { return __builtin_bit_cast(s16x4, __builtin_amdgcn_ds_read_tr16_b64_v4i16((LAS v4i16_t*)p)); }
DI bf16x8 lds_b128(LAS const char* p) { return *(LAS const bf16x8*)p; }
DI bf16x8 lds_afrag(LAS const char* p) { const s16x4 lo = lds_tr(p), hi = lds_tr(p + 2048); return __builtin_shufflevector(lo, hi, 0, 1, 2, 3, 4, 5, 6, 7); }
#define RLX_AGENT __ATOMIC_RELAXED, __HIP_MEMORY_SCOPE_AGENT
template <int X> DI float xor_lane(float v) { return __int_as_float(__builtin_amdgcn_ds_swizzle(__float_as_int(v), (X << 10) | 0x1f)); }
DI float half_sum(float v) { auto r = __builtin_amdgcn_permlane32_swap(__float_as_uint(v), __float_as_uint(v), false, false); return __uint_as_float(r[0]) + __uint_as_float(r[1]); }
DI float half_max(float v) { auto r = __builtin_amdgcn_permlane32_swap(__float_as_uint(v), __float_as_uint(v), false, false); return fmaxf(__uint_as_float(r[0]), __uint_as_float(r[1])); }
DI float half_lo(float v) { auto r = __builtin_amdgcn_permlane32_swap(__float_as_uint(v), __float_as_uint(v), false, false); return __uint_as_float(r[0]); }
template <int CTRL, int ROWS> DI float dpp_add(float v) { return v + __int_as_float(__builtin_amdgcn_update_dpp(0, __float_as_int(v), CTRL, ROWS, 0xf, true)); }
DI float wave_sum_dpp(float v) {
    v = dpp_add<0x111, 0xf>(v); v = dpp_add<0x112, 0xf>(v); v = dpp_add<0x114, 0xf>(v); v = dpp_add<0x118, 0xf>(v);
    v = dpp_add<0x142, 0xa>(v); v = dpp_add<0x143, 0xc>(v);
    return __int_as_float(__builtin_amdgcn_readlane(__float_as_int(v), 63));
}
DI float wave_sum(float v) { v += xor_lane<1>(v); v += xor_lane<2>(v); v += xor_lane<4>(v); v += xor_lane<8>(v); v += xor_lane<16>(v); return half_sum(v); }
DI float wave_max(float v) { v = fmaxf(v, xor_lane<1>(v)); v = fmaxf(v, xor_lane<2>(v)); v = fmaxf(v, xor_lane<4>(v)); v = fmaxf(v, xor_lane<8>(v)); v = fmaxf(v, xor_lane<16>(v)); return half_max(v); }
DI int fresh_lane() { int t; asm volatile("v_mbcnt_lo_u32_b32 %0, -1, 0\n\tv_mbcnt_hi_u32_b32 %0, -1, %0" : "=v"(t)); return t; }
DI void st_agent64(void* p, unsigned lo, unsigned hi) { __hip_atomic_store((GAS unsigned long long*)p, ((unsigned long long)hi << 32) | lo, RLX_AGENT); }
DI void wait_tags(const char* flags, unsigned off, bool use, unsigned tag) {
    for (unsigned spins = 0;; ++spins) {
        const unsigned v = use ? __hip_atomic_load((GAS unsigned*)(flags + off), RLX_AGENT) : tag;
        if (__all(v == tag)) break;
        if (spins > (1u << 19)) { __hip_atomic_store((GAS unsigned*)(flags + FL_TMO), tag, RLX_AGENT); break; }
        __builtin_amdgcn_s_sleep(1);
    }
    FENCE();
}
#define STAMP_DECL
#define STAMP(K, i)
#define STAMP_END(K)

#define DMA345 dma16_sc1
constexpr int ST1 = 49152;
constexpr int LDS_D0 = 0, LDS_D1 = 49152, LDS_H = 86016, LDS_Q1 = 36864, LDS_Q2 = 45056;
constexpr int LDS_PF = 147456;
constexpr int LDS_KM = 46080;
constexpr int LDS_D2 = 126976;
constexpr int LDS_KV0 = 125952;
constexpr int LDS_ZERO = 122880;
constexpr int LDS_CTL = LDS_ZERO + 640;
constexpr int LDS_MX = LDS_CTL + 64;
constexpr int LDS_RED = 2 * KVCH + 9 * 1024;
constexpr int LDS_OT = LDS_RED + 8 * 11 * 256;
constexpr int LDS_BYTES = 163840;
DI unsigned wo_lds(int p) { return p < 37 ? LDS_KV0 + p * 1024 : p < 74 ? (p - 37) * 1024 : p < 111 ? KVCH + (p - 74) * 1024 : 2 * KVCH + (p - 111) * 1024; }

template <int NM, bool FIRST, int ND, int DPG, int AD = 3, class Dma>
DI void gemm_step(f32x4 (&acc)[10][4], bf16x8 (&ca)[2], bf16x8 (&cb)[4], bf16x8 (&nb)[4], LAS const char* ap, LAS const char* bp, const Dma& dma) {
    bf16x8 a[10];
#pragma unroll
    for (int n = 0; n < 4; ++n) nb[n] = lds_b128(bp + n * 1024);
    a[0] = lds_afrag(ap); a[1] = lds_afrag(ap + 4096); if (AD > 2) a[2] = lds_afrag(ap + 2 * 4096);
    SB();
    int d = 0;
    if (!FIRST) {
#pragma unroll
        for (int k = 0; k < DPG; ++k) if (d < ND) dma(d++);
#pragma unroll
        for (int n = 0; n < 4; ++n) acc[NM - 2][n] = MFMA16(ca[0], cb[n], acc[NM - 2][n]);
        PIN4(acc[NM - 2]); SB();
#pragma unroll
        for (int k = 0; k < DPG; ++k) if (d < ND) dma(d++);
#pragma unroll
        for (int n = 0; n < 4; ++n) acc[NM - 1][n] = MFMA16(ca[1], cb[n], acc[NM - 1][n]);
        PIN4(acc[NM - 1]); SB();
    }
#pragma unroll
    for (int m = 0; m < NM - 2; ++m) {
        if (m + AD < NM) a[m + AD] = lds_afrag(ap + (m + AD) * 4096);
#pragma unroll
        for (int k = 0; k < DPG; ++k) if (d < ND) dma(d++);
#pragma unroll
        for (int n = 0; n < 4; ++n) acc[m][n] = MFMA16(a[m], nb[n], acc[m][n]);
        PIN4(acc[m]); SB();
    }
    ca[0] = a[NM - 2]; ca[1] = a[NM - 1];
}
template <int NM>
DI void gemm_flush(f32x4 (&acc)[10][4], const bf16x8 (&ca)[2], const bf16x8 (&cb)[4]) {
#pragma unroll
    for (int n = 0; n < 4; ++n) acc[NM - 2][n] = MFMA16(ca[0], cb[n], acc[NM - 2][n]);
#pragma unroll
    for (int n = 0; n < 4; ++n) acc[NM - 1][n] = MFMA16(ca[1], cb[n], acc[NM - 1][n]);
}

__global__ __launch_bounds__(512, 2) void fused_block(const u16* __restrict__ x, const u16* __restrict__ wq, const u16* __restrict__ wk, const u16* __restrict__ wv,
                                                       const u16* __restrict__ wo, const u16* __restrict__ wg, const u16* __restrict__ wu, const u16* __restrict__ wd,
                                                       char* __restrict__ ws, u16* __restrict__ out) {
    __shared__ __attribute__((aligned(1024))) char lds[LDS_BYTES];
    const int tid = threadIdx.x, l = tid & 63;
    const int w = __builtin_amdgcn_readfirstlane(tid >> 6);
    const int b = blockIdx.x, s = 2 * (b & 7) + ((b >> 3) & 1), r = b >> 4, wgi = r * 16 + s;
    const int tg = w >> 2, fg = w & 3;
    constexpr unsigned lds0 = 0;
    LAS char* L = (LAS char*)lds;
    char* flags = ws + WS_FLAGS;
    const unsigned tag = (((unsigned)amd_dispatch_id() + 1u) * 0x9E3779B1u) ^ ((unsigned)((unsigned long long)__builtin_amdgcn_dispatch_ptr() >> 6) * 0x85EBCA77u) ^ 0x5bd1e995u;

    const int dmat = w & 1, dR = (w >> 1) & 1, dcg0 = 5 * (w >> 2);
    const int di = l >> 2, dsig = l & 3;
    const int dmi = dsig ^ (2 * dmat + ((di >> 2) & 1)), dkrow = 8 * (di >> 2) + (di & 3);
    const i32x4 srd_w = make_srd(dmat ? wu : wg, DM * FF * 2);
    const i32x4 srd_q = make_srd(ws + WS_WQX, DM * 1024 * 2);
    const i32x4 srd_x = make_srd(ws + WS_XN, SEQ * DM * 2);
    const unsigned vo_w = dkrow * (FF * 2) + 16 * dmi, vo_q = dkrow * 2048 + 16 * dmi, vo_x = l * 16;
    const unsigned dlds = lds0 + dR * 2048 + dmat * 1024;
    auto pieceW = [&](int j, unsigned slot, int cg) { dma16(srd_w, vo_w, (32 * j + 4 * dR) * (FF * 2) + 288 * s * 2 + cg * 64, dlds + slot + cg * 4096); };
    auto pieceQ = [&](int j, unsigned slot) { dma16_sc1(srd_q, vo_q, (32 * j + 4 * dR) * 2048 + (64 * s + 32 * dmat) * 2, dlds + slot + 9 * 4096); };
    auto pieceX = [&](int j, unsigned slot, int n) { dma16_sc1(srd_x, vo_x, ((r * 18 + j) * 8 + n) * 1024, lds0 + slot + 40960 + n * 1024); };
    auto piece1k = [&](int j, unsigned slot, int i, int kind) {
        if (i < 3) { if (kind & 1) pieceW(j, slot, dcg0 + i); }
        else if (i == 3) { if (w == 3) { if (kind & 2) pieceQ(j, slot); } else if (kind & 1) pieceW(j, slot, dcg0 + 3); }
        else if (i == 4) {
            if (w < 3) { if (kind & 1) pieceW(j, slot, 4); } else if (w == 3) { if (kind & 2) pieceX(j, slot, 7); }
            else if (w < 7) { if (kind & 2) pieceQ(j, slot); } else if (kind & 1) pieceW(j, slot, 3);
        } else { if (w == 7) { if (kind & 1) pieceW(j, slot, 4); } else if (kind & 2) pieceX(j, slot, w); }
    };
    auto piece2 = [&](int j, unsigned slot, int i) {
        const int ii = (w >= 4 && i == 4) ? 3 : i;
        const unsigned vo_d = ((vo_w & ~63u) >> 3) + ((vo_w & 48u) << 1);
        dma16(make_srd(wd, FF * DM * 2), vo_d, (288 * s + 32 * j + 4 * dR) * (DM * 2) + 16 * dmat + (dcg0 + ii) * 128, dlds + slot + (dcg0 + ii) * 4096);
    };

    STAMP_DECL;
    STAMP(1, 0);
    if (w == 7) {
        unsigned long long pc; unsigned len;
        asm volatile("s_getpc_b64 %0\n\tcode_here_%=:\n\ts_mov_b32 %1, fused_block_code_end-code_here_%=" : "=s"(pc), "=s"(len));
        i32x4 sc; sc.x = (int)(unsigned)pc; sc.y = (int)((unsigned)(pc >> 32) & 0xffffu); sc.z = (int)len; sc.w = 0x00020000;
        dma4(sc, l < 20 ? (unsigned)(((b >> 3) * 20 + l) * 128) : 0x80000000u, 0, lds0 + LDS_PF);
    }
    {
        const u32x4* xs = (const u32x4*)(x + (size_t)(128 * r + 8 * s + w) * DM);
        const u32x4 v0 = xs[l];
        u32x4 v1 = xs[64 + (l & 7)];
        unsigned qv[8];
        {
            const int tq = min(tid, 287), k = 36 * r + (tq >> 3), mat = (tq >> 2) & 1, t = tq & 3;
            const u16* wc = ((s & 1) ? wv : wk) + k * DH;
            const u16* wh = wq + (size_t)k * DM + (2 * s + (t >> 1)) * DH;
#pragma unroll
            for (int e = 0; e < 8; ++e) {
                const int rho = mat * 8 + e, cc = (t == 1 ? 0 : 12) + rho - 4;
                const bool isq = (t & 1) == 0 || rho < 2, isc = !isq && rho >= 4 && cc < (t == 1 ? 12 : 18);
                const u16* p = isc ? wc + cc : wh + ((t & 1) == 0 ? rho : 16 + (rho & 1));
                const unsigned val = *p;
                qv[e] = (isq || isc) ? val : 0u;
            }
        }
        const int cgp = s & 7, wp = 4 * (r * 2 + (s >> 3)) + ((min(max(tid, 192), 447) - 192) >> 6);
        const bool wo_act = tid >= 192 && tid < 448 && wp < 120;
        u32x4 wov;
        {
            const int m0 = cgp < 4 ? 5 * cgp : 20 + 4 * (cgp - 4), wpc = min(wp, 119);
            const int pp = wpc / 40, js = (wpc % 40) >> 1, R = wpc & 1, i = l >> 2, ch = (l & 3) ^ (2 * ((i >> 2) & 1));
            const int kp = 32 * js + 8 * (i >> 2) + (i & 3) + 4 * R, sp = kp / 40, d = kp - 40 * sp, hd = d >= 20 ? 1 : 0, dd = d - 20 * hd;
            const int row = 36 * sp + 18 * hd + (dd < 18 ? dd : 17), col = min(16 * (m0 + 2 * pp) + 8 * ch, DM - 8);
            wov = *(const u32x4*)(wo + (size_t)row * DM + col);
        }
        FENCE();
        STAMP(1, 13);
        if (l >= 8) v1 = (u32x4){0u, 0u, 0u, 0u};
        float sm = 0.f;
#pragma unroll
        for (int e = 0; e < 4; ++e) sm += bflo(v0[e]) + bfhi(v0[e]) + bflo(v1[e]) + bfhi(v1[e]);
        const float mean = wave_sum_dpp(sm) * (1.0f / DM);
        float q = 0.f, q1 = 0.f;
#pragma unroll
        for (int e = 0; e < 4; ++e) { const float a = bflo(v0[e]) - mean, c = bfhi(v0[e]) - mean; q += a * a + c * c; }
#pragma unroll
        for (int e = 0; e < 4; ++e) { const float a = bflo(v1[e]) - mean, c = bfhi(v1[e]) - mean; q1 += a * a + c * c; }
        if (l < 8) q += q1;
        const float m = rbf(mean), var = rbf(wave_sum_dpp(q) * (1.0f / DM));
        const float rs = 1.0f / rbf(sqrtf(rbf(var + 1e-5f)));
        LAS char* xst = L + 2 * ST1;
        {
            u32x4 o;
#pragma unroll
            for (int e = 0; e < 4; ++e) o[e] = cvtpk(rbf(bflo(v0[e]) - m) * rs, rbf(bfhi(v0[e]) - m) * rs);
            *(LAS u32x4*)(xst + l * 128 + w * 16) = o;
#pragma unroll
            for (int e = 0; e < 4; ++e) o[e] = cvtpk(rbf(bflo(v1[e]) - m) * rs, rbf(bfhi(v1[e]) - m) * rs);
            if (l < 8) *(LAS u32x4*)(xst + (64 + l) * 128 + w * 16) = o;
        }
        asm volatile("" : "+v"(wov));
        LBAR();
        STAMP(1, 14);
        if (w < 7) {
            const __amdgpu_buffer_rsrc_t rx = __builtin_amdgcn_make_buffer_rsrc(ws + WS_XN, 0, SEQ * DM * 2, 0x00020000);
#pragma unroll
            for (int u = 0; u < 2; ++u) {
                const int i = tid + 448 * u, c = i >> 3, t = i & 7;
                const u32x4 v = *(LAS const u32x4*)(xst + min(i, 575) * 16);
                __builtin_amdgcn_raw_buffer_store_b128(v, rx, i < 576 ? (unsigned)(((r * 18 + (c >> 2)) * 8 + (s >> 1)) * 1024 + ((c & 3) * 16 + 8 * (s & 1) + t) * 16) : 0xfffffff0u, 0, 16);
            }
            {
                u32x4 o; o[0] = qv[0] | (qv[1] << 16); o[1] = qv[2] | (qv[3] << 16); o[2] = qv[4] | (qv[5] << 16); o[3] = qv[6] | (qv[7] << 16);
                const __amdgpu_buffer_rsrc_t rq = __builtin_amdgcn_make_buffer_rsrc(ws + WS_WQX, 0, DM * 1024 * 2, 0x00020000);
                __builtin_amdgcn_raw_buffer_store_b128(o, rq, tid < 288 ? (unsigned)(((36 * r + (tid >> 3)) * 128 + s * 8 + (tid & 7)) * 16) : 0xfffffff0u, 0, 16);
            }
            WAIT_BAR(0);
        } else LBAR();
        STAMP(1, 15);
        if (tid == 0) __hip_atomic_store((GAS unsigned*)(flags + FL_PRO + wgi * 4), tag, RLX_AGENT);
#pragma unroll
        for (int i = 0; i < 6; ++i) piece1k(0, 0, i, 1);
#pragma unroll
        for (int i = 0; i < 6; ++i) piece1k(1, ST1, i, 1);
        if (w == 7) {
            const unsigned o = (l & 7) < 5 ? (unsigned)(((l >> 3) * 16 + r) * (FF * 2) + ((288 * s * 2) & ~127) + (l & 7) * 128) : 0x80000000u;
#pragma unroll
            for (int g = 0; g < 2; ++g) { dma4(make_srd(wg, DM * FF * 2), o, g * 128 * (FF * 2), lds0 + LDS_PF); dma4(srd_w, o, g * 128 * (FF * 2), lds0 + LDS_PF); }
        }
        if (w < 7) {
            const __amdgpu_buffer_rsrc_t rw = __builtin_amdgcn_make_buffer_rsrc(ws + WS_WO, 0, 8 * WOIMG, 0x00020000);
            __builtin_amdgcn_raw_buffer_store_b128(wov, rw, wo_act ? (unsigned)(cgp * WOIMG + wp * 1024 + l * 16) : 0xfffffff0u, 0, 16);
            const __amdgpu_buffer_rsrc_t ro = __builtin_amdgcn_make_buffer_rsrc(ws + WS_O + (size_t)wgi * 128 * 80, 0, 128 * 80, 0x00020000);
            const u32x4 e = (u32x4){O_EMPTY, O_EMPTY, O_EMPTY, O_EMPTY};
            __builtin_amdgcn_raw_buffer_store_b128(e, ro, tid * 16, 0, 16);
            __builtin_amdgcn_raw_buffer_store_b128(e, ro, (tid + 448) * 16, 0, 16);
        }
        STAMP(1, 12);
        if (w == 0) wait_tags(flags, FL_PRO + (l < 16 ? (r * 16 + l) * 4 : ((l - 16) * 16 + s) * 4), l < 32, tag);
        asm volatile("s_barrier" ::: "memory");
#pragma unroll
        for (int i = 3; i < 6; ++i) piece1k(0, 0, i, 2);
#pragma unroll
        for (int i = 3; i < 6; ++i) piece1k(1, ST1, i, 2);
    }
    const int lg = fresh_lane();
    const unsigned a_off = ((lg & 3) >> 1) * 1024 + 16 * (4 * (4 * (lg >> 4) + ((lg >> 2) & 3)) + (fg ^ (2 * ((lg & 3) >> 1) + ((lg >> 4) & 1)))) + 8 * (lg & 1);

    f32x4 acc[10][4];
#pragma unroll
    for (int m = 0; m < 10; ++m)
#pragma unroll
        for (int n = 0; n < 4; ++n) acc[m][n] = (f32x4){0.f, 0.f, 0.f, 0.f};
    bf16x8 ca[2], bA[4], bB[4];
    {
        const unsigned bo = 40960 + tg * 4096 + lg * 16;
        WAIT_BAR(0);
        STAMP(1, 1);
        const bool q3 = w == 3, w4 = w < 3 || w == 7, x4 = w == 3, w5 = w == 7;
        const i32x4 srd3 = q3 ? srd_q : srd_w, srd4 = w4 ? srd_w : x4 ? srd_x : srd_q, srd5 = w5 ? srd_w : srd_x;
        const unsigned vo3 = q3 ? vo_q : vo_w, vo4 = w4 ? vo_w : x4 ? vo_x : vo_q, vo5 = w5 ? vo_w : vo_x;
        const unsigned soW = 4 * dR * (FF * 2) + 288 * s * 2, soQ = 4 * dR * 2048 + (64 * s + 32 * dmat) * 2, soX = r * 18 * 8192;
        const unsigned stW = 32 * FF * 2, stQ = 32 * 2048, stX = 8192;
        const unsigned so3 = q3 ? soQ : soW + (dcg0 + 3) * 64, st3 = q3 ? stQ : stW, lo3 = q3 ? dlds + 9 * 4096 : dlds + (dcg0 + 3) * 4096;
        const int cg4 = w < 3 ? 4 : 3;
        const unsigned so4 = w4 ? soW + cg4 * 64 : x4 ? soX + 7 * 1024 : soQ, st4 = w4 ? stW : x4 ? stX : stQ, lo4 = w4 ? dlds + cg4 * 4096 : x4 ? lds0 + 40960 + 7 * 1024 : dlds + 9 * 4096;
        const unsigned so5 = w5 ? soW + 4 * 64 : soX + w * 1024, st5 = w5 ? stW : stX, lo5 = w5 ? dlds + 4 * 4096 : lds0 + 40960 + w * 1024;
        unsigned vo_pf;
        { const int lp = fresh_lane(); vo_pf = (lp & 7) < 5 ? (unsigned)(((lp >> 3) * 16 + r) * (FF * 2) + ((288 * s * 2) & ~127) + (lp & 7) * 128) : 0x80000000u; }
        auto stage1 = [&](int j, unsigned slot, int i) {
            if (i < 3) dma16(srd_w, vo_w, soW + (dcg0 + i) * 64 + j * stW, dlds + (dcg0 + i) * 4096 + slot);
            else if (i == 3) DMA345(srd3, vo3, so3 + j * st3, lo3 + slot);
            else if (i == 4) DMA345(srd4, vo4, so4 + j * st4, lo4 + slot);
            else if (i == 5) DMA345(srd5, vo5, so5 + j * st5, lo5 + slot);
            else if (w >= 6) {
                const int t = j - 2;
                if (t == 6) { const int lp = fresh_lane(); vo_pf = (w == 7 && (lp & 15) < 9) ? (unsigned)((288 * s + (lp >> 4) * 16 + r) * (DM * 2) + (lp & 15) * 128) : 0x80000000u; }
                const unsigned so = t < 6 ? ((t & 1) ? 0x70000000u : (unsigned)(2 + (t >> 1)) * 128 * (FF * 2)) : t < 11 ? (unsigned)(t - 6) * 64 * (DM * 2) : 0x70000000u;
                dma4(t < 6 ? srd_w : make_srd(wd, FF * DM * 2), vo_pf, so, lds0 + LDS_PF);
            }
        };
        gemm_step<10, true, 7, 1>(acc, ca, bB, bA, L + a_off, L + bo, [&](int i) { stage1(2, 2 * ST1, i); });
        unsigned slot = ST1, slot2 = 0;
        if (w >= 6) { WAIT_BAR(7); } else { WAIT_BAR(6); }
        gemm_step<10, false, 7, 1>(acc, ca, bA, bB, L + slot + a_off, L + slot + bo, [&](int i) { stage1(3, slot2, i); });
        slot = 2 * ST1; slot2 = ST1;
#pragma unroll 1
        for (int j = 2; j < 16; j += 2) {
            if (w >= 6) { WAIT_BAR(8); } else { WAIT_BAR(6); }
            gemm_step<10, false, 7, 1>(acc, ca, bB, bA, L + slot + a_off, L + slot + bo, [&](int i) { stage1(j + 2, slot2, i); });
            slot = (slot == 2 * ST1) ? 0 : slot + ST1; slot2 = (slot2 == 2 * ST1) ? 0 : slot2 + ST1;
            if (w >= 6) { WAIT_BAR(8); } else { WAIT_BAR(6); }
            gemm_step<10, false, 7, 1>(acc, ca, bA, bB, L + slot + a_off, L + slot + bo, [&](int i) { stage1(j + 3, slot2, i); });
            slot = (slot == 2 * ST1) ? 0 : slot + ST1; slot2 = (slot2 == 2 * ST1) ? 0 : slot2 + ST1;
        }
#pragma unroll
        for (int n = 0; n < 4; ++n) bA[n] = bB[n];
#pragma unroll 1
        for (int j = 16; j < 18; ++j) {
            if (j == 16 && w >= 6) { WAIT_BAR(8); } else if (j == 16) { WAIT_BAR(6); } else { WAIT_BAR(5); }
            gemm_step<10, false, 5, 1>(acc, ca, bA, bB, L + slot + a_off, L + slot + bo, [&](int i) { piece2(j - 16, j == 16 ? LDS_D0 : LDS_D1, i); });
#pragma unroll
            for (int n = 0; n < 4; ++n) bA[n] = bB[n];
            slot += ST1;
        }
        gemm_flush<10>(acc, ca, bA);
        SB(); FENCE();
    }
    if ((fg & 1) && tg == (s >> 3)) {
        const int l = fresh_lane(), tok = l & 15, g4 = l >> 4;
        const int e = s >> 1;
        f32x4 cv = acc[9][0];
        if ((e & 3) == 1) cv = acc[9][1]; else if ((e & 3) == 2) cv = acc[9][2]; else if ((e & 3) == 3) cv = acc[9][3];
        const int c0 = (fg == 1 ? 0 : 12) + 4 * (g4 - 1);
        const bool act = g4 >= 1 && c0 < 18;
        const unsigned p0 = cvtpk(cv[0], cv[1]), p1 = cvtpk(cv[2], cv[3]);
        const int kt = 4 * r + (e >> 1);
        char* kvb = ws + WS_KV + ((kt >> 3) & 3) * KVCH; const int lt = 8 * (kt >> 5) + (kt & 7);
        if ((s & 1) == 0) {
            const int key = 16 * (e & 1) + tok;
            if (act) {
                if (c0 < 16) st_agent64(kvb + lt * 1024 + ((c0 >> 3) * 32 + key) * 16 + (c0 & 4) * 2, p0, p1);
                else __hip_atomic_store((GAS unsigned*)(kvb + KV_K2 + lt * 128 + key * 4), p0, RLX_AGENT);
            }
            float n2 = 0.f;
            if (act) { n2 = bflo(p0) * bflo(p0) + bfhi(p0) * bfhi(p0); if (c0 < 16) n2 += bflo(p1) * bflo(p1) + bfhi(p1) * bfhi(p1); }
            n2 += xor_lane<16>(n2); n2 = half_sum(n2);
            n2 = wave_max(n2);
            if (l == 0) *(LAS float*)(L + LDS_KM + (fg == 1 ? 0 : 4)) = n2;
        } else {
            if (l == 0) *(LAS float*)(L + LDS_KM + (fg == 1 ? 0 : 4)) = 0.f;
            if (fg == 1 && l < 2) {
                char* op = kvb + KV_V + lt * 1216 + (e & 1) * 608 + l * 304 + 18 * 16;
                st_agent64(op, 0x3f803f80u, 0x3f803f80u); st_agent64(op + 8, 0x3f803f80u, 0x3f803f80u);
            }
            const int hv = (tok >> 2) & 1, jj = 4 * (tok >> 3) + (tok & 3);
            GAS u16* vb = (GAS u16*)(kvb + KV_V + lt * 1216 + (e & 1) * 608 + hv * 304 + jj * 2);
            if (act) {
                __hip_atomic_store(vb + (c0 + 0) * 8, (u16)(p0 & 0xffff), RLX_AGENT);
                __hip_atomic_store(vb + (c0 + 1) * 8, (u16)(p0 >> 16), RLX_AGENT);
                if (c0 < 16) { __hip_atomic_store(vb + (c0 + 2) * 8, (u16)(p1 & 0xffff), RLX_AGENT);
                               __hip_atomic_store(vb + (c0 + 3) * 8, (u16)(p1 >> 16), RLX_AGENT); }
            }
        }
    }
    LBAR();
    STAMP(1, 2);
    {
        const int l = fresh_lane(), tok = l & 15, g4 = l >> 4;
#pragma unroll
        for (int cg = 0; cg < 9; ++cg)
#pragma unroll
            for (int pr = 0; pr < 2; ++pr) {
                float h[4];
#pragma unroll
                for (int e = 0; e < 4; ++e) {
                    auto sw = __builtin_amdgcn_permlane32_swap(__float_as_uint(acc[cg][2 * pr][e]), __float_as_uint(acc[cg][2 * pr + 1][e]), false, false);
                    h[e] = fmaxf(__uint_as_float(sw[0]), 0.f) * __uint_as_float(sw[1]);
                }
                const int n = 4 * tg + 2 * pr + (l >> 5);
                u32x2 o; o[0] = cvtpk(h[0], h[1]); o[1] = cvtpk(h[2], h[3]);
                *(LAS u32x2*)(L + LDS_H + ((8 - cg) * 8 + n) * 1024 + (fg * 16 + tok) * 16 + 8 * (g4 & 1)) = o;
            }
        const float qs = 0.23570226039551584f * 1.4426950408889634f;
        const int hh = fg >> 1;
#pragma unroll
        for (int n = 0; n < 4; ++n) {
            const int ct = 2 * tg + (n >> 1), query = 16 * (n & 1) + tok;
            const unsigned p0 = cvtpk(acc[9][n][0] * qs, acc[9][n][1] * qs), p1 = cvtpk(acc[9][n][2] * qs, acc[9][n][3] * qs);
            if ((fg & 1) == 0) *(LAS u32x2*)(L + LDS_Q1 + (hh * 4 + ct) * 1024 + ((g4 >> 1) * 32 + query) * 16 + (g4 & 1) * 8) = (u32x2){p0, p1};
            else if (g4 == 0) *(LAS unsigned*)(L + LDS_Q2 + (hh * 4 + ct) * 128 + query * 4) = p0;
        }
    }
    WAIT_BAR(0);
    if (w == 0 && fresh_lane() == 0) {
        st_agent64(flags + FL_KV + wgi * 16, tag, *(LAS const unsigned*)(L + LDS_KM));
        st_agent64(flags + FL_KV + wgi * 16 + 8, tag, *(LAS const unsigned*)(L + LDS_KM + 4));
    }
    STAMP(1, 3);
    const i32x4 srd_kv = make_srd(ws + WS_KV, 4 * KVCH);
    auto piece_kv = [&](int c, int i) {
        const int p = min(w + 8 * i, 36);
        dma16_sc1(srd_kv, vo_x, c * KVCH + p * 1024, lds0 + (c == 0 ? LDS_KV0 : (c - 1) * KVCH) + p * 1024);
    };
#pragma unroll
    for (int m = 0; m < 9; ++m)
#pragma unroll
        for (int n = 0; n < 4; ++n) acc[m][n] = (f32x4){0.f, 0.f, 0.f, 0.f};
    {
        LAS const char* hb = L + LDS_H + 8 * 8192 + 4 * tg * 1024 + l * 16;
        gemm_step<9, true, 0, 1>(acc, ca, bB, bA, L + LDS_D0 + a_off, hb, [](int) {});
        LBAR();
        gemm_step<9, false, 5, 2>(acc, ca, bA, bB, L + LDS_D1 + a_off, hb - 1 * 8192, [&](int i) { piece2(2, LDS_D0, i); });
        WAIT_BAR(0);
        gemm_step<9, false, 5, 2>(acc, ca, bB, bA, L + LDS_D0 + a_off, hb - 2 * 8192, [&](int i) { piece2(3, LDS_D1, i); });
        WAIT_BAR(0);
        gemm_step<9, false, 5, 2>(acc, ca, bA, bB, L + LDS_D1 + a_off, hb - 3 * 8192, [&](int i) { piece2(4, LDS_D0, i); });
        WAIT_BAR(0);
        gemm_step<9, false, 10, 2>(acc, ca, bB, bA, L + LDS_D0 + a_off, hb - 4 * 8192, [&](int i) { if (i < 5) piece2(5, LDS_D1, i); else piece2(6, LDS_D2, i - 5); });
        WAIT_BAR(5);
        gemm_step<9, false, 5, 1>(acc, ca, bA, bB, L + LDS_D1 + a_off, hb - 5 * 8192, [&](int i) { piece2(7, LDS_D0, i); });
        WAIT_BAR(5);
        gemm_step<9, false, 5, 1>(acc, ca, bB, bA, L + LDS_D2 + a_off, hb - 6 * 8192, [&](int i) { piece2(8, LDS_D1, i); });
        if (w == 0) {
            const int lk = fresh_lane();
            const __amdgpu_buffer_rsrc_t rk = __builtin_amdgcn_make_buffer_rsrc(flags + FL_KV, 0, 4096, 0x00020000);
            float ma, mb;
            for (unsigned spins = 0;; ++spins) {
                bool ok = true; ma = 0.f; mb = 0.f;
#pragma unroll
                for (int i = 0; i < 4; ++i) {
                    const u32x4 v = __builtin_amdgcn_raw_buffer_load_b128(rk, (i * 64 + lk) * 16, 0, 16);
                    ok = ok && v[0] == tag && v[2] == tag; ma = fmaxf(ma, __uint_as_float(v[1])); mb = fmaxf(mb, __uint_as_float(v[3]));
                }
                if (__all(ok)) break;
                if (spins > (1u << 19)) { __hip_atomic_store((GAS unsigned*)(flags + FL_TMO), tag, RLX_AGENT); break; }
                __builtin_amdgcn_s_sleep(1);
            }
            ma = wave_max(ma); mb = wave_max(mb);
            if (lk == 0) *(LAS float*)(L + LDS_KM + 8) = ma + mb;
        }
        WAIT_BAR(5);
        gemm_step<9, false, 5, 1>(acc, ca, bA, bB, L + LDS_D0 + a_off, hb - 7 * 8192, [&](int i) { piece_kv(0, i); });
        WAIT_BAR(5);
        gemm_step<9, false, 0, 1>(acc, ca, bB, bA, L + LDS_D1 + a_off, hb - 8 * 8192, [](int) {});
        gemm_flush<9>(acc, ca, bA);
        SB(); FENCE();
    }
    STAMP(1, 4);
    const int la = fresh_lane();
#define l la
    const int hh = w >> 2, kh = (w >> 1) & 1, cp = w & 1;
#define h5 (l >> 5)
    bf16x8 q1[2]; u32x2 q2[2];
#pragma unroll
    for (int c = 0; c < 2; ++c) {
        q1[c] = lds_b128(L + LDS_Q1 + (hh * 4 + 2 * cp + c) * 1024 + l * 16);
        q2[c][0] = *(LAS const unsigned*)(L + LDS_Q2 + (hh * 4 + 2 * cp + c) * 128 + (l & 31) * 4);
    }
    u32x4 pk[18];
#pragma unroll
    for (int m = 0; m < 9; ++m)
#pragma unroll
        for (int pr = 0; pr < 2; ++pr) {
            u32x4 o; o[0] = cvtpk(acc[m][2 * pr][0], acc[m][2 * pr][1]); o[1] = cvtpk(acc[m][2 * pr][2], acc[m][2 * pr][3]);
            o[2] = cvtpk(acc[m][2 * pr + 1][0], acc[m][2 * pr + 1][1]); o[3] = cvtpk(acc[m][2 * pr + 1][2], acc[m][2 * pr + 1][3]);
            pk[m * 2 + pr] = o;
        }
    {
        const float kmax2 = *(LAS const float*)(L + LDS_KM + 8);
#pragma unroll
        for (int c = 0; c < 2; ++c) {
            const u32x4 a = __builtin_bit_cast(u32x4, q1[c]); const unsigned t = h5 ? 0u : q2[c][0];
            float n2 = bflo(t) * bflo(t) + bfhi(t) * bfhi(t);
#pragma unroll
            for (int e = 0; e < 4; ++e) n2 += bflo(a[e]) * bflo(a[e]) + bfhi(a[e]) * bfhi(a[e]);
            n2 = half_sum(n2);
            const float cb = sqrtf(n2 * kmax2) * 1.001f + 0.01f;
            const float chi = rbf(cb), clo = cb - chi;
            q2[c][0] = t; q2[c][1] = h5 ? 0u : cvtpk(-chi, -clo);
        }
    }
    if (w == 0 && l < 40) *(LAS u32x4*)(L + LDS_ZERO + l * 16) = (u32x4){0u, 0u, 0u, 0u};
    if (w == 0 && l == 40) *(LAS unsigned*)(L + LDS_CTL) = 0u;
    WAIT_BAR(0);
    STAMP(1, 5);
#undef l
    const int lb = fresh_lane();
#define l lb
    const unsigned kone = h5 ? 0u : 0x3f803f80u;
    const bool vrow = (l & 31) < 19;
    const unsigned k1_base = 8 * kh * 1024 + l * 16, k2_base = KV_K2 + 8 * kh * 128 + (l & 31) * 4;
    const unsigned v_base = vrow ? KV_V + 8 * kh * 1216 + h5 * 304 + (l & 31) * 16 : LDS_ZERO, v_mul = vrow ? 1u : 0u;
    const int tgf = s >> 3, cgp = s & 7, nmt = cgp < 4 ? 5 : 4, m0 = cgp < 4 ? 5 * cgp : 20 + 4 * (cgp - 4);
    const int pr = w & 1, qk = w >> 1;
    const i32x4 srd_wo = make_srd(ws + WS_WO + (size_t)cgp * WOIMG, WOIMG);
    auto piece_wo = [&](int p) { dma16(srd_wo, vo_x, p * 1024, lds0 + wo_lds(p)); };
    const __amdgpu_buffer_rsrc_t rpart = __builtin_amdgcn_make_buffer_rsrc(ws + WS_PART + (size_t)r * 16 * 8 * 18 * 1024, 0, 16 * 8 * 18 * 1024, 0x00020000);
    float own[11];
    u32x4 pl[5][4];

    f32x16 o0, o1, sX, sY; bf16x8 kA, kB, v0A, v1A, v0B, v1B; unsigned k2A, k2B; u32x4 p0, p1; float e3[4];
    unsigned aK, aK2, aV;
#define SLOT(SC, SN, KN, K2N, CN, OP, V1P, OC, V0C) do { float ea[4], eb[4]; \
        SN = MFMA32(KN, q1[CN], (f32x16){}); SB(); \
        ea[0] = EXP2(SC[0]); ea[1] = EXP2(SC[1]); ea[2] = EXP2(SC[2]); ea[3] = EXP2(SC[3]); \
        p1[2] = cvtpk(e3[0], e3[1]); p1[3] = cvtpk(e3[2], e3[3]); SB(); \
        OP = MFMA32(V1P, __builtin_bit_cast(bf16x8, p1), OP); SB(); \
        eb[0] = EXP2(SC[4]); eb[1] = EXP2(SC[5]); eb[2] = EXP2(SC[6]); eb[3] = EXP2(SC[7]); \
        p0[0] = cvtpk(ea[0], ea[1]); p0[1] = cvtpk(ea[2], ea[3]); SB(); \
        SN = MFMA32K8(__builtin_bit_cast(s16x4, ((u32x2){K2N, kone})), __builtin_bit_cast(s16x4, q2[CN]), SN); SB(); \
        ea[0] = EXP2(SC[8]); ea[1] = EXP2(SC[9]); ea[2] = EXP2(SC[10]); ea[3] = EXP2(SC[11]); \
        p0[2] = cvtpk(eb[0], eb[1]); p0[3] = cvtpk(eb[2], eb[3]); SB(); \
        OC = MFMA32(V0C, __builtin_bit_cast(bf16x8, p0), OC); SB(); \
        e3[0] = EXP2(SC[12]); e3[1] = EXP2(SC[13]); e3[2] = EXP2(SC[14]); e3[3] = EXP2(SC[15]); \
        p1[0] = cvtpk(ea[0], ea[1]); p1[1] = cvtpk(ea[2], ea[3]); SB(); } while (0)
#define PAIR(DK, DK2, DV, H0, H1, H2, H3) do { \
        kB = lds_b128(L + aK + 1024); k2B = *(LAS const unsigned*)(L + aK2 + 128); v0B = lds_b128(L + aV + v_mul * 1216); SB(); \
        H0; SLOT(sX, sY, kA, k2A, 1, o1, v1B, o0, v0A); \
        v1B = lds_b128(L + aV + v_mul * 1216 + 608); SB(); \
        H1; SLOT(sY, sX, kB, k2B, 0, o0, v1A, o1, v0A); \
        aK += (DK); aK2 += (DK2); aV += v_mul * (DV); \
        kA = lds_b128(L + aK); k2A = *(LAS const unsigned*)(L + aK2); v0A = lds_b128(L + aV); SB(); \
        H2; SLOT(sX, sY, kB, k2B, 1, o1, v1A, o0, v0B); \
        v1A = lds_b128(L + aV + 608); SB(); \
        H3; SLOT(sY, sX, kA, k2A, 0, o0, v1B, o1, v0B); } while (0)
#define PAIR_IN(H0, H1, H2, H3) PAIR(2048, 256, 2432, H0, H1, H2, H3)
#define PAIR_JUMP(J, H0, H1, H2, H3) PAIR((J) - 6 * 1024, (J) - 6 * 128, (J) - 6 * 1216, H0, H1, H2, H3)
#define NOP_ ((void)0)
#define ATT_INIT() do { o0 = (f32x16){}; o1 = (f32x16){}; aK = k1_base + LDS_KV0; aK2 = k2_base + LDS_KV0; aV = v_base + v_mul * LDS_KV0; \
        kA = lds_b128(L + aK); k2A = *(LAS const unsigned*)(L + aK2); v0A = lds_b128(L + aV); v1A = lds_b128(L + aV + 608); v1B = v1A; \
        sX = MFMA32(kA, q1[0], (f32x16){}); sX = MFMA32K8(__builtin_bit_cast(s16x4, ((u32x2){k2A, kone})), __builtin_bit_cast(s16x4, q2[0]), sX); \
        p1 = (u32x4){0u, 0u, 0u, 0u}; e3[0] = e3[1] = e3[2] = e3[3] = 0.f; } while (0)
#define ATT_FIN() do { p1[2] = cvtpk(e3[0], e3[1]); p1[3] = cvtpk(e3[2], e3[3]); o1 = MFMA32(v1B, __builtin_bit_cast(bf16x8, p1), o1); } while (0)
    constexpr int J01 = 0 - LDS_KV0, J12 = KVCH;
    {
        const __amdgpu_buffer_rsrc_t rp = __builtin_amdgcn_make_buffer_rsrc(ws + WS_PART + (size_t)(r * 16 + s) * 8 * 18 * 1024, 0, 8 * 18 * 1024, 0x00020000);
#define KV(C, I) do { piece_kv(C, I); SB(); } while (0)
#define ST(I) do { __builtin_amdgcn_raw_buffer_store_b128(pk[I], rp, vo_x, (w * 18 + (I)) * 1024, 16); SB(); } while (0)
#define WO(P0, I) do { piece_wo(min((P0) + w + 8 * (I), (P0) + 36)); SB(); } while (0)
#define PL(T, SP) do { const int m_ = m0 + ((T) < nmt ? (T) : 0); \
            pl[T][SP] = __builtin_amdgcn_raw_buffer_load_b128(rpart, vo_x, ((((4 * qk + (SP)) * 8 + tgf * 4 + (m_ & 3)) * 9 + (m_ >> 2)) * 2 + pr) * 1024, 16); SB(); } while (0)
#define PL2(A) do { PL((A) >> 2, (A) & 3); PL(((A) + 1) >> 2, ((A) + 1) & 3); } while (0)
        ATT_INIT();
        PAIR_IN(KV(1, 0), KV(1, 1), KV(1, 2), KV(1, 3));
        PAIR_IN(KV(1, 4), ST(0), KV(2, 0), ST(1));
        PAIR_IN(KV(2, 1), ST(2), KV(2, 2), ST(3));
        WAIT_BAR(7);
        PAIR_JUMP(J01, KV(2, 3), ST(4), KV(2, 4), ST(5));
        PAIR_IN(KV(3, 0), ST(6), KV(3, 1), ST(7));
        PAIR_IN(KV(3, 2), ST(8), KV(3, 3), ST(9));
        PAIR_IN(KV(3, 4), ST(10), NOP_, ST(11));
        WAIT_BAR(12);
        PAIR_JUMP(J12, WO(0, 0), ST(12), WO(0, 1), ST(13));
        PAIR_IN(WO(0, 2), ST(14), WO(0, 3), ST(15));
        PAIR_IN(WO(0, 4), ST(16), NOP_, ST(17));
        PAIR_IN(NOP_, NOP_, NOP_, NOP_);
        WAIT_BAR(0);
        if (w == 0 && fresh_lane() == 0) __hip_atomic_store((GAS unsigned*)(flags + FL_P + wgi * 4), tag, RLX_AGENT);
        PAIR_JUMP(J12, WO(37, 0), WO(37, 1), WO(37, 2), WO(37, 3));
        const unsigned pflag = __hip_atomic_load((GAS unsigned*)(flags + FL_P + (r * 16 + (l & 15)) * 4), RLX_AGENT);
        PAIR_IN(WO(37, 4), NOP_, NOP_, NOP_);
        LBAR();
        if (!__all(pflag == tag)) wait_tags(flags, FL_P + (r * 16 + (l & 15)) * 4, true, tag);
#define WP(I, A) do { WO(74, I); PL((A) >> 2, (A) & 3); } while (0)
        PAIR_IN(WP(0, 0), WP(1, 1), WP(2, 2), WP(3, 3));
        PAIR_IN(WP(4, 4), PL2(5), PL2(7), PL2(9));
        PAIR(0, 0, 0, PL2(11), PL2(13), PL2(15), PL2(17));
        PL(4, 3);
        ATT_FIN();
    }
    STAMP(1, 6);
#undef l
    const int lc = fresh_lane();
#define l lc
    for (int attempt = 0;; ++attempt) {
        if (attempt == 1) {
            const unsigned kone = h5 ? 0u : 0x3f803f80u;
            const bool vrow = (l & 31) < 19;
            const unsigned k1_base = 8 * kh * 1024 + l * 16, k2_base = KV_K2 + 8 * kh * 128 + (l & 31) * 4;
            const unsigned v_base = vrow ? KV_V + 8 * kh * 1216 + h5 * 304 + (l & 31) * 16 : LDS_ZERO, v_mul = vrow ? 1u : 0u;
            WAIT_BAR(0);
#pragma unroll
            for (int c = 0; c < 4; ++c)
#pragma unroll
                for (int i = 0; i < 5; ++i) piece_kv(c, i);
            if (w == 0 && l < 40) *(LAS u32x4*)(L + LDS_ZERO + l * 16) = (u32x4){0u, 0u, 0u, 0u};
            WAIT_BAR(0);
            float m[2] = {-INFINITY, -INFINITY};
            for (int i = 0; i < 32; ++i) {
                const unsigned to = (i >> 3) == 0 ? LDS_KV0 : ((i >> 3) - 1) * KVCH;
                const bf16x8 k1 = lds_b128(L + k1_base + to + (i & 7) * 1024);
                const u32x2 k2 = {*(LAS const unsigned*)(L + k2_base + to + (i & 7) * 128), kone};
#pragma unroll
                for (int c = 0; c < 2; ++c) {
                    const u32x2 qz = {q2[c][0], 0u};
                    f32x16 sc = MFMA32(k1, q1[c], (f32x16){});
                    sc = MFMA32K8(__builtin_bit_cast(s16x4, k2), __builtin_bit_cast(s16x4, qz), sc);
#pragma unroll
                    for (int e = 0; e < 16; ++e) m[c] = fmaxf(m[c], sc[e]);
                }
            }
            LAS float* mx = (LAS float*)(L + LDS_MX);
#pragma unroll
            for (int c = 0; c < 2; ++c) { m[c] = half_max(m[c]); if (l < 32) mx[(w * 2 + c) * 32 + l] = m[c]; }
            LBAR();
#pragma unroll
            for (int c = 0; c < 2; ++c) {
                const float mm = fmaxf(m[c], mx[((w ^ 2) * 2 + c) * 32 + (l & 31)]);
                const float chi = rbf(mm), clo = mm - chi;
                q2[c][1] = h5 ? 0u : cvtpk(-chi, -clo);
            }
            o0 = (f32x16){}; o1 = (f32x16){};
            for (int i = 0; i < 32; ++i) {
                const unsigned to = (i >> 3) == 0 ? LDS_KV0 : ((i >> 3) - 1) * KVCH;
                const bf16x8 k1 = lds_b128(L + k1_base + to + (i & 7) * 1024);
                const u32x2 k2 = {*(LAS const unsigned*)(L + k2_base + to + (i & 7) * 128), kone};
                const unsigned va = v_base + v_mul * (to + (i & 7) * 1216);
                const bf16x8 v0 = lds_b128(L + va), v1 = lds_b128(L + va + 608);
#pragma unroll
                for (int c = 0; c < 2; ++c) {
                    f32x16 sc = MFMA32(k1, q1[c], (f32x16){});
                    sc = MFMA32K8(__builtin_bit_cast(s16x4, k2), __builtin_bit_cast(s16x4, q2[c]), sc);
                    u32x4 pa, pb;
#pragma unroll
                    for (int e = 0; e < 4; ++e) { pa[e] = cvtpk(EXP2(sc[2 * e]), EXP2(sc[2 * e + 1])); pb[e] = cvtpk(EXP2(sc[8 + 2 * e]), EXP2(sc[9 + 2 * e])); }
                    if (c == 0) { o0 = MFMA32(v0, __builtin_bit_cast(bf16x8, pa), o0); o0 = MFMA32(v1, __builtin_bit_cast(bf16x8, pb), o0); }
                    else { o1 = MFMA32(v0, __builtin_bit_cast(bf16x8, pa), o1); o1 = MFMA32(v1, __builtin_bit_cast(bf16x8, pb), o1); }
                }
            }
        }
        LBAR();
        if (attempt == 0) {
            piece_wo(111 + w); if (w == 0) piece_wo(119);
        } else {
#pragma unroll
            for (int q = 0; q < 15; ++q) piece_wo(w * 15 + q);
        }
        LAS float* red = (LAS float*)(L + LDS_RED);
#pragma unroll
        for (int e = 0; e < 11; ++e) { red[(w * 11 + e) * 64 + l] = kh ? o0[e] : o1[e]; own[e] = kh ? o1[e] : o0[e]; }
        LBAR();
#pragma unroll
        for (int e = 0; e < 11; ++e) own[e] += red[((w ^ 2) * 11 + e) * 64 + l];
        const float lsum = half_lo(own[10]);
        if (!(lsum >= 8.67e-19f && lsum < 3.0e38f) && attempt == 0) *(LAS unsigned*)(L + LDS_CTL) = 1u;
        const float rl = 1.0f / lsum;
        LAS char* ot = L + LDS_OT + (32 * (2 * cp + kh) + (l & 31)) * 80 + 40 * hh;
        *(LAS u32x2*)(ot + 8 * h5) = (u32x2){cvtpk(own[0] * rl, own[1] * rl), cvtpk(own[2] * rl, own[3] * rl)};
        *(LAS u32x2*)(ot + 16 + 8 * h5) = (u32x2){cvtpk(own[4] * rl, own[5] * rl), cvtpk(own[6] * rl, own[7] * rl)};
        if (h5 == 0) *(LAS u32x2*)(ot + 32) = (u32x2){cvtpk(own[8] * rl, own[9] * rl), 0u};
        LBAR();
        const unsigned isbad = *(LAS const unsigned*)(L + LDS_CTL);
        if (!(isbad && attempt == 0)) break;
    }
    STAMP(1, 7);
    {
        const __amdgpu_buffer_rsrc_t rw = __builtin_amdgcn_make_buffer_rsrc(ws + WS_O + (size_t)(r * 16 + s) * 128 * 80, 0, 128 * 80, 0x00020000);
        __builtin_amdgcn_raw_buffer_store_b128(*(LAS const u32x4*)(L + LDS_OT + w * 1024 + l * 16), rw, l * 16, w * 1024, 16);
        if (w < 2) __builtin_amdgcn_raw_buffer_store_b128(*(LAS const u32x4*)(L + LDS_OT + (8 + w) * 1024 + l * 16), rw, l * 16, (8 + w) * 1024, 16);
    }
    {
        const int tok = l & 15, g4 = l >> 4, q4 = (l >> 2) & 3, p4 = l & 3;
        u32x2 xv[3];
#pragma unroll
        for (int u = 0; u < 3; ++u) {
            const int tt = min(qk + 4 * u, 2 * nmt - 1);
            xv[u] = *(const u32x2*)(x + (size_t)(128 * r + 64 * tgf + 32 * pr + 16 * (tt & 1) + tok) * DM + 16 * (m0 + (tt >> 1)) + 4 * g4);
        }
        SB();
        STAMP(1, 8);
        const __amdgpu_buffer_rsrc_t ro = __builtin_amdgcn_make_buffer_rsrc(ws + WS_O + (size_t)r * 16 * 128 * 80, 0, 16 * 128 * 80, 0x00020000);
        u32x4 bf[5][2];
        unsigned pend = 0x3ffu;
        for (unsigned spins = 0;; ++spins) {
#pragma unroll
            for (int i = 0; i < 5; ++i) {
                const int kp = 32 * (5 * qk + i) + 8 * g4, sp = kp / 40, d = kp - 40 * sp;
#pragma unroll
                for (int nn = 0; nn < 2; ++nn)
                    if (pend & (1u << (2 * i + nn))) bf[i][nn] = __builtin_amdgcn_raw_buffer_load_b128(ro, (sp * 128 + 64 * tgf + 32 * pr + 16 * nn + tok) * 80 + d * 2, 0, 16);
            }
#pragma unroll
            for (int i = 0; i < 5; ++i)
#pragma unroll
                for (int nn = 0; nn < 2; ++nn)
                    if ((pend & (1u << (2 * i + nn))) && __all(bf[i][nn][0] != O_EMPTY && bf[i][nn][1] != O_EMPTY && bf[i][nn][2] != O_EMPTY && bf[i][nn][3] != O_EMPTY)) pend &= ~(1u << (2 * i + nn));
            if (pend == 0u) break;
            if (spins > (1u << 17)) { __hip_atomic_store((GAS unsigned*)(flags + FL_TMO), tag, RLX_AGENT); break; }
            __builtin_amdgcn_s_sleep(8);
        }
        f32x4 fa[5][2];
#pragma unroll
        for (int t = 0; t < 5; ++t) {
            fa[t][0] = (f32x4){0.f, 0.f, 0.f, 0.f}; fa[t][1] = (f32x4){0.f, 0.f, 0.f, 0.f};
#pragma unroll
            for (int sp = 0; sp < 4; ++sp) {
                const u32x4 p = pl[t][sp];
                fa[t][0][0] += bflo(p[0]); fa[t][0][1] += bfhi(p[0]); fa[t][0][2] += bflo(p[1]); fa[t][0][3] += bfhi(p[1]);
                fa[t][1][0] += bflo(p[2]); fa[t][1][1] += bfhi(p[2]); fa[t][1][2] += bflo(p[3]); fa[t][1][3] += bfhi(p[3]);
            }
        }
        WAIT_BAR(0);
        STAMP(1, 9);
        const unsigned ao = (4 * g4 + q4) * 64 + 8 * (p4 & 1);
#pragma unroll
        for (int t = 0; t < 5; ++t)
            if (t < nmt) {
#pragma unroll
                for (int i = 0; i < 5; ++i) {
                    const int p = (t >> 1) * 40 + (5 * qk + i) * 2;
                    const unsigned sw = ao + 16 * ((2 * (t & 1) + (p4 >> 1)) ^ (2 * (g4 & 1)));
                    const s16x4 lo = lds_tr(L + wo_lds(p) + sw), hi = lds_tr(L + wo_lds(p + 1) + sw);
                    const bf16x8 a = __builtin_shufflevector(lo, hi, 0, 1, 2, 3, 4, 5, 6, 7);
                    fa[t][0] = MFMA16(a, __builtin_bit_cast(bf16x8, bf[i][0]), fa[t][0]); fa[t][1] = MFMA16(a, __builtin_bit_cast(bf16x8, bf[i][1]), fa[t][1]);
                }
            }
        STAMP(1, 10);
        LBAR();
        LAS f32x4* ex = (LAS f32x4*)L;
#pragma unroll
        for (int t = 0; t < 5; ++t) { ex[(w * 10 + 2 * t) * 64 + l] = fa[t][0]; ex[(w * 10 + 2 * t + 1) * 64 + l] = fa[t][1]; }
        LBAR();
#pragma unroll
        for (int u = 0; u < 3; ++u) {
            const int tt = qk + 4 * u;
            if (tt < 2 * nmt) {
                f32x4 v = ex[((0 * 2 + pr) * 10 + tt) * 64 + l];
#pragma unroll
                for (int k = 1; k < 4; ++k) v += ex[((k * 2 + pr) * 10 + tt) * 64 + l];
                const size_t off = (size_t)(128 * r + 64 * tgf + 32 * pr + 16 * (tt & 1) + tok) * DM + 16 * (m0 + (tt >> 1)) + 4 * g4;
                *(u32x2*)(out + off) = (u32x2){cvtpk(v[0] + bflo(xv[u][0]), v[1] + bfhi(xv[u][0])), cvtpk(v[2] + bflo(xv[u][1]), v[3] + bfhi(xv[u][1]))};
            }
        }
    }
#undef l
#undef h5
    STAMP(1, 11); STAMP_END(1);
    asm volatile("fused_block_code_end:");
}

extern "C" void kernel_launch(void* const* d_in, const int* in_sizes, int n_in, void* d_out, int out_size, void* d_ws, size_t ws_size, hipStream_t stream) {
    const u16* x = (const u16*)d_in[0]; const u16* wq = (const u16*)d_in[1]; const u16* wk = (const u16*)d_in[2]; const u16* wv = (const u16*)d_in[3];
    const u16* wo = (const u16*)d_in[4]; const u16* wg = (const u16*)d_in[6]; const u16* wu = (const u16*)d_in[7]; const u16* wd = (const u16*)d_in[8];
    fused_block<<<256, 512, 0, stream>>>(x, wq, wk, wv, wo, wg, wu, wd, (char*)d_ws, (u16*)d_out);
}
```

```cpp
#include <hip/hip_runtime.h>
#include <hip/hip_bf16.h>
#include <stdint.h>

typedef unsigned short u16;
typedef short bf16x8 __attribute__((ext_vector_type(8)));
typedef short s16x4 __attribute__((ext_vector_type(4)));
typedef float f32x4 __attribute__((ext_vector_type(4)));
typedef float f32x16 __attribute__((ext_vector_type(16)));
typedef int i32x4 __attribute__((ext_vector_type(4)));
typedef unsigned u32x4 __attribute__((ext_vector_type(4)));
typedef unsigned u32x2 __attribute__((ext_vector_type(2)));
typedef __bf16 bf16x2_t __attribute__((ext_vector_type(2)));
typedef float f32x2 __attribute__((ext_vector_type(2)));
typedef short v4i16_t __attribute__((ext_vector_type(4)));
extern "C" __device__ unsigned long long amd_dispatch_id() __asm("llvm.amdgcn.dispatch.id");
#define LAS __attribute__((address_space(3)))
#define GAS __attribute__((address_space(1)))
#define DI __device__ __forceinline__

constexpr int SEQ = 2048, DM = 576, NH = 32, DH = 18, FF = 4608;

constexpr int KVCH = 37888;
constexpr int KV_V = 16384, KV_K2 = 35840;
constexpr int WOIMG = 120 * 1024;
constexpr size_t WS_FLAGS = 0;
constexpr size_t WS_XN    = 16384;
constexpr size_t WS_WQX   = WS_XN + (size_t)SEQ * DM * 2;
constexpr size_t WS_WO    = WS_WQX + (size_t)DM * 1024 * 2;
constexpr size_t WS_KV    = WS_WO + (size_t)8 * WOIMG;
constexpr size_t WS_O     = WS_KV + 4 * KVCH;
constexpr size_t WS_PART  = WS_O + (size_t)256 * 128 * 80;
constexpr size_t WS_END   = WS_PART + (size_t)256 * 8 * 18 * 1024;
constexpr int FL_PRO = 0, FL_P = 1024, FL_KV = 2048, FL_TMO = 8192;
constexpr unsigned O_EMPTY = 0xffffffffu;

DI float bflo(unsigned v) { return __uint_as_float(v << 16); }
DI float bfhi(unsigned v) { return __uint_as_float(v & 0xffff0000u); }
DI unsigned cvtpk(float lo, float hi) { return __builtin_bit_cast(unsigned, __builtin_convertvector((f32x2){lo, hi}, bf16x2_t)); }
DI float rbf(float f) { return bflo(cvtpk(f, f)); }
DI i32x4 make_srd(const void* p, unsigned bytes) {
    unsigned long long a = (unsigned long long)p; i32x4 s;
    s.x = __builtin_amdgcn_readfirstlane((unsigned)a); s.y = __builtin_amdgcn_readfirstlane((unsigned)(a >> 32) & 0xffffu);
    s.z = (int)bytes; s.w = 0x00020000; return s;
}
DI void dma16(i32x4 srd, unsigned voff, unsigned soff, unsigned lds_addr) {
    unsigned keep;
    asm volatile("s_mov_b32 %0, m0\n\ts_mov_b32 m0, %4\n\ts_nop 0\n\tbuffer_load_dwordx4 %1, %2, %3 offen lds\n\ts_mov_b32 m0, %0"
                 : "=&s"(keep) : "v"(voff), "s"(srd), "s"(soff), "s"(lds_addr) : "memory");
}
DI void dma16_sc1(i32x4 srd, unsigned voff, unsigned soff, unsigned lds_addr) {
    unsigned keep;
    asm volatile("s_mov_b32 %0, m0\n\ts_mov_b32 m0, %4\n\ts_nop 0\n\tbuffer_load_dwordx4 %1, %2, %3 offen sc1 lds\n\ts_mov_b32 m0, %0"
                 : "=&s"(keep) : "v"(voff), "s"(srd), "s"(soff), "s"(lds_addr) : "memory");
}
DI void dma4(i32x4 srd, unsigned voff, unsigned soff, unsigned lds_addr) {
    unsigned keep;
    asm volatile("s_mov_b32 %0, m0\n\ts_mov_b32 m0, %4\n\ts_nop 0\n\tbuffer_load_dword %1, %2, %3 offen lds\n\ts_mov_b32 m0, %0"
                 : "=&s"(keep) : "v"(voff), "s"(srd), "s"(soff), "s"(lds_addr) : "memory");
}
#define WAIT_BAR(N) asm volatile("s_waitcnt vmcnt(" #N ") lgkmcnt(0)\n\ts_barrier" ::: "memory")
#define LBAR() asm volatile("s_waitcnt lgkmcnt(0)\n\ts_barrier" ::: "memory")
#define SB() __builtin_amdgcn_sched_barrier(0)
#define FENCE() asm volatile("" ::: "memory")
#define PIN4(a) asm volatile("" : "+v"((a)[0]), "+v"((a)[1]), "+v"((a)[2]), "+v"((a)[3]))
#define MFMA16(a, b, c) __builtin_amdgcn_mfma_f32_16x16x32_bf16(a, b, c, 0, 0, 0)
#define MFMA32(a, b, c) __builtin_amdgcn_mfma_f32_32x32x16_bf16(a, b, c, 0, 0, 0)
#define MFMA32K8(a, b, c) __builtin_amdgcn_mfma_f32_32x32x8bf16_1k(a, b, c, 0, 0, 0)
#define EXP2(x) __builtin_amdgcn_exp2f(x)
DI s16x4 lds_tr(LAS const char* p) { return __builtin_bit_cast(s16x4, __builtin_amdgcn_ds_read_tr16_b64_v4i16((LAS v4i16_t*)p)); }
DI bf16x8 lds_b128(LAS const char* p) { return *(LAS const bf16x8*)p; }
DI bf16x8 lds_afrag(LAS const char* p) { const s16x4 lo = lds_tr(p), hi = lds_tr(p + 2048); return __builtin_shufflevector(lo, hi, 0, 1, 2, 3, 4, 5, 6, 7); }
#define RLX_AGENT __ATOMIC_RELAXED, __HIP_MEMORY_SCOPE_AGENT
template <int X> DI float xor_lane(float v) { return __int_as_float(__builtin_amdgcn_ds_swizzle(__float_as_int(v), (X << 10) | 0x1f)); }
DI float half_sum(float v) { auto r = __builtin_amdgcn_permlane32_swap(__float_as_uint(v), __float_as_uint(v), false, false); return __uint_as_float(r[0]) + __uint_as_float(r[1]); }
DI float half_max(float v) { auto r = __builtin_amdgcn_permlane32_swap(__float_as_uint(v), __float_as_uint(v), false, false); return fmaxf(__uint_as_float(r[0]), __uint_as_float(r[1])); }
DI float half_lo(float v) { auto r = __builtin_amdgcn_permlane32_swap(__float_as_uint(v), __float_as_uint(v), false, false); return __uint_as_float(r[0]); }
template <int CTRL, int ROWS> DI float dpp_add(float v) { return v + __int_as_float(__builtin_amdgcn_update_dpp(0, __float_as_int(v), CTRL, ROWS, 0xf, true)); }
DI float wave_sum_dpp(float v) {
    v = dpp_add<0x111, 0xf>(v); v = dpp_add<0x112, 0xf>(v); v = dpp_add<0x114, 0xf>(v); v = dpp_add<0x118, 0xf>(v);
    v = dpp_add<0x142, 0xa>(v); v = dpp_add<0x143, 0xc>(v);
    return __int_as_float(__builtin_amdgcn_readlane(__float_as_int(v), 63));
}
DI float wave_sum(float v) { v += xor_lane<1>(v); v += xor_lane<2>(v); v += xor_lane<4>(v); v += xor_lane<8>(v); v += xor_lane<16>(v); return half_sum(v); }
DI float wave_max(float v) { v = fmaxf(v, xor_lane<1>(v)); v = fmaxf(v, xor_lane<2>(v)); v = fmaxf(v, xor_lane<4>(v)); v = fmaxf(v, xor_lane<8>(v)); v = fmaxf(v, xor_lane<16>(v)); return half_max(v); }
DI int fresh_lane() { int t; asm volatile("v_mbcnt_lo_u32_b32 %0, -1, 0\n\tv_mbcnt_hi_u32_b32 %0, -1, %0" : "=v"(t)); return t; }
DI void st_agent64(void* p, unsigned lo, unsigned hi) { __hip_atomic_store((GAS unsigned long long*)p, ((unsigned long long)hi << 32) | lo, RLX_AGENT); }
DI void wait_tags(const char* flags, unsigned off, bool use, unsigned tag) {
    for (unsigned spins = 0;; ++spins) {
        const unsigned v = use ? __hip_atomic_load((GAS unsigned*)(flags + off), RLX_AGENT) : tag;
        if (__all(v == tag)) break;
        if (spins > (1u << 19)) { __hip_atomic_store((GAS unsigned*)(flags + FL_TMO), tag, RLX_AGENT); break; }
        __builtin_amdgcn_s_sleep(1);
    }
    FENCE();
}
#define STAMP_DECL
#define STAMP(K, i)
#define STAMP_END(K)

#define DMA345 dma16_sc1
constexpr int ST1 = 49152;
constexpr int LDS_D0 = 0, LDS_D1 = 49152, LDS_H = 86016, LDS_Q1 = 36864, LDS_Q2 = 45056;
constexpr int LDS_PF = 147456;
constexpr int LDS_KM = 46080;
constexpr int LDS_D2 = 126976;
constexpr int LDS_KV0 = 125952;
constexpr int LDS_OT = 113664;
constexpr int LDS_MX = LDS_OT;
constexpr int LDS_ZERO = LDS_OT + 10240;
constexpr int LDS_CTL = LDS_ZERO + 640;
constexpr int LDS_RED = KVCH + 9 * 1024;
constexpr int LDS_BYTES = 163840;
DI unsigned wo_lds(int p) { return p < 37 ? LDS_KV0 + p * 1024 : p < 74 ? (p - 37) * 1024 : p < 111 ? 2 * KVCH + (p - 74) * 1024 : KVCH + (p - 111) * 1024; }

template <int NM, bool FIRST, int ND, int DPG, int AD = 3, bool PIN = false, class Dma>
DI void gemm_step(f32x4 (&acc)[10][4], bf16x8 (&ca)[2], bf16x8 (&cb)[4], bf16x8 (&nb)[4], LAS const char* ap, LAS const char* bp, const Dma& dma) {
    bf16x8 a[10];
#pragma unroll
    for (int n = 0; n < 4; ++n) nb[n] = lds_b128(bp + n * 1024);
    a[0] = lds_afrag(ap); a[1] = lds_afrag(ap + 4096); if (AD > 2) a[2] = lds_afrag(ap + 2 * 4096);
    SB();
    int d = 0;
    if (!FIRST) {
#pragma unroll
        for (int k = 0; k < DPG; ++k) if (d < ND) dma(d++);
#pragma unroll
        for (int n = 0; n < 4; ++n) acc[NM - 2][n] = MFMA16(ca[0], cb[n], acc[NM - 2][n]);
        if (PIN) PIN4(acc[NM - 2]); SB();
#pragma unroll
        for (int k = 0; k < DPG; ++k) if (d < ND) dma(d++);
#pragma unroll
        for (int n = 0; n < 4; ++n) acc[NM - 1][n] = MFMA16(ca[1], cb[n], acc[NM - 1][n]);
        if (PIN) PIN4(acc[NM - 1]); SB();
    }
#pragma unroll
    for (int m = 0; m < NM - 2; ++m) {
        if (m + AD < NM) a[m + AD] = lds_afrag(ap + (m + AD) * 4096);
#pragma unroll
        for (int k = 0; k < DPG; ++k) if (d < ND) dma(d++);
#pragma unroll
        for (int n = 0; n < 4; ++n) acc[m][n] = MFMA16(a[m], nb[n], acc[m][n]);
        if (PIN) PIN4(acc[m]); SB();
    }
    ca[0] = a[NM - 2]; ca[1] = a[NM - 1];
}
template <int NM>
DI void gemm_flush(f32x4 (&acc)[10][4], const bf16x8 (&ca)[2], const bf16x8 (&cb)[4]) {
#pragma unroll
    for (int n = 0; n < 4; ++n) acc[NM - 2][n] = MFMA16(ca[0], cb[n], acc[NM - 2][n]);
#pragma unroll
    for (int n = 0; n < 4; ++n) acc[NM - 1][n] = MFMA16(ca[1], cb[n], acc[NM - 1][n]);
}

__global__ __launch_bounds__(512, 2) void fused_block(const u16* __restrict__ x, const u16* __restrict__ wq, const u16* __restrict__ wk, const u16* __restrict__ wv,
                                                       const u16* __restrict__ wo, const u16* __restrict__ wg, const u16* __restrict__ wu, const u16* __restrict__ wd,
                                                       char* __restrict__ ws, u16* __restrict__ out) {
    __shared__ __attribute__((aligned(1024))) char lds[LDS_BYTES];
    const int tid = threadIdx.x, l = tid & 63;
    const int w = __builtin_amdgcn_readfirstlane(tid >> 6);
    const int b = blockIdx.x, s = 2 * (b & 7) + ((b >> 3) & 1), r = b >> 4, wgi = r * 16 + s;
    const int tg = w >> 2, fg = w & 3;
    constexpr unsigned lds0 = 0;
    LAS char* L = (LAS char*)lds;
    char* flags = ws + WS_FLAGS;
    const unsigned tag = (((unsigned)amd_dispatch_id() + 1u) * 0x9E3779B1u) ^ ((unsigned)((unsigned long long)__builtin_amdgcn_dispatch_ptr() >> 6) * 0x85EBCA77u) ^ 0x5bd1e995u;

    const int dmat = w & 1, dR = (w >> 1) & 1, dcg0 = 5 * (w >> 2);
    const int di = l >> 2, dsig = l & 3;
    const int dmi = dsig ^ (2 * dmat + ((di >> 2) & 1)), dkrow = 8 * (di >> 2) + (di & 3);
    const i32x4 srd_w = make_srd(dmat ? wu : wg, DM * FF * 2);
    const i32x4 srd_q = make_srd(ws + WS_WQX, DM * 1024 * 2);
    const i32x4 srd_x = make_srd(ws + WS_XN, SEQ * DM * 2);
    const unsigned vo_w = dkrow * (FF * 2) + 16 * dmi, vo_q = dkrow * 2048 + 16 * dmi, vo_x = l * 16;
    const unsigned dlds = lds0 + dR * 2048 + dmat * 1024;
    auto pieceW = [&](int j, unsigned slot, int cg) { dma16(srd_w, vo_w, (32 * j + 4 * dR) * (FF * 2) + 288 * s * 2 + cg * 64, dlds + slot + cg * 4096); };
    auto pieceQ = [&](int j, unsigned slot) { dma16_sc1(srd_q, vo_q, (32 * j + 4 * dR) * 2048 + (64 * s + 32 * dmat) * 2, dlds + slot + 9 * 4096); };
    auto pieceX = [&](int j, unsigned slot, int n) { dma16_sc1(srd_x, vo_x, ((r * 18 + j) * 8 + n) * 1024, lds0 + slot + 40960 + n * 1024); };
    auto piece1k = [&](int j, unsigned slot, int i, int kind) {
        if (i < 3) { if (kind & 1) pieceW(j, slot, dcg0 + i); }
        else if (i == 3) { if (w == 3) { if (kind & 2) pieceQ(j, slot); } else if (kind & 1) pieceW(j, slot, dcg0 + 3); }
        else if (i == 4) {
            if (w < 3) { if (kind & 1) pieceW(j, slot, 4); } else if (w == 3) { if (kind & 2) pieceX(j, slot, 7); }
            else if (w < 7) { if (kind & 2) pieceQ(j, slot); } else if (kind & 1) pieceW(j, slot, 3);
        } else { if (w == 7) { if (kind & 1) pieceW(j, slot, 4); } else if (kind & 2) pieceX(j, slot, w); }
    };
    auto piece2 = [&](int j, unsigned slot, int i) {
        const int ii = (w >= 4 && i == 4) ? 3 : i;
        const unsigned vo_d = ((vo_w & ~63u) >> 3) + ((vo_w & 48u) << 1);
        dma16(make_srd(wd, FF * DM * 2), vo_d, (288 * s + 32 * j + 4 * dR) * (DM * 2) + 16 * dmat + (dcg0 + ii) * 128, dlds + slot + (dcg0 + ii) * 4096);
    };

    STAMP_DECL;
    STAMP(1, 0);
    if (w == 7) {
        unsigned long long pc; unsigned len;
        asm volatile("s_getpc_b64 %0\n\tcode_here_%=:\n\ts_mov_b32 %1, fused_block_code_end-code_here_%=" : "=s"(pc), "=s"(len));
        i32x4 sc; sc.x = (int)(unsigned)pc; sc.y = (int)((unsigned)(pc >> 32) & 0xffffu); sc.z = (int)len; sc.w = 0x00020000;
        dma4(sc, l < 20 ? (unsigned)(((b >> 3) * 20 + l) * 128) : 0x80000000u, 0, lds0 + LDS_PF);
    }
    {
        const u32x4* xs = (const u32x4*)(x + (size_t)(128 * r + 8 * s + w) * DM);
        const u32x4 v0 = xs[l];
        u32x4 v1 = xs[64 + (l & 7)];
        unsigned qv[8];
        {
            const int tq = min(tid, 287), k = 36 * r + (tq >> 3), mat = (tq >> 2) & 1, t = tq & 3;
            const u16* wc = ((s & 1) ? wv : wk) + k * DH;
            const u16* wh = wq + (size_t)k * DM + (2 * s + (t >> 1)) * DH;
#pragma unroll
            for (int e = 0; e < 8; ++e) {
                const int rho = mat * 8 + e, cc = (t == 1 ? 0 : 12) + rho - 4;
                const bool isq = (t & 1) == 0 || rho < 2, isc = !isq && rho >= 4 && cc < (t == 1 ? 12 : 18);
                const u16* p = isc ? wc + cc : wh + ((t & 1) == 0 ? rho : 16 + (rho & 1));
                const unsigned val = *p;
                qv[e] = (isq || isc) ? val : 0u;
            }
        }
        const int cgp = s & 7, wp = 4 * (r * 2 + (s >> 3)) + ((min(max(tid, 192), 447) - 192) >> 6);
        const bool wo_act = tid >= 192 && tid < 448 && wp < 120;
        u32x4 wov;
        {
            const int m0 = cgp < 4 ? 5 * cgp : 20 + 4 * (cgp - 4), wpc = min(wp, 119);
            const int pp = wpc / 40, js = (wpc % 40) >> 1, R = wpc & 1, i = l >> 2, ch = (l & 3) ^ (2 * ((i >> 2) & 1));
            const int kp = 32 * js + 8 * (i >> 2) + (i & 3) + 4 * R, sp = kp / 40, d = kp - 40 * sp, hd = d >= 20 ? 1 : 0, dd = d - 20 * hd;
            const int row = 36 * sp + 18 * hd + (dd < 18 ? dd : 17), col = min(16 * (m0 + 2 * pp) + 8 * ch, DM - 8);
            wov = *(const u32x4*)(wo + (size_t)row * DM + col);
        }
        FENCE();
        STAMP(1, 13);
        if (l >= 8) v1 = (u32x4){0u, 0u, 0u, 0u};
        float sm = 0.f;
#pragma unroll
        for (int e = 0; e < 4; ++e) sm += bflo(v0[e]) + bfhi(v0[e]) + bflo(v1[e]) + bfhi(v1[e]);
        const float mean = wave_sum_dpp(sm) * (1.0f / DM);
        float q = 0.f, q1 = 0.f;
#pragma unroll
        for (int e = 0; e < 4; ++e) { const float a = bflo(v0[e]) - mean, c = bfhi(v0[e]) - mean; q += a * a + c * c; }
#pragma unroll
        for (int e = 0; e < 4; ++e) { const float a = bflo(v1[e]) - mean, c = bfhi(v1[e]) - mean; q1 += a * a + c * c; }
        if (l < 8) q += q1;
        const float m = rbf(mean), var = rbf(wave_sum_dpp(q) * (1.0f / DM));
        const float rs = 1.0f / rbf(sqrtf(rbf(var + 1e-5f)));
        LAS char* xst = L + 2 * ST1;
        {
            u32x4 o;
#pragma unroll
            for (int e = 0; e < 4; ++e) o[e] = cvtpk(rbf(bflo(v0[e]) - m) * rs, rbf(bfhi(v0[e]) - m) * rs);
            *(LAS u32x4*)(xst + l * 128 + w * 16) = o;
#pragma unroll
            for (int e = 0; e < 4; ++e) o[e] = cvtpk(rbf(bflo(v1[e]) - m) * rs, rbf(bfhi(v1[e]) - m) * rs);
            if (l < 8) *(LAS u32x4*)(xst + (64 + l) * 128 + w * 16) = o;
        }
        asm volatile("" : "+v"(wov));
        LBAR();
        STAMP(1, 14);
        if (w < 7) {
            const __amdgpu_buffer_rsrc_t rx = __builtin_amdgcn_make_buffer_rsrc(ws + WS_XN, 0, SEQ * DM * 2, 0x00020000);
#pragma unroll
            for (int u = 0; u < 2; ++u) {
                const int i = tid + 448 * u, c = i >> 3, t = i & 7;
                const u32x4 v = *(LAS const u32x4*)(xst + min(i, 575) * 16);
                __builtin_amdgcn_raw_buffer_store_b128(v, rx, i < 576 ? (unsigned)(((r * 18 + (c >> 2)) * 8 + (s >> 1)) * 1024 + ((c & 3) * 16 + 8 * (s & 1) + t) * 16) : 0xfffffff0u, 0, 16);
            }
            {
                u32x4 o; o[0] = qv[0] | (qv[1] << 16); o[1] = qv[2] | (qv[3] << 16); o[2] = qv[4] | (qv[5] << 16); o[3] = qv[6] | (qv[7] << 16);
                const __amdgpu_buffer_rsrc_t rq = __builtin_amdgcn_make_buffer_rsrc(ws + WS_WQX, 0, DM * 1024 * 2, 0x00020000);
                __builtin_amdgcn_raw_buffer_store_b128(o, rq, tid < 288 ? (unsigned)(((36 * r + (tid >> 3)) * 128 + s * 8 + (tid & 7)) * 16) : 0xfffffff0u, 0, 16);
            }
            WAIT_BAR(0);
        } else LBAR();
        STAMP(1, 15);
        if (tid == 0) __hip_atomic_store((GAS unsigned*)(flags + FL_PRO + wgi * 4), tag, RLX_AGENT);
#pragma unroll
        for (int i = 0; i < 6; ++i) piece1k(0, 0, i, 1);
#pragma unroll
        for (int i = 0; i < 6; ++i) piece1k(1, ST1, i, 1);
        if (w == 7) {
            const unsigned o = (l & 7) < 5 ? (unsigned)(((l >> 3) * 16 + r) * (FF * 2) + ((288 * s * 2) & ~127) + (l & 7) * 128) : 0x80000000u;
#pragma unroll
            for (int g = 0; g < 2; ++g) { dma4(make_srd(wg, DM * FF * 2), o, g * 128 * (FF * 2), lds0 + LDS_PF); dma4(srd_w, o, g * 128 * (FF * 2), lds0 + LDS_PF); }
        }
        if (w < 7) {
            const __amdgpu_buffer_rsrc_t rw = __builtin_amdgcn_make_buffer_rsrc(ws + WS_WO, 0, 8 * WOIMG, 0x00020000);
            __builtin_amdgcn_raw_buffer_store_b128(wov, rw, wo_act ? (unsigned)(cgp * WOIMG + wp * 1024 + l * 16) : 0xfffffff0u, 0, 16);
            const __amdgpu_buffer_rsrc_t ro = __builtin_amdgcn_make_buffer_rsrc(ws + WS_O + (size_t)wgi * 128 * 80, 0, 128 * 80, 0x00020000);
            const u32x4 e = (u32x4){O_EMPTY, O_EMPTY, O_EMPTY, O_EMPTY};
            __builtin_amdgcn_raw_buffer_store_b128(e, ro, tid * 16, 0, 16);
            __builtin_amdgcn_raw_buffer_store_b128(e, ro, (tid + 448) * 16, 0, 16);
        }
        STAMP(1, 12);
        if (w == 0) wait_tags(flags, FL_PRO + (l < 16 ? (r * 16 + l) * 4 : ((l - 16) * 16 + s) * 4), l < 32, tag);
        asm volatile("s_barrier" ::: "memory");
#pragma unroll
        for (int i = 3; i < 6; ++i) piece1k(0, 0, i, 2);
#pragma unroll
        for (int i = 3; i < 6; ++i) piece1k(1, ST1, i, 2);
    }
    const int lg = fresh_lane();
    const unsigned a_off = ((lg & 3) >> 1) * 1024 + 16 * (4 * (4 * (lg >> 4) + ((lg >> 2) & 3)) + (fg ^ (2 * ((lg & 3) >> 1) + ((lg >> 4) & 1)))) + 8 * (lg & 1);

    f32x4 acc[10][4];
#pragma unroll
    for (int m = 0; m < 10; ++m)
#pragma unroll
        for (int n = 0; n < 4; ++n) acc[m][n] = (f32x4){0.f, 0.f, 0.f, 0.f};
    bf16x8 ca[2], bA[4], bB[4];
    {
        const unsigned bo = 40960 + tg * 4096 + lg * 16;
        WAIT_BAR(0);
        STAMP(1, 1);
        const bool q3 = w == 3, w4 = w < 3 || w == 7, x4 = w == 3, w5 = w == 7;
        const i32x4 srd3 = q3 ? srd_q : srd_w, srd4 = w4 ? srd_w : x4 ? srd_x : srd_q, srd5 = w5 ? srd_w : srd_x;
        const unsigned vo3 = q3 ? vo_q : vo_w, vo4 = w4 ? vo_w : x4 ? vo_x : vo_q, vo5 = w5 ? vo_w : vo_x;
        const unsigned soW = 4 * dR * (FF * 2) + 288 * s * 2, soQ = 4 * dR * 2048 + (64 * s + 32 * dmat) * 2, soX = r * 18 * 8192;
        const unsigned stW = 32 * FF * 2, stQ = 32 * 2048, stX = 8192;
        const unsigned so3 = q3 ? soQ : soW + (dcg0 + 3) * 64, st3 = q3 ? stQ : stW, lo3 = q3 ? dlds + 9 * 4096 : dlds + (dcg0 + 3) * 4096;
        const int cg4 = w < 3 ? 4 : 3;
        const unsigned so4 = w4 ? soW + cg4 * 64 : x4 ? soX + 7 * 1024 : soQ, st4 = w4 ? stW : x4 ? stX : stQ, lo4 = w4 ? dlds + cg4 * 4096 : x4 ? lds0 + 40960 + 7 * 1024 : dlds + 9 * 4096;
        const unsigned so5 = w5 ? soW + 4 * 64 : soX + w * 1024, st5 = w5 ? stW : stX, lo5 = w5 ? dlds + 4 * 4096 : lds0 + 40960 + w * 1024;
        unsigned vo_pf;
        { const int lp = fresh_lane(); vo_pf = (lp & 7) < 5 ? (unsigned)(((lp >> 3) * 16 + r) * (FF * 2) + ((288 * s * 2) & ~127) + (lp & 7) * 128) : 0x80000000u; }
        auto stage1 = [&](int j, unsigned slot, int i) {
            if (i < 3) dma16(srd_w, vo_w, soW + (dcg0 + i) * 64 + j * stW, dlds + (dcg0 + i) * 4096 + slot);
            else if (i == 3) DMA345(srd3, vo3, so3 + j * st3, lo3 + slot);
            else if (i == 4) DMA345(srd4, vo4, so4 + j * st4, lo4 + slot);
            else if (i == 5) DMA345(srd5, vo5, so5 + j * st5, lo5 + slot);
            else if (w >= 6) {
                const int t = j - 2;
                if (t == 6) { const int lp = fresh_lane(); vo_pf = (w == 7 && (lp & 15) < 9) ? (unsigned)((288 * s + (lp >> 4) * 16 + r) * (DM * 2) + (lp & 15) * 128) : 0x80000000u; }
                const unsigned so = t < 6 ? ((t & 1) ? 0x70000000u : (unsigned)(2 + (t >> 1)) * 128 * (FF * 2)) : t < 11 ? (unsigned)(t - 6) * 64 * (DM * 2) : 0x70000000u;
                dma4(t < 6 ? srd_w : make_srd(wd, FF * DM * 2), vo_pf, so, lds0 + LDS_PF);
            }
        };
        gemm_step<10, true, 7, 1>(acc, ca, bB, bA, L + a_off, L + bo, [&](int i) { stage1(2, 2 * ST1, i); });
        unsigned slot = ST1, slot2 = 0;
        if (w >= 6) { WAIT_BAR(7); } else { WAIT_BAR(6); }
        gemm_step<10, false, 7, 1>(acc, ca, bA, bB, L + slot + a_off, L + slot + bo, [&](int i) { stage1(3, slot2, i); });
        slot = 2 * ST1; slot2 = ST1;
#pragma unroll 1
        for (int j = 2; j < 16; j += 2) {
            if (w >= 6) { WAIT_BAR(8); } else { WAIT_BAR(6); }
            gemm_step<10, false, 7, 1>(acc, ca, bB, bA, L + slot + a_off, L + slot + bo, [&](int i) { stage1(j + 2, slot2, i); });
            slot = (slot == 2 * ST1) ? 0 : slot + ST1; slot2 = (slot2 == 2 * ST1) ? 0 : slot2 + ST1;
            if (w >= 6) { WAIT_BAR(8); } else { WAIT_BAR(6); }
            gemm_step<10, false, 7, 1>(acc, ca, bA, bB, L + slot + a_off, L + slot + bo, [&](int i) { stage1(j + 3, slot2, i); });
            slot = (slot == 2 * ST1) ? 0 : slot + ST1; slot2 = (slot2 == 2 * ST1) ? 0 : slot2 + ST1;
        }
#pragma unroll
        for (int n = 0; n < 4; ++n) bA[n] = bB[n];
#pragma unroll 1
        for (int j = 16; j < 18; ++j) {
            if (j == 16 && w >= 6) { WAIT_BAR(8); } else if (j == 16) { WAIT_BAR(6); } else { WAIT_BAR(5); }
            gemm_step<10, false, 5, 1, 3, true>(acc, ca, bA, bB, L + slot + a_off, L + slot + bo, [&](int i) { piece2(j - 16, j == 16 ? LDS_D0 : LDS_D1, i); });
#pragma unroll
            for (int n = 0; n < 4; ++n) bA[n] = bB[n];
            slot += ST1;
        }
        gemm_flush<10>(acc, ca, bA);
        SB(); FENCE();
    }
    WAIT_BAR(0);
    STAMP(1, 2);
    float kn2 = 0.f;
    if ((fg & 1) && tg == (s >> 3)) {
        const int l = fresh_lane(), tok = l & 15, g4 = l >> 4;
        const int e = s >> 1;
        f32x4 cv = acc[9][0];
        if ((e & 3) == 1) cv = acc[9][1]; else if ((e & 3) == 2) cv = acc[9][2]; else if ((e & 3) == 3) cv = acc[9][3];
        const int c0 = (fg == 1 ? 0 : 12) + 4 * (g4 - 1);
        const bool act = g4 >= 1 && c0 < 18;
        const unsigned p0 = cvtpk(cv[0], cv[1]), p1 = cvtpk(cv[2], cv[3]);
        const int kt = 4 * r + (e >> 1);
        char* kvb = ws + WS_KV + ((kt >> 3) & 3) * KVCH; const int lt = 8 * (kt >> 5) + (kt & 7);
        if ((s & 1) == 0) {
            const int key = 16 * (e & 1) + tok;
            if (act) {
                if (c0 < 16) st_agent64(kvb + lt * 1024 + ((c0 >> 3) * 32 + key) * 16 + (c0 & 4) * 2, p0, p1);
                else __hip_atomic_store((GAS unsigned*)(kvb + KV_K2 + lt * 128 + key * 4), p0, RLX_AGENT);
            }
            float n2 = 0.f;
            if (act) { n2 = bflo(p0) * bflo(p0) + bfhi(p0) * bfhi(p0); if (c0 < 16) n2 += bflo(p1) * bflo(p1) + bfhi(p1) * bfhi(p1); }
            kn2 = n2;
        } else {
            if (fg == 1 && l < 2) {
                char* op = kvb + KV_V + lt * 1216 + (e & 1) * 608 + l * 304 + 18 * 16;
                st_agent64(op, 0x3f803f80u, 0x3f803f80u); st_agent64(op + 8, 0x3f803f80u, 0x3f803f80u);
            }
            const int hv = (tok >> 2) & 1, jj = 4 * (tok >> 3) + (tok & 3);
            GAS u16* vb = (GAS u16*)(kvb + KV_V + lt * 1216 + (e & 1) * 608 + hv * 304 + jj * 2);
            if (act) {
                __hip_atomic_store(vb + (c0 + 0) * 8, (u16)(p0 & 0xffff), RLX_AGENT);
                __hip_atomic_store(vb + (c0 + 1) * 8, (u16)(p0 >> 16), RLX_AGENT);
                if (c0 < 16) { __hip_atomic_store(vb + (c0 + 2) * 8, (u16)(p1 & 0xffff), RLX_AGENT);
                               __hip_atomic_store(vb + (c0 + 3) * 8, (u16)(p1 >> 16), RLX_AGENT); }
            }
        }
    }
    {
        const int l = fresh_lane(), tok = l & 15, g4 = l >> 4;
#pragma unroll
        for (int cg = 0; cg < 9; ++cg)
#pragma unroll
            for (int pr = 0; pr < 2; ++pr) {
                float h[4];
#pragma unroll
                for (int e = 0; e < 4; ++e) {
                    auto sw = __builtin_amdgcn_permlane32_swap(__float_as_uint(acc[cg][2 * pr][e]), __float_as_uint(acc[cg][2 * pr + 1][e]), false, false);
                    h[e] = __int_as_float(max((int)sw[0], 0)) * __uint_as_float(sw[1]);
                }
                const int n = 4 * tg + 2 * pr + (l >> 5);
                u32x2 o; o[0] = cvtpk(h[0], h[1]); o[1] = cvtpk(h[2], h[3]);
                *(LAS u32x2*)(L + LDS_H + ((8 - cg) * 8 + n) * 1024 + (fg * 16 + tok) * 16 + 8 * (g4 & 1)) = o;
            }
        const float qs = 0.23570226039551584f * 1.4426950408889634f;
        const int hh = fg >> 1;
        unsigned p0[4], p1[4];
#pragma unroll
        for (int n = 0; n < 4; ++n) { p0[n] = cvtpk(acc[9][n][0] * qs, acc[9][n][1] * qs); p1[n] = cvtpk(acc[9][n][2] * qs, acc[9][n][3] * qs); }
        if ((fg & 1) == 0) {
            LAS char* qd = L + LDS_Q1 + (hh * 4 + 2 * tg) * 1024 + ((g4 >> 1) * 32 + tok) * 16 + (g4 & 1) * 8;
#pragma unroll
            for (int n = 0; n < 4; ++n) *(LAS u32x2*)(qd + (n >> 1) * 1024 + (n & 1) * 256) = (u32x2){p0[n], p1[n]};
        } else if (g4 == 0) {
            LAS char* qd = L + LDS_Q2 + (hh * 4 + 2 * tg) * 128 + tok * 4;
#pragma unroll
            for (int n = 0; n < 4; ++n) *(LAS unsigned*)(qd + (n >> 1) * 128 + (n & 1) * 64) = p0[n];
        }
    }
    if ((fg & 1) && tg == (s >> 3)) {
        kn2 += xor_lane<16>(kn2); kn2 = wave_max(half_sum(kn2));
        if (fresh_lane() == 0) *(LAS float*)(L + LDS_KM + (fg == 1 ? 0 : 4)) = kn2;
    }
    LBAR();
    STAMP(1, 3);
    const i32x4 srd_kv = make_srd(ws + WS_KV, 4 * KVCH);
    auto piece_kv = [&](int c, int i) {
        const int p = min(w + 8 * i, 36);
        dma16_sc1(srd_kv, vo_x, c * KVCH + p * 1024, lds0 + (c == 0 ? LDS_KV0 : (c - 1) * KVCH) + p * 1024);
    };
#pragma unroll
    for (int m = 0; m < 9; ++m)
#pragma unroll
        for (int n = 0; n < 4; ++n) acc[m][n] = (f32x4){0.f, 0.f, 0.f, 0.f};
    {
        LAS const char* hb = L + LDS_H + 8 * 8192 + 4 * tg * 1024 + l * 16;
        gemm_step<9, true, 0, 1>(acc, ca, bB, bA, L + LDS_D0 + a_off, hb, [](int) {});
        WAIT_BAR(0);
        if (w == 0 && fresh_lane() == 0) {
            st_agent64(flags + FL_KV + wgi * 16, tag, *(LAS const unsigned*)(L + LDS_KM));
            st_agent64(flags + FL_KV + wgi * 16 + 8, tag, *(LAS const unsigned*)(L + LDS_KM + 4));
        }
        gemm_step<9, false, 5, 2>(acc, ca, bA, bB, L + LDS_D1 + a_off, hb - 1 * 8192, [&](int i) { piece2(2, LDS_D0, i); });
        WAIT_BAR(0);
        gemm_step<9, false, 5, 2>(acc, ca, bB, bA, L + LDS_D0 + a_off, hb - 2 * 8192, [&](int i) { piece2(3, LDS_D1, i); });
        WAIT_BAR(0);
        gemm_step<9, false, 5, 2>(acc, ca, bA, bB, L + LDS_D1 + a_off, hb - 3 * 8192, [&](int i) { piece2(4, LDS_D0, i); });
        WAIT_BAR(0);
        gemm_step<9, false, 10, 2>(acc, ca, bB, bA, L + LDS_D0 + a_off, hb - 4 * 8192, [&](int i) { if (i < 5) piece2(5, LDS_D1, i); else piece2(6, LDS_D2, i - 5); });
        WAIT_BAR(5);
        gemm_step<9, false, 5, 1>(acc, ca, bA, bB, L + LDS_D1 + a_off, hb - 5 * 8192, [&](int i) { piece2(7, LDS_D0, i); });
        WAIT_BAR(5);
        gemm_step<9, false, 5, 1>(acc, ca, bB, bA, L + LDS_D2 + a_off, hb - 6 * 8192, [&](int i) { piece2(8, LDS_D1, i); });
        if (w == 0) {
            const int lk = fresh_lane();
            const __amdgpu_buffer_rsrc_t rk = __builtin_amdgcn_make_buffer_rsrc(flags + FL_KV, 0, 4096, 0x00020000);
            float ma, mb;
            for (unsigned spins = 0;; ++spins) {
                bool ok = true; ma = 0.f; mb = 0.f;
#pragma unroll
                for (int i = 0; i < 4; ++i) {
                    const u32x4 v = __builtin_amdgcn_raw_buffer_load_b128(rk, (i * 64 + lk) * 16, 0, 16);
                    ok = ok && v[0] == tag && v[2] == tag; ma = fmaxf(ma, __uint_as_float(v[1])); mb = fmaxf(mb, __uint_as_float(v[3]));
                }
                if (__all(ok)) break;
                if (spins > (1u << 19)) { __hip_atomic_store((GAS unsigned*)(flags + FL_TMO), tag, RLX_AGENT); break; }
                __builtin_amdgcn_s_sleep(1);
            }
            ma = wave_max(ma); mb = wave_max(mb);
            if (lk == 0) *(LAS float*)(L + LDS_KM + 8) = ma + mb;
        }
        WAIT_BAR(5);
        gemm_step<9, false, 5, 1>(acc, ca, bA, bB, L + LDS_D0 + a_off, hb - 7 * 8192, [&](int i) { piece_kv(0, i); });
        WAIT_BAR(5);
        gemm_step<9, false, 0, 1>(acc, ca, bB, bA, L + LDS_D1 + a_off, hb - 8 * 8192, [](int) {});
        gemm_flush<9>(acc, ca, bA);
        SB(); FENCE();
    }
    STAMP(1, 4);
    const int la = fresh_lane();
#define l la
    const int hh = w >> 2, kh = (w >> 1) & 1, cp = w & 1;
#define h5 (l >> 5)
    bf16x8 q1[2]; u32x2 q2[2];
#pragma unroll
    for (int c = 0; c < 2; ++c) {
        q1[c] = lds_b128(L + LDS_Q1 + (hh * 4 + 2 * cp + c) * 1024 + l * 16);
        q2[c][0] = *(LAS const unsigned*)(L + LDS_Q2 + (hh * 4 + 2 * cp + c) * 128 + (l & 31) * 4);
    }
    u32x4 pk[18];
#pragma unroll
    for (int m = 0; m < 9; ++m)
#pragma unroll
        for (int pr = 0; pr < 2; ++pr) {
            u32x4 o; o[0] = cvtpk(acc[m][2 * pr][0], acc[m][2 * pr][1]); o[1] = cvtpk(acc[m][2 * pr][2], acc[m][2 * pr][3]);
            o[2] = cvtpk(acc[m][2 * pr + 1][0], acc[m][2 * pr + 1][1]); o[3] = cvtpk(acc[m][2 * pr + 1][2], acc[m][2 * pr + 1][3]);
            pk[m * 2 + pr] = o;
        }
    {
        const float kmax2 = *(LAS const float*)(L + LDS_KM + 8);
#pragma unroll
        for (int c = 0; c < 2; ++c) {
            const u32x4 a = __builtin_bit_cast(u32x4, q1[c]); const unsigned t = h5 ? 0u : q2[c][0];
            float n2 = bflo(t) * bflo(t) + bfhi(t) * bfhi(t);
#pragma unroll
            for (int e = 0; e < 4; ++e) n2 += bflo(a[e]) * bflo(a[e]) + bfhi(a[e]) * bfhi(a[e]);
            n2 = half_sum(n2);
            const float cb = sqrtf(n2 * kmax2) * 1.001f + 0.01f;
            const float chi = rbf(cb), clo = cb - chi;
            q2[c][0] = t; q2[c][1] = h5 ? 0u : cvtpk(-chi, -clo);
        }
    }
    if (w == 0 && l < 40) *(LAS u32x4*)(L + LDS_ZERO + l * 16) = (u32x4){0u, 0u, 0u, 0u};
    if (w == 0 && l == 40) *(LAS unsigned*)(L + LDS_CTL) = 0u;
    WAIT_BAR(0);
    STAMP(1, 5);
#undef l
    const int lb = fresh_lane();
#define l lb
    const unsigned kone = h5 ? 0u : 0x3f803f80u;
    const bool vrow = (l & 31) < 19;
    const unsigned k1_base = 8 * kh * 1024 + l * 16, k2_base = KV_K2 + 8 * kh * 128 + (l & 31) * 4;
    const unsigned v_base = vrow ? KV_V + 8 * kh * 1216 + h5 * 304 + (l & 31) * 16 : LDS_ZERO, v_mul = vrow ? 1u : 0u;
    const int tgf = s >> 3, cgp = s & 7, nmt = cgp < 4 ? 5 : 4, m0 = cgp < 4 ? 5 * cgp : 20 + 4 * (cgp - 4);
    const int pr = w & 1, qk = w >> 1;
    const i32x4 srd_wo = make_srd(ws + WS_WO + (size_t)cgp * WOIMG, WOIMG);
    auto piece_wo = [&](int p) { dma16(srd_wo, vo_x, p * 1024, lds0 + wo_lds(p)); };
    const __amdgpu_buffer_rsrc_t rpart = __builtin_amdgcn_make_buffer_rsrc(ws + WS_PART + (size_t)r * 16 * 8 * 18 * 1024, 0, 16 * 8 * 18 * 1024, 0x00020000);
    float own[11];
    u32x4 pl[5][4];

    f32x16 o0, o1, sX, sY; bf16x8 kA, kB, v0A, v1A, v0B, v1B; unsigned k2A, k2B; u32x4 p0, p1; float e3[4];
    unsigned aK, aK2, aV;
#define SLOT(SC, SN, KN, K2N, CN, OP, V1P, OC, V0C) do { float ea[4], eb[4]; \
        SN = MFMA32(KN, q1[CN], (f32x16){}); SB(); \
        ea[0] = EXP2(SC[0]); ea[1] = EXP2(SC[1]); ea[2] = EXP2(SC[2]); ea[3] = EXP2(SC[3]); \
        p1[2] = cvtpk(e3[0], e3[1]); p1[3] = cvtpk(e3[2], e3[3]); SB(); \
        OP = MFMA32(V1P, __builtin_bit_cast(bf16x8, p1), OP); SB(); \
        eb[0] = EXP2(SC[4]); eb[1] = EXP2(SC[5]); eb[2] = EXP2(SC[6]); eb[3] = EXP2(SC[7]); \
        p0[0] = cvtpk(ea[0], ea[1]); p0[1] = cvtpk(ea[2], ea[3]); SB(); \
        SN = MFMA32K8(__builtin_bit_cast(s16x4, ((u32x2){K2N, kone})), __builtin_bit_cast(s16x4, q2[CN]), SN); SB(); \
        ea[0] = EXP2(SC[8]); ea[1] = EXP2(SC[9]); ea[2] = EXP2(SC[10]); ea[3] = EXP2(SC[11]); \
        p0[2] = cvtpk(eb[0], eb[1]); p0[3] = cvtpk(eb[2], eb[3]); SB(); \
        OC = MFMA32(V0C, __builtin_bit_cast(bf16x8, p0), OC); SB(); \
        e3[0] = EXP2(SC[12]); e3[1] = EXP2(SC[13]); e3[2] = EXP2(SC[14]); e3[3] = EXP2(SC[15]); \
        p1[0] = cvtpk(ea[0], ea[1]); p1[1] = cvtpk(ea[2], ea[3]); SB(); } while (0)
#define PAIR(DK, DK2, DV, H0, H1, H2, H3) do { \
        kB = lds_b128(L + aK + 1024); k2B = *(LAS const unsigned*)(L + aK2 + 128); v0B = lds_b128(L + aV + v_mul * 1216); SB(); \
        H0; SLOT(sX, sY, kA, k2A, 1, o1, v1B, o0, v0A); \
        v1B = lds_b128(L + aV + v_mul * 1216 + 608); SB(); \
        H1; SLOT(sY, sX, kB, k2B, 0, o0, v1A, o1, v0A); \
        aK += (DK); aK2 += (DK2); aV += v_mul * (DV); \
        kA = lds_b128(L + aK); k2A = *(LAS const unsigned*)(L + aK2); v0A = lds_b128(L + aV); SB(); \
        H2; SLOT(sX, sY, kB, k2B, 1, o1, v1A, o0, v0B); \
        v1A = lds_b128(L + aV + 608); SB(); \
        H3; SLOT(sY, sX, kA, k2A, 0, o0, v1B, o1, v0B); } while (0)
#define PAIR_IN(H0, H1, H2, H3) PAIR(2048, 256, 2432, H0, H1, H2, H3)
#define PAIR_JUMP(J, H0, H1, H2, H3) PAIR((J) - 6 * 1024, (J) - 6 * 128, (J) - 6 * 1216, H0, H1, H2, H3)
#define NOP_ ((void)0)
#define ATT_INIT() do { o0 = (f32x16){}; o1 = (f32x16){}; aK = k1_base + LDS_KV0; aK2 = k2_base + LDS_KV0; aV = v_base + v_mul * LDS_KV0; \
        kA = lds_b128(L + aK); k2A = *(LAS const unsigned*)(L + aK2); v0A = lds_b128(L + aV); v1A = lds_b128(L + aV + 608); v1B = v1A; \
        sX = MFMA32(kA, q1[0], (f32x16){}); sX = MFMA32K8(__builtin_bit_cast(s16x4, ((u32x2){k2A, kone})), __builtin_bit_cast(s16x4, q2[0]), sX); \
        p1 = (u32x4){0u, 0u, 0u, 0u}; e3[0] = e3[1] = e3[2] = e3[3] = 0.f; } while (0)
#define ATT_FIN() do { p1[2] = cvtpk(e3[0], e3[1]); p1[3] = cvtpk(e3[2], e3[3]); o1 = MFMA32(v1B, __builtin_bit_cast(bf16x8, p1), o1); } while (0)
    constexpr int J01 = 0 - LDS_KV0, J12 = KVCH;
    {
        const __amdgpu_buffer_rsrc_t rp = __builtin_amdgcn_make_buffer_rsrc(ws + WS_PART + (size_t)(r * 16 + s) * 8 * 18 * 1024, 0, 8 * 18 * 1024, 0x00020000);
#define KV(C, I) do { piece_kv(C, I); SB(); } while (0)
#define ST(I) do { __builtin_amdgcn_raw_buffer_store_b128(pk[I], rp, vo_x, (w * 18 + (I)) * 1024, 16); SB(); } while (0)
#define WO(P0, I) do { piece_wo(min((P0) + w + 8 * (I), (P0) + 36)); SB(); } while (0)
#define PL(T, SP) do { const int m_ = m0 + ((T) < nmt ? (T) : 0); \
            pl[T][SP] = __builtin_amdgcn_raw_buffer_load_b128(rpart, vo_x, ((((4 * qk + (SP)) * 8 + tgf * 4 + (m_ & 3)) * 9 + (m_ >> 2)) * 2 + pr) * 1024, 16); SB(); } while (0)
#define PL2(A) do { PL((A) >> 2, (A) & 3); PL(((A) + 1) >> 2, ((A) + 1) & 3); } while (0)
        ATT_INIT();
        PAIR_IN(KV(1, 0), KV(1, 1), KV(1, 2), KV(1, 3));
        PAIR_IN(KV(1, 4), ST(0), KV(2, 0), ST(1));
        PAIR_IN(KV(2, 1), ST(2), KV(2, 2), ST(3));
        WAIT_BAR(7);
        PAIR_JUMP(J01, KV(2, 3), ST(4), KV(2, 4), ST(5));
        PAIR_IN(KV(3, 0), ST(6), KV(3, 1), ST(7));
        PAIR_IN(KV(3, 2), ST(8), KV(3, 3), ST(9));
        PAIR_IN(KV(3, 4), ST(10), NOP_, ST(11));
        WAIT_BAR(12);
        PAIR_JUMP(J12, WO(0, 0), ST(12), WO(0, 1), ST(13));
        PAIR_IN(WO(0, 2), ST(14), WO(0, 3), ST(15));
        PAIR_IN(WO(0, 4), ST(16), NOP_, ST(17));
        PAIR_IN(NOP_, NOP_, NOP_, NOP_);
        WAIT_BAR(0);
        if (w == 0 && fresh_lane() == 0) __hip_atomic_store((GAS unsigned*)(flags + FL_P + wgi * 4), tag, RLX_AGENT);
        PAIR_JUMP(J12, WO(37, 0), WO(37, 1), WO(37, 2), WO(37, 3));
        const unsigned pflag = __hip_atomic_load((GAS unsigned*)(flags + FL_P + (r * 16 + (l & 15)) * 4), RLX_AGENT);
        PAIR_IN(WO(37, 4), NOP_, NOP_, NOP_);
        LBAR();
        if (!__all(pflag == tag)) wait_tags(flags, FL_P + (r * 16 + (l & 15)) * 4, true, tag);
#define WOL() do { piece_wo(111 + w); if (w == 0) piece_wo(119); SB(); } while (0)
        PAIR_IN(WOL(), PL2(0), PL2(2), PL2(4));
        PAIR_IN(PL2(6), PL2(8), PL2(10), PL2(12));
        PAIR(0, 0, 0, PL2(14), PL2(16), PL(4, 2), NOP_);
        PL(4, 3);
        ATT_FIN();
    }
    STAMP(1, 6);
#undef l
    const int lc = fresh_lane();
#define l lc
    for (int attempt = 0;; ++attempt) {
        if (attempt == 1) {
            const unsigned kone = h5 ? 0u : 0x3f803f80u;
            const bool vrow = (l & 31) < 19;
            const unsigned k1_base = 8 * kh * 1024 + l * 16, k2_base = KV_K2 + 8 * kh * 128 + (l & 31) * 4;
            const unsigned v_base = vrow ? KV_V + 8 * kh * 1216 + h5 * 304 + (l & 31) * 16 : LDS_ZERO, v_mul = vrow ? 1u : 0u;
            WAIT_BAR(0);
#pragma unroll
            for (int c = 0; c < 4; ++c)
#pragma unroll
                for (int i = 0; i < 5; ++i) piece_kv(c, i);
            if (w == 0 && l < 40) *(LAS u32x4*)(L + LDS_ZERO + l * 16) = (u32x4){0u, 0u, 0u, 0u};
            WAIT_BAR(0);
            float m[2] = {-INFINITY, -INFINITY};
            for (int i = 0; i < 32; ++i) {
                const unsigned to = (i >> 3) == 0 ? LDS_KV0 : ((i >> 3) - 1) * KVCH;
                const bf16x8 k1 = lds_b128(L + k1_base + to + (i & 7) * 1024);
                const u32x2 k2 = {*(LAS const unsigned*)(L + k2_base + to + (i & 7) * 128), kone};
#pragma unroll
                for (int c = 0; c < 2; ++c) {
                    const u32x2 qz = {q2[c][0], 0u};
                    f32x16 sc = MFMA32(k1, q1[c], (f32x16){});
                    sc = MFMA32K8(__builtin_bit_cast(s16x4, k2), __builtin_bit_cast(s16x4, qz), sc);
#pragma unroll
                    for (int e = 0; e < 16; ++e) m[c] = fmaxf(m[c], sc[e]);
                }
            }
            LAS float* mx = (LAS float*)(L + LDS_MX);
#pragma unroll
            for (int c = 0; c < 2; ++c) { m[c] = half_max(m[c]); if (l < 32) mx[(w * 2 + c) * 32 + l] = m[c]; }
            LBAR();
#pragma unroll
            for (int c = 0; c < 2; ++c) {
                const float mm = fmaxf(m[c], mx[((w ^ 2) * 2 + c) * 32 + (l & 31)]);
                const float chi = rbf(mm), clo = mm - chi;
                q2[c][1] = h5 ? 0u : cvtpk(-chi, -clo);
            }
            o0 = (f32x16){}; o1 = (f32x16){};
            for (int i = 0; i < 32; ++i) {
                const unsigned to = (i >> 3) == 0 ? LDS_KV0 : ((i >> 3) - 1) * KVCH;
                const bf16x8 k1 = lds_b128(L + k1_base + to + (i & 7) * 1024);
                const u32x2 k2 = {*(LAS const unsigned*)(L + k2_base + to + (i & 7) * 128), kone};
                const unsigned va = v_base + v_mul * (to + (i & 7) * 1216);
                const bf16x8 v0 = lds_b128(L + va), v1 = lds_b128(L + va + 608);
#pragma unroll
                for (int c = 0; c < 2; ++c) {
                    f32x16 sc = MFMA32(k1, q1[c], (f32x16){});
                    sc = MFMA32K8(__builtin_bit_cast(s16x4, k2), __builtin_bit_cast(s16x4, q2[c]), sc);
                    u32x4 pa, pb;
#pragma unroll
                    for (int e = 0; e < 4; ++e) { pa[e] = cvtpk(EXP2(sc[2 * e]), EXP2(sc[2 * e + 1])); pb[e] = cvtpk(EXP2(sc[8 + 2 * e]), EXP2(sc[9 + 2 * e])); }
                    if (c == 0) { o0 = MFMA32(v0, __builtin_bit_cast(bf16x8, pa), o0); o0 = MFMA32(v1, __builtin_bit_cast(bf16x8, pb), o0); }
                    else { o1 = MFMA32(v0, __builtin_bit_cast(bf16x8, pa), o1); o1 = MFMA32(v1, __builtin_bit_cast(bf16x8, pb), o1); }
                }
            }
        }
        LAS float* red = (LAS float*)(L + LDS_RED);
        if (attempt == 1) LBAR();
#pragma unroll
        for (int e = 0; e < 11; ++e) { red[(w * 11 + e) * 64 + l] = kh ? o0[e] : o1[e]; own[e] = kh ? o1[e] : o0[e]; }
        LBAR();
        if (attempt == 0) {
#pragma unroll
            for (int q = 0; q < 5; ++q) piece_wo(min(74 + w + 8 * q, 110));
        } else {
#pragma unroll
            for (int q = 0; q < 15; ++q) piece_wo(w * 15 + q);
        }
#pragma unroll
        for (int e = 0; e < 11; ++e) own[e] += red[((w ^ 2) * 11 + e) * 64 + l];
        const float lsum = half_lo(own[10]);
        if (!(lsum >= 8.67e-19f && lsum < 3.0e38f) && attempt == 0) *(LAS unsigned*)(L + LDS_CTL) = 1u;
        const float rl = 1.0f / lsum;
        LAS char* ot = L + LDS_OT + (32 * (2 * cp + kh) + (l & 31)) * 80 + 40 * hh;
        *(LAS u32x2*)(ot + 8 * h5) = (u32x2){cvtpk(own[0] * rl, own[1] * rl), cvtpk(own[2] * rl, own[3] * rl)};
        *(LAS u32x2*)(ot + 16 + 8 * h5) = (u32x2){cvtpk(own[4] * rl, own[5] * rl), cvtpk(own[6] * rl, own[7] * rl)};
        if (h5 == 0) *(LAS u32x2*)(ot + 32) = (u32x2){cvtpk(own[8] * rl, own[9] * rl), 0u};
        LBAR();
        const unsigned isbad = *(LAS const unsigned*)(L + LDS_CTL);
        if (!(isbad && attempt == 0)) break;
    }
    STAMP(1, 7);
    {
        const __amdgpu_buffer_rsrc_t rw = __builtin_amdgcn_make_buffer_rsrc(ws + WS_O + (size_t)(r * 16 + s) * 128 * 80, 0, 128 * 80, 0x00020000);
        __builtin_amdgcn_raw_buffer_store_b128(*(LAS const u32x4*)(L + LDS_OT + w * 1024 + l * 16), rw, l * 16, w * 1024, 16);
        if (w < 2) __builtin_amdgcn_raw_buffer_store_b128(*(LAS const u32x4*)(L + LDS_OT + (8 + w) * 1024 + l * 16), rw, l * 16, (8 + w) * 1024, 16);
    }
    {
        const int tok = l & 15, g4 = l >> 4, q4 = (l >> 2) & 3, p4 = l & 3;
        u32x2 xv[3];
#pragma unroll
        for (int u = 0; u < 3; ++u) {
            const int tt = min(qk + 4 * u, 2 * nmt - 1);
            xv[u] = *(const u32x2*)(x + (size_t)(128 * r + 64 * tgf + 32 * pr + 16 * (tt & 1) + tok) * DM + 16 * (m0 + (tt >> 1)) + 4 * g4);
        }
        SB();
        STAMP(1, 8);
        const __amdgpu_buffer_rsrc_t ro = __builtin_amdgcn_make_buffer_rsrc(ws + WS_O + (size_t)r * 16 * 128 * 80, 0, 16 * 128 * 80, 0x00020000);
        u32x4 bf[5][2];
        unsigned pend = 0x3ffu;
        for (unsigned spins = 0;; ++spins) {
#pragma unroll
            for (int i = 0; i < 5; ++i) {
                const int kp = 32 * (5 * qk + i) + 8 * g4, sp = kp / 40, d = kp - 40 * sp;
#pragma unroll
                for (int nn = 0; nn < 2; ++nn)
                    if (pend & (1u << (2 * i + nn))) bf[i][nn] = __builtin_amdgcn_raw_buffer_load_b128(ro, (sp * 128 + 64 * tgf + 32 * pr + 16 * nn + tok) * 80 + d * 2, 0, 16);
            }
#pragma unroll
            for (int i = 0; i < 5; ++i)
#pragma unroll
                for (int nn = 0; nn < 2; ++nn)
                    if ((pend & (1u << (2 * i + nn))) && __all(bf[i][nn][0] != O_EMPTY && bf[i][nn][1] != O_EMPTY && bf[i][nn][2] != O_EMPTY && bf[i][nn][3] != O_EMPTY)) pend &= ~(1u << (2 * i + nn));
            if (pend == 0u) break;
            if (spins > (1u << 17)) { __hip_atomic_store((GAS unsigned*)(flags + FL_TMO), tag, RLX_AGENT); break; }
            __builtin_amdgcn_s_sleep(8);
        }
        f32x4 fa[5][2];
#pragma unroll
        for (int t = 0; t < 5; ++t) {
            fa[t][0] = (f32x4){0.f, 0.f, 0.f, 0.f}; fa[t][1] = (f32x4){0.f, 0.f, 0.f, 0.f};
#pragma unroll
            for (int sp = 0; sp < 4; ++sp) {
                const u32x4 p = pl[t][sp];
                fa[t][0][0] += bflo(p[0]); fa[t][0][1] += bfhi(p[0]); fa[t][0][2] += bflo(p[1]); fa[t][0][3] += bfhi(p[1]);
                fa[t][1][0] += bflo(p[2]); fa[t][1][1] += bfhi(p[2]); fa[t][1][2] += bflo(p[3]); fa[t][1][3] += bfhi(p[3]);
            }
        }
        WAIT_BAR(0);
        STAMP(1, 9);
        const unsigned ao = (4 * g4 + q4) * 64 + 8 * (p4 & 1);
#pragma unroll
        for (int t = 0; t < 5; ++t)
            if (t < nmt) {
#pragma unroll
                for (int i = 0; i < 5; ++i) {
                    const int p = (t >> 1) * 40 + (5 * qk + i) * 2;
                    const unsigned sw = ao + 16 * ((2 * (t & 1) + (p4 >> 1)) ^ (2 * (g4 & 1)));
                    const s16x4 lo = lds_tr(L + wo_lds(p) + sw), hi = lds_tr(L + wo_lds(p + 1) + sw);
                    const bf16x8 a = __builtin_shufflevector(lo, hi, 0, 1, 2, 3, 4, 5, 6, 7);
                    fa[t][0] = MFMA16(a, __builtin_bit_cast(bf16x8, bf[i][0]), fa[t][0]); fa[t][1] = MFMA16(a, __builtin_bit_cast(bf16x8, bf[i][1]), fa[t][1]);
                }
            }
        STAMP(1, 10);
        LBAR();
        LAS f32x4* ex = (LAS f32x4*)L;
#pragma unroll
        for (int t = 0; t < 5; ++t) { ex[(w * 10 + 2 * t) * 64 + l] = fa[t][0]; ex[(w * 10 + 2 * t + 1) * 64 + l] = fa[t][1]; }
        LBAR();
#pragma unroll
        for (int u = 0; u < 3; ++u) {
            const int tt = qk + 4 * u;
            if (tt < 2 * nmt) {
                f32x4 v = ex[((0 * 2 + pr) * 10 + tt) * 64 + l];
#pragma unroll
                for (int k = 1; k < 4; ++k) v += ex[((k * 2 + pr) * 10 + tt) * 64 + l];
                const size_t off = (size_t)(128 * r + 64 * tgf + 32 * pr + 16 * (tt & 1) + tok) * DM + 16 * (m0 + (tt >> 1)) + 4 * g4;
                *(u32x2*)(out + off) = (u32x2){cvtpk(v[0] + bflo(xv[u][0]), v[1] + bfhi(xv[u][0])), cvtpk(v[2] + bflo(xv[u][1]), v[3] + bfhi(xv[u][1]))};
            }
        }
    }
#undef l
#undef h5
    STAMP(1, 11); STAMP_END(1);
    asm volatile("fused_block_code_end:");
}

extern "C" void kernel_launch(void* const* d_in, const int* in_sizes, int n_in, void* d_out, int out_size, void* d_ws, size_t ws_size, hipStream_t stream) {
    const u16* x = (const u16*)d_in[0]; const u16* wq = (const u16*)d_in[1]; const u16* wk = (const u16*)d_in[2]; const u16* wv = (const u16*)d_in[3];
    const u16* wo = (const u16*)d_in[4]; const u16* wg = (const u16*)d_in[6]; const u16* wu = (const u16*)d_in[7]; const u16* wd = (const u16*)d_in[8];
    fused_block<<<256, 512, 0, stream>>>(x, wq, wk, wv, wo, wg, wu, wd, (char*)d_ws, (u16*)d_out);
}
```

```cpp
#include <hip/hip_runtime.h>
#include <hip/hip_bf16.h>
#include <stdint.h>

typedef unsigned short u16;
typedef short bf16x8 __attribute__((ext_vector_type(8)));
typedef short s16x4 __attribute__((ext_vector_type(4)));
typedef float f32x4 __attribute__((ext_vector_type(4)));
typedef float f32x16 __attribute__((ext_vector_type(16)));
typedef int i32x4 __attribute__((ext_vector_type(4)));
typedef unsigned u32x4 __attribute__((ext_vector_type(4)));
typedef unsigned u32x2 __attribute__((ext_vector_type(2)));
typedef __bf16 bf16x2_t __attribute__((ext_vector_type(2)));
typedef float f32x2 __attribute__((ext_vector_type(2)));
typedef short v4i16_t __attribute__((ext_vector_type(4)));
extern "C" __device__ unsigned long long amd_dispatch_id() __asm("llvm.amdgcn.dispatch.id");
#define LAS __attribute__((address_space(3)))
#define GAS __attribute__((address_space(1)))
#define DI __device__ __forceinline__

constexpr int SEQ = 2048, DM = 576, NH = 32, DH = 18, FF = 4608;

constexpr int KVCH = 37888;
constexpr int KV_V = 16384, KV_K2 = 35840;
constexpr int WOIMG = 120 * 1024;
constexpr size_t WS_FLAGS = 0;
constexpr size_t WS_XN    = 16384;
constexpr size_t WS_WQX   = WS_XN + (size_t)SEQ * DM * 2;
constexpr size_t WS_WO    = WS_WQX + (size_t)DM * 1024 * 2;
constexpr size_t WS_KV    = WS_WO + (size_t)8 * WOIMG;
constexpr size_t WS_O     = WS_KV + 4 * KVCH;
constexpr size_t WS_PART  = WS_O + (size_t)256 * 128 * 80;
constexpr size_t WS_END   = WS_PART + (size_t)256 * 8 * 18 * 1024;
constexpr int FL_PRO = 0, FL_P = 1024, FL_KV = 2048, FL_TMO = 8192;
constexpr unsigned O_EMPTY = 0xffffffffu;

DI float bflo(unsigned v) { return __uint_as_float(v << 16); }
DI float bfhi(unsigned v) { return __uint_as_float(v & 0xffff0000u); }
DI unsigned cvtpk(float lo, float hi) { return __builtin_bit_cast(unsigned, __builtin_convertvector((f32x2){lo, hi}, bf16x2_t)); }
DI float rbf(float f) { return bflo(cvtpk(f, f)); }
DI i32x4 make_srd(const void* p, unsigned bytes) {
    unsigned long long a = (unsigned long long)p; i32x4 s;
    s.x = __builtin_amdgcn_readfirstlane((unsigned)a); s.y = __builtin_amdgcn_readfirstlane((unsigned)(a >> 32) & 0xffffu);
    s.z = (int)bytes; s.w = 0x00020000; return s;
}
DI void dma16(i32x4 srd, unsigned voff, unsigned soff, unsigned lds_addr) {
    unsigned keep;
    asm volatile("s_mov_b32 %0, m0\n\ts_mov_b32 m0, %4\n\ts_nop 0\n\tbuffer_load_dwordx4 %1, %2, %3 offen lds\n\ts_mov_b32 m0, %0"
                 : "=&s"(keep) : "v"(voff), "s"(srd), "s"(soff), "s"(lds_addr) : "memory");
}
DI void dma16_sc1(i32x4 srd, unsigned voff, unsigned soff, unsigned lds_addr) {
    unsigned keep;
    asm volatile("s_mov_b32 %0, m0\n\ts_mov_b32 m0, %4\n\ts_nop 0\n\tbuffer_load_dwordx4 %1, %2, %3 offen sc1 lds\n\ts_mov_b32 m0, %0"
                 : "=&s"(keep) : "v"(voff), "s"(srd), "s"(soff), "s"(lds_addr) : "memory");
}
DI void dma4(i32x4 srd, unsigned voff, unsigned soff, unsigned lds_addr) {
    unsigned keep;
    asm volatile("s_mov_b32 %0, m0\n\ts_mov_b32 m0, %4\n\ts_nop 0\n\tbuffer_load_dword %1, %2, %3 offen lds\n\ts_mov_b32 m0, %0"
                 : "=&s"(keep) : "v"(voff), "s"(srd), "s"(soff), "s"(lds_addr) : "memory");
}
#define WAIT_BAR(N) asm volatile("s_waitcnt vmcnt(" #N ") lgkmcnt(0)\n\ts_barrier" ::: "memory")
#define LBAR() asm volatile("s_waitcnt lgkmcnt(0)\n\ts_barrier" ::: "memory")
#define SB() __builtin_amdgcn_sched_barrier(0)
#define FENCE() asm volatile("" ::: "memory")
#define PIN4(a) asm volatile("" : "+v"((a)[0]), "+v"((a)[1]), "+v"((a)[2]), "+v"((a)[3]))
#define MFMA16(a, b, c) __builtin_amdgcn_mfma_f32_16x16x32_bf16(a, b, c, 0, 0, 0)
#define MFMA32(a, b, c) __builtin_amdgcn_mfma_f32_32x32x16_bf16(a, b, c, 0, 0, 0)
#define MFMA32K8(a, b, c) __builtin_amdgcn_mfma_f32_32x32x8bf16_1k(a, b, c, 0, 0, 0)
#define EXP2(x) __builtin_amdgcn_exp2f(x)
DI s16x4 lds_tr(LAS const char* p) { return __builtin_bit_cast(s16x4, __builtin_amdgcn_ds_read_tr16_b64_v4i16((LAS v4i16_t*)p)); }
DI bf16x8 lds_b128(LAS const char* p) { return *(LAS const bf16x8*)p; }
DI bf16x8 lds_afrag(LAS const char* p) { const s16x4 lo = lds_tr(p), hi = lds_tr(p + 2048); return __builtin_shufflevector(lo, hi, 0, 1, 2, 3, 4, 5, 6, 7); }
#define RLX_AGENT __ATOMIC_RELAXED, __HIP_MEMORY_SCOPE_AGENT
template <int X> DI float xor_lane(float v) { return __int_as_float(__builtin_amdgcn_ds_swizzle(__float_as_int(v), (X << 10) | 0x1f)); }
DI float half_sum(float v) { auto r = __builtin_amdgcn_permlane32_swap(__float_as_uint(v), __float_as_uint(v), false, false); return __uint_as_float(r[0]) + __uint_as_float(r[1]); }
DI float half_max(float v) { auto r = __builtin_amdgcn_permlane32_swap(__float_as_uint(v), __float_as_uint(v), false, false); return fmaxf(__uint_as_float(r[0]), __uint_as_float(r[1])); }
DI float half_lo(float v) { auto r = __builtin_amdgcn_permlane32_swap(__float_as_uint(v), __float_as_uint(v), false, false); return __uint_as_float(r[0]); }
template <int CTRL, int ROWS> DI float dpp_add(float v) { return v + __int_as_float(__builtin_amdgcn_update_dpp(0, __float_as_int(v), CTRL, ROWS, 0xf, true)); }
DI float wave_sum_dpp(float v) {
    v = dpp_add<0x111, 0xf>(v); v = dpp_add<0x112, 0xf>(v); v = dpp_add<0x114, 0xf>(v); v = dpp_add<0x118, 0xf>(v);
    v = dpp_add<0x142, 0xa>(v); v = dpp_add<0x143, 0xc>(v);
    return __int_as_float(__builtin_amdgcn_readlane(__float_as_int(v), 63));
}
DI float wave_sum(float v) { v += xor_lane<1>(v); v += xor_lane<2>(v); v += xor_lane<4>(v); v += xor_lane<8>(v); v += xor_lane<16>(v); return half_sum(v); }
DI float wave_max(float v) { v = fmaxf(v, xor_lane<1>(v)); v = fmaxf(v, xor_lane<2>(v)); v = fmaxf(v, xor_lane<4>(v)); v = fmaxf(v, xor_lane<8>(v)); v = fmaxf(v, xor_lane<16>(v)); return half_max(v); }
DI int fresh_lane() { int t; asm volatile("v_mbcnt_lo_u32_b32 %0, -1, 0\n\tv_mbcnt_hi_u32_b32 %0, -1, %0" : "=v"(t)); return t; }
DI void st_agent64(void* p, unsigned lo, unsigned hi) { __hip_atomic_store((GAS unsigned long long*)p, ((unsigned long long)hi << 32) | lo, RLX_AGENT); }
DI void wait_tags(const char* flags, unsigned off, bool use, unsigned tag) {
    for (unsigned spins = 0;; ++spins) {
        const unsigned v = use ? __hip_atomic_load((GAS unsigned*)(flags + off), RLX_AGENT) : tag;
        if (__all(v == tag)) break;
        if (spins > (1u << 19)) { __hip_atomic_store((GAS unsigned*)(flags + FL_TMO), tag, RLX_AGENT); break; }
        __builtin_amdgcn_s_sleep(1);
    }
    FENCE();
}
#define STAMP_DECL
#define STAMP(K, i)
#define STAMP_END(K)

#define DMA345 dma16_sc1
#define OSLEEP 8
constexpr int ST1 = 49152;
constexpr int LDS_D0 = 0, LDS_D1 = 49152, LDS_H = 86016, LDS_Q1 = 36864, LDS_Q2 = 45056;
constexpr int LDS_PF = 147456;
constexpr int LDS_KM = 46080;
constexpr int LDS_D2 = 126976;
constexpr int LDS_KV0 = 125952;
constexpr int LDS_OT = 113664;
constexpr int LDS_MX = LDS_OT;
constexpr int LDS_ZERO = LDS_OT + 10240;
constexpr int LDS_CTL = LDS_ZERO + 640;
constexpr int LDS_RED = 2 * KVCH + 9 * 1024;
constexpr int LDS_BYTES = 163840;
DI unsigned wo_lds(int p) { return p < 37 ? LDS_KV0 + p * 1024 : p < 74 ? (p - 37) * 1024 : p < 111 ? KVCH + (p - 74) * 1024 : 2 * KVCH + (p - 111) * 1024; }

template <int NM, bool FIRST, int ND, int DPG, int AD = 3, bool PIN = false, class Dma>
DI void gemm_step(f32x4 (&acc)[10][4], bf16x8 (&ca)[2], bf16x8 (&cb)[4], bf16x8 (&nb)[4], LAS const char* ap, LAS const char* bp, const Dma& dma) {
    bf16x8 a[10];
#pragma unroll
    for (int n = 0; n < 4; ++n) nb[n] = lds_b128(bp + n * 1024);
    a[0] = lds_afrag(ap); a[1] = lds_afrag(ap + 4096); if (AD > 2) a[2] = lds_afrag(ap + 2 * 4096);
    SB();
    int d = 0;
    if (!FIRST) {
#pragma unroll
        for (int k = 0; k < DPG; ++k) if (d < ND) dma(d++);
#pragma unroll
        for (int n = 0; n < 4; ++n) acc[NM - 2][n] = MFMA16(ca[0], cb[n], acc[NM - 2][n]);
        if (PIN) PIN4(acc[NM - 2]); SB();
#pragma unroll
        for (int k = 0; k < DPG; ++k) if (d < ND) dma(d++);
#pragma unroll
        for (int n = 0; n < 4; ++n) acc[NM - 1][n] = MFMA16(ca[1], cb[n], acc[NM - 1][n]);
        if (PIN) PIN4(acc[NM - 1]); SB();
    }
#pragma unroll
    for (int m = 0; m < NM - 2; ++m) {
        if (m + AD < NM) a[m + AD] = lds_afrag(ap + (m + AD) * 4096);
#pragma unroll
        for (int k = 0; k < DPG; ++k) if (d < ND) dma(d++);
#pragma unroll
        for (int n = 0; n < 4; ++n) acc[m][n] = MFMA16(a[m], nb[n], acc[m][n]);
        if (PIN) PIN4(acc[m]); SB();
    }
    ca[0] = a[NM - 2]; ca[1] = a[NM - 1];
}
template <int NM>
DI void gemm_flush(f32x4 (&acc)[10][4], const bf16x8 (&ca)[2], const bf16x8 (&cb)[4]) {
#pragma unroll
    for (int n = 0; n < 4; ++n) acc[NM - 2][n] = MFMA16(ca[0], cb[n], acc[NM - 2][n]);
#pragma unroll
    for (int n = 0; n < 4; ++n) acc[NM - 1][n] = MFMA16(ca[1], cb[n], acc[NM - 1][n]);
}

__global__ __launch_bounds__(512, 2) void fused_block(const u16* __restrict__ x, const u16* __restrict__ wq, const u16* __restrict__ wk, const u16* __restrict__ wv,
                                                       const u16* __restrict__ wo, const u16* __restrict__ wg, const u16* __restrict__ wu, const u16* __restrict__ wd,
                                                       char* __restrict__ ws, u16* __restrict__ out) {
    __shared__ __attribute__((aligned(1024))) char lds[LDS_BYTES];
    const int tid = threadIdx.x, l = tid & 63;
    const int w = __builtin_amdgcn_readfirstlane(tid >> 6);
    const int b = blockIdx.x, s = 2 * (b & 7) + ((b >> 3) & 1), r = b >> 4, wgi = r * 16 + s;
    const int tg = w >> 2, fg = w & 3;
    constexpr unsigned lds0 = 0;
    LAS char* L = (LAS char*)lds;
    char* flags = ws + WS_FLAGS;
    const unsigned tag = (((unsigned)amd_dispatch_id() + 1u) * 0x9E3779B1u) ^ ((unsigned)((unsigned long long)__builtin_amdgcn_dispatch_ptr() >> 6) * 0x85EBCA77u) ^ 0x5bd1e995u;

    const int dmat = w & 1, dR = (w >> 1) & 1, dcg0 = 5 * (w >> 2);
    const int di = l >> 2, dsig = l & 3;
    const int dmi = dsig ^ (2 * dmat + ((di >> 2) & 1)), dkrow = 8 * (di >> 2) + (di & 3);
    const i32x4 srd_w = make_srd(dmat ? wu : wg, DM * FF * 2);
    const i32x4 srd_q = make_srd(ws + WS_WQX, DM * 1024 * 2);
    const i32x4 srd_x = make_srd(ws + WS_XN, SEQ * DM * 2);
    const unsigned vo_w = dkrow * (FF * 2) + 16 * dmi, vo_q = dkrow * 2048 + 16 * dmi, vo_x = l * 16;
    const unsigned dlds = lds0 + dR * 2048 + dmat * 1024;
    auto pieceW = [&](int j, unsigned slot, int cg) { dma16(srd_w, vo_w, (32 * j + 4 * dR) * (FF * 2) + 288 * s * 2 + cg * 64, dlds + slot + cg * 4096); };
    auto pieceQ = [&](int j, unsigned slot) { dma16_sc1(srd_q, vo_q, (32 * j + 4 * dR) * 2048 + (64 * s + 32 * dmat) * 2, dlds + slot + 9 * 4096); };
    auto pieceX = [&](int j, unsigned slot, int n) { dma16_sc1(srd_x, vo_x, ((r * 18 + j) * 8 + n) * 1024, lds0 + slot + 40960 + n * 1024); };
    auto piece1k = [&](int j, unsigned slot, int i, int kind) {
        if (i < 3) { if (kind & 1) pieceW(j, slot, dcg0 + i); }
        else if (i == 3) { if (w == 3) { if (kind & 2) pieceQ(j, slot); } else if (kind & 1) pieceW(j, slot, dcg0 + 3); }
        else if (i == 4) {
            if (w < 3) { if (kind & 1) pieceW(j, slot, 4); } else if (w == 3) { if (kind & 2) pieceX(j, slot, 7); }
            else if (w < 7) { if (kind & 2) pieceQ(j, slot); } else if (kind & 1) pieceW(j, slot, 3);
        } else { if (w == 7) { if (kind & 1) pieceW(j, slot, 4); } else if (kind & 2) pieceX(j, slot, w); }
    };
    auto piece2 = [&](int j, unsigned slot, int i) {
        const int ii = (w >= 4 && i == 4) ? 3 : i;
        const unsigned vo_d = ((vo_w & ~63u) >> 3) + ((vo_w & 48u) << 1);
        dma16(make_srd(wd, FF * DM * 2), vo_d, (288 * s + 32 * j + 4 * dR) * (DM * 2) + 16 * dmat + (dcg0 + ii) * 128, dlds + slot + (dcg0 + ii) * 4096);
    };

    STAMP_DECL;
    STAMP(1, 0);
    const u32x4* xs = (const u32x4*)(x + (size_t)(128 * r + 8 * s + w) * DM);
    const u32x4 v0 = xs[l];
    u32x4 v1 = xs[64 + (l & 7)];
    FENCE();
    if (w == 7) {
        unsigned long long pc; unsigned len;
        asm volatile("s_getpc_b64 %0\n\tcode_here_%=:\n\ts_mov_b32 %1, fused_block_code_end-code_here_%=" : "=s"(pc), "=s"(len));
        i32x4 sc; sc.x = (int)(unsigned)pc; sc.y = (int)((unsigned)(pc >> 32) & 0xffffu); sc.z = (int)len; sc.w = 0x00020000;
        dma4(sc, l < 20 ? (unsigned)(((b >> 3) * 20 + l) * 128) : 0x80000000u, 0, lds0 + LDS_PF);
    }
    {
        unsigned qv[8];
        {
            const int tq = min(tid, 287), k = 36 * r + (tq >> 3), mat = (tq >> 2) & 1, t = tq & 3;
            const u16* wc = ((s & 1) ? wv : wk) + k * DH;
            const u16* wh = wq + (size_t)k * DM + (2 * s + (t >> 1)) * DH;
#pragma unroll
            for (int e = 0; e < 8; ++e) {
                const int rho = mat * 8 + e, cc = (t == 1 ? 0 : 12) + rho - 4;
                const bool isq = (t & 1) == 0 || rho < 2, isc = !isq && rho >= 4 && cc < (t == 1 ? 12 : 18);
                const u16* p = isc ? wc + cc : wh + ((t & 1) == 0 ? rho : 16 + (rho & 1));
                const unsigned val = *p;
                qv[e] = (isq || isc) ? val : 0u;
            }
        }
        const int cgp = s & 7, wp = 4 * (r * 2 + (s >> 3)) + ((min(max(tid, 192), 447) - 192) >> 6);
        const bool wo_act = tid >= 192 && tid < 448 && wp < 120;
        u32x4 wov;
        {
            const int m0 = cgp < 4 ? 5 * cgp : 20 + 4 * (cgp - 4), wpc = min(wp, 119);
            const int pp = wpc / 40, js = (wpc % 40) >> 1, R = wpc & 1, i = l >> 2, ch = (l & 3) ^ (2 * ((i >> 2) & 1));
            const int kp = 32 * js + 8 * (i >> 2) + (i & 3) + 4 * R, sp = kp / 40, d = kp - 40 * sp, hd = d >= 20 ? 1 : 0, dd = d - 20 * hd;
            const int row = 36 * sp + 18 * hd + (dd < 18 ? dd : 17), col = min(16 * (m0 + 2 * pp) + 8 * ch, DM - 8);
            wov = *(const u32x4*)(wo + (size_t)row * DM + col);
        }
        FENCE();
        STAMP(1, 13);
        if (l >= 8) v1 = (u32x4){0u, 0u, 0u, 0u};
        float sm = 0.f;
#pragma unroll
        for (int e = 0; e < 4; ++e) sm += bflo(v0[e]) + bfhi(v0[e]) + bflo(v1[e]) + bfhi(v1[e]);
        const float mean = wave_sum_dpp(sm) * (1.0f / DM);
        float q = 0.f, q1 = 0.f;
#pragma unroll
        for (int e = 0; e < 4; ++e) { const float a = bflo(v0[e]) - mean, c = bfhi(v0[e]) - mean; q += a * a + c * c; }
#pragma unroll
        for (int e = 0; e < 4; ++e) { const float a = bflo(v1[e]) - mean, c = bfhi(v1[e]) - mean; q1 += a * a + c * c; }
        if (l < 8) q += q1;
        const float m = rbf(mean), var = rbf(wave_sum_dpp(q) * (1.0f / DM));
        const float rs = 1.0f / rbf(sqrtf(rbf(var + 1e-5f)));
        LAS char* xst = L + 2 * ST1;
        {
            u32x4 o;
#pragma unroll
            for (int e = 0; e < 4; ++e) o[e] = cvtpk(rbf(bflo(v0[e]) - m) * rs, rbf(bfhi(v0[e]) - m) * rs);
            *(LAS u32x4*)(xst + l * 128 + w * 16) = o;
#pragma unroll
            for (int e = 0; e < 4; ++e) o[e] = cvtpk(rbf(bflo(v1[e]) - m) * rs, rbf(bfhi(v1[e]) - m) * rs);
            if (l < 8) *(LAS u32x4*)(xst + (64 + l) * 128 + w * 16) = o;
        }
        asm volatile("" : "+v"(wov));
        LBAR();
        STAMP(1, 14);
        if (w < 7) {
            const __amdgpu_buffer_rsrc_t rx = __builtin_amdgcn_make_buffer_rsrc(ws + WS_XN, 0, SEQ * DM * 2, 0x00020000);
#pragma unroll
            for (int u = 0; u < 2; ++u) {
                const int i = tid + 448 * u, c = i >> 3, t = i & 7;
                const u32x4 v = *(LAS const u32x4*)(xst + min(i, 575) * 16);
                __builtin_amdgcn_raw_buffer_store_b128(v, rx, i < 576 ? (unsigned)(((r * 18 + (c >> 2)) * 8 + (s >> 1)) * 1024 + ((c & 3) * 16 + 8 * (s & 1) + t) * 16) : 0xfffffff0u, 0, 16);
            }
            {
                u32x4 o; o[0] = qv[0] | (qv[1] << 16); o[1] = qv[2] | (qv[3] << 16); o[2] = qv[4] | (qv[5] << 16); o[3] = qv[6] | (qv[7] << 16);
                const __amdgpu_buffer_rsrc_t rq = __builtin_amdgcn_make_buffer_rsrc(ws + WS_WQX, 0, DM * 1024 * 2, 0x00020000);
                __builtin_amdgcn_raw_buffer_store_b128(o, rq, tid < 288 ? (unsigned)(((36 * r + (tid >> 3)) * 128 + s * 8 + (tid & 7)) * 16) : 0xfffffff0u, 0, 16);
            }
            WAIT_BAR(0);
        } else LBAR();
        STAMP(1, 15);
        if (tid == 0) __hip_atomic_store((GAS unsigned*)(flags + FL_PRO + wgi * 4), tag, RLX_AGENT);
#pragma unroll
        for (int i = 0; i < 6; ++i) piece1k(0, 0, i, 1);
#pragma unroll
        for (int i = 0; i < 6; ++i) piece1k(1, ST1, i, 1);
        if (w == 7) {
            const unsigned o = (l & 7) < 5 ? (unsigned)(((l >> 3) * 16 + r) * (FF * 2) + ((288 * s * 2) & ~127) + (l & 7) * 128) : 0x80000000u;
#pragma unroll
            for (int g = 0; g < 2; ++g) { dma4(make_srd(wg, DM * FF * 2), o, g * 128 * (FF * 2), lds0 + LDS_PF); dma4(srd_w, o, g * 128 * (FF * 2), lds0 + LDS_PF); }
        }
        if (w < 7) {
            const __amdgpu_buffer_rsrc_t rw = __builtin_amdgcn_make_buffer_rsrc(ws + WS_WO, 0, 8 * WOIMG, 0x00020000);
            __builtin_amdgcn_raw_buffer_store_b128(wov, rw, wo_act ? (unsigned)(cgp * WOIMG + wp * 1024 + l * 16) : 0xfffffff0u, 0, 16);
            const __amdgpu_buffer_rsrc_t ro = __builtin_amdgcn_make_buffer_rsrc(ws + WS_O + (size_t)wgi * 128 * 80, 0, 128 * 80, 0x00020000);
            const u32x4 e = (u32x4){O_EMPTY, O_EMPTY, O_EMPTY, O_EMPTY};
            __builtin_amdgcn_raw_buffer_store_b128(e, ro, tid * 16, 0, 16);
            __builtin_amdgcn_raw_buffer_store_b128(e, ro, (tid + 448) * 16, 0, 16);
        }
        STAMP(1, 12);
        if (w == 0) wait_tags(flags, FL_PRO + (l < 16 ? (r * 16 + l) * 4 : ((l - 16) * 16 + s) * 4), l < 32, tag);
        asm volatile("s_barrier" ::: "memory");
#pragma unroll
        for (int i = 3; i < 6; ++i) piece1k(0, 0, i, 2);
#pragma unroll
        for (int i = 3; i < 6; ++i) piece1k(1, ST1, i, 2);
    }
    const int lg = fresh_lane();
    const unsigned a_off = ((lg & 3) >> 1) * 1024 + 16 * (4 * (4 * (lg >> 4) + ((lg >> 2) & 3)) + (fg ^ (2 * ((lg & 3) >> 1) + ((lg >> 4) & 1)))) + 8 * (lg & 1);

    f32x4 acc[10][4];
#pragma unroll
    for (int m = 0; m < 10; ++m)
#pragma unroll
        for (int n = 0; n < 4; ++n) acc[m][n] = (f32x4){0.f, 0.f, 0.f, 0.f};
    bf16x8 ca[2], bA[4], bB[4];
    {
        const unsigned bo = 40960 + tg * 4096 + lg * 16;
        WAIT_BAR(0);
        STAMP(1, 1);
        const bool q3 = w == 3, w4 = w < 3 || w == 7, x4 = w == 3, w5 = w == 7;
        const i32x4 srd3 = q3 ? srd_q : srd_w, srd4 = w4 ? srd_w : x4 ? srd_x : srd_q, srd5 = w5 ? srd_w : srd_x;
        const unsigned vo3 = q3 ? vo_q : vo_w, vo4 = w4 ? vo_w : x4 ? vo_x : vo_q, vo5 = w5 ? vo_w : vo_x;
        const unsigned soW = 4 * dR * (FF * 2) + 288 * s * 2, soQ = 4 * dR * 2048 + (64 * s + 32 * dmat) * 2, soX = r * 18 * 8192;
        const unsigned stW = 32 * FF * 2, stQ = 32 * 2048, stX = 8192;
        const unsigned so3 = q3 ? soQ : soW + (dcg0 + 3) * 64, st3 = q3 ? stQ : stW, lo3 = q3 ? dlds + 9 * 4096 : dlds + (dcg0 + 3) * 4096;
        const int cg4 = w < 3 ? 4 : 3;
        const unsigned so4 = w4 ? soW + cg4 * 64 : x4 ? soX + 7 * 1024 : soQ, st4 = w4 ? stW : x4 ? stX : stQ, lo4 = w4 ? dlds + cg4 * 4096 : x4 ? lds0 + 40960 + 7 * 1024 : dlds + 9 * 4096;
        const unsigned so5 = w5 ? soW + 4 * 64 : soX + w * 1024, st5 = w5 ? stW : stX, lo5 = w5 ? dlds + 4 * 4096 : lds0 + 40960 + w * 1024;
        unsigned vo_pf;
        { const int lp = fresh_lane(); vo_pf = (lp & 7) < 5 ? (unsigned)(((lp >> 3) * 16 + r) * (FF * 2) + ((288 * s * 2) & ~127) + (lp & 7) * 128) : 0x80000000u; }
        auto stage1 = [&](int j, unsigned slot, int i) {
            if (i < 3) dma16(srd_w, vo_w, soW + (dcg0 + i) * 64 + j * stW, dlds + (dcg0 + i) * 4096 + slot);
            else if (i == 3) DMA345(srd3, vo3, so3 + j * st3, lo3 + slot);
            else if (i == 4) DMA345(srd4, vo4, so4 + j * st4, lo4 + slot);
            else if (i == 5) DMA345(srd5, vo5, so5 + j * st5, lo5 + slot);
            else if (w >= 6) {
                const int t = j - 2;
                if (t == 6) { const int lp = fresh_lane(); vo_pf = (w == 7 && (lp & 15) < 9) ? (unsigned)((288 * s + (lp >> 4) * 16 + r) * (DM * 2) + (lp & 15) * 128) : 0x80000000u; }
                const unsigned so = t < 6 ? ((t & 1) ? 0x70000000u : (unsigned)(2 + (t >> 1)) * 128 * (FF * 2)) : t < 11 ? (unsigned)(t - 6) * 64 * (DM * 2) : 0x70000000u;
                dma4(t < 6 ? srd_w : make_srd(wd, FF * DM * 2), vo_pf, so, lds0 + LDS_PF);
            }
        };
        gemm_step<10, true, 7, 1>(acc, ca, bB, bA, L + a_off, L + bo, [&](int i) { stage1(2, 2 * ST1, i); });
        unsigned slot = ST1, slot2 = 0;
        if (w >= 6) { WAIT_BAR(7); } else { WAIT_BAR(6); }
        gemm_step<10, false, 7, 1>(acc, ca, bA, bB, L + slot + a_off, L + slot + bo, [&](int i) { stage1(3, slot2, i); });
        slot = 2 * ST1; slot2 = ST1;
#pragma unroll 1
        for (int j = 2; j < 16; j += 2) {
            if (w >= 6) { WAIT_BAR(8); } else { WAIT_BAR(6); }
            gemm_step<10, false, 7, 1>(acc, ca, bB, bA, L + slot + a_off, L + slot + bo, [&](int i) { stage1(j + 2, slot2, i); });
            slot = (slot == 2 * ST1) ? 0 : slot + ST1; slot2 = (slot2 == 2 * ST1) ? 0 : slot2 + ST1;
            if (w >= 6) { WAIT_BAR(8); } else { WAIT_BAR(6); }
            gemm_step<10, false, 7, 1>(acc, ca, bA, bB, L + slot + a_off, L + slot + bo, [&](int i) { stage1(j + 3, slot2, i); });
            slot = (slot == 2 * ST1) ? 0 : slot + ST1; slot2 = (slot2 == 2 * ST1) ? 0 : slot2 + ST1;
        }
#pragma unroll
        for (int n = 0; n < 4; ++n) bA[n] = bB[n];
#pragma unroll 1
        for (int j = 16; j < 18; ++j) {
            if (j == 16 && w >= 6) { WAIT_BAR(8); } else if (j == 16) { WAIT_BAR(6); } else { WAIT_BAR(5); }
            gemm_step<10, false, 5, 1, 3, true>(acc, ca, bA, bB, L + slot + a_off, L + slot + bo, [&](int i) { piece2(j - 16, j == 16 ? LDS_D0 : LDS_D1, i); });
#pragma unroll
            for (int n = 0; n < 4; ++n) bA[n] = bB[n];
            slot += ST1;
        }
        gemm_flush<10>(acc, ca, bA);
        SB(); FENCE();
    }
    WAIT_BAR(0);
    STAMP(1, 2);
    float kn2 = 0.f;
    if ((fg & 1) && tg == (s >> 3)) {
        const int l = fresh_lane(), tok = l & 15, g4 = l >> 4;
        const int e = s >> 1;
        f32x4 cv = acc[9][0];
        if ((e & 3) == 1) cv = acc[9][1]; else if ((e & 3) == 2) cv = acc[9][2]; else if ((e & 3) == 3) cv = acc[9][3];
        const int c0 = (fg == 1 ? 0 : 12) + 4 * (g4 - 1);
        const bool act = g4 >= 1 && c0 < 18;
        const unsigned p0 = cvtpk(cv[0], cv[1]), p1 = cvtpk(cv[2], cv[3]);
        const int kt = 4 * r + (e >> 1);
        char* kvb = ws + WS_KV + ((kt >> 3) & 3) * KVCH; const int lt = 8 * (kt >> 5) + (kt & 7);
        if ((s & 1) == 0) {
            const int key = 16 * (e & 1) + tok;
            if (act) {
                if (c0 < 16) st_agent64(kvb + lt * 1024 + ((c0 >> 3) * 32 + key) * 16 + (c0 & 4) * 2, p0, p1);
                else __hip_atomic_store((GAS unsigned*)(kvb + KV_K2 + lt * 128 + key * 4), p0, RLX_AGENT);
            }
            float n2 = 0.f;
            if (act) { n2 = bflo(p0) * bflo(p0) + bfhi(p0) * bfhi(p0); if (c0 < 16) n2 += bflo(p1) * bflo(p1) + bfhi(p1) * bfhi(p1); }
            kn2 = n2;
        } else {
            if (fg == 1 && l < 2) {
                char* op = kvb + KV_V + lt * 1216 + (e & 1) * 608 + l * 304 + 18 * 16;
                st_agent64(op, 0x3f803f80u, 0x3f803f80u); st_agent64(op + 8, 0x3f803f80u, 0x3f803f80u);
            }
            const int hv = (tok >> 2) & 1, jj = 4 * (tok >> 3) + (tok & 3);
            GAS u16* vb = (GAS u16*)(kvb + KV_V + lt * 1216 + (e & 1) * 608 + hv * 304 + jj * 2);
            if (act) {
                __hip_atomic_store(vb + (c0 + 0) * 8, (u16)(p0 & 0xffff), RLX_AGENT);
                __hip_atomic_store(vb + (c0 + 1) * 8, (u16)(p0 >> 16), RLX_AGENT);
                if (c0 < 16) { __hip_atomic_store(vb + (c0 + 2) * 8, (u16)(p1 & 0xffff), RLX_AGENT);
                               __hip_atomic_store(vb + (c0 + 3) * 8, (u16)(p1 >> 16), RLX_AGENT); }
            }
        }
    }
    {
        const int l = fresh_lane(), tok = l & 15, g4 = l >> 4;
#pragma unroll
        for (int cg = 0; cg < 9; ++cg)
#pragma unroll
            for (int pr = 0; pr < 2; ++pr) {
                float h[4];
#pragma unroll
                for (int e = 0; e < 4; ++e) {
                    auto sw = __builtin_amdgcn_permlane32_swap(__float_as_uint(acc[cg][2 * pr][e]), __float_as_uint(acc[cg][2 * pr + 1][e]), false, false);
                    h[e] = __int_as_float(max((int)sw[0], 0)) * __uint_as_float(sw[1]);
                }
                const int n = 4 * tg + 2 * pr + (l >> 5);
                u32x2 o; o[0] = cvtpk(h[0], h[1]); o[1] = cvtpk(h[2], h[3]);
                *(LAS u32x2*)(L + LDS_H + ((8 - cg) * 8 + n) * 1024 + (fg * 16 + tok) * 16 + 8 * (g4 & 1)) = o;
            }
        const float qs = 0.23570226039551584f * 1.4426950408889634f;
        const int hh = fg >> 1;
        unsigned p0[4], p1[4];
#pragma unroll
        for (int n = 0; n < 4; ++n) { p0[n] = cvtpk(acc[9][n][0] * qs, acc[9][n][1] * qs); p1[n] = cvtpk(acc[9][n][2] * qs, acc[9][n][3] * qs); }
        if ((fg & 1) == 0) {
            LAS char* qd = L + LDS_Q1 + (hh * 4 + 2 * tg) * 1024 + ((g4 >> 1) * 32 + tok) * 16 + (g4 & 1) * 8;
#pragma unroll
            for (int n = 0; n < 4; ++n) *(LAS u32x2*)(qd + (n >> 1) * 1024 + (n & 1) * 256) = (u32x2){p0[n], p1[n]};
        } else if (g4 == 0) {
            LAS char* qd = L + LDS_Q2 + (hh * 4 + 2 * tg) * 128 + tok * 4;
#pragma unroll
            for (int n = 0; n < 4; ++n) *(LAS unsigned*)(qd + (n >> 1) * 128 + (n & 1) * 64) = p0[n];
        }
    }
    if ((fg & 1) && tg == (s >> 3)) {
        kn2 += xor_lane<16>(kn2); kn2 = wave_max(half_sum(kn2));
        if (fresh_lane() == 0) *(LAS float*)(L + LDS_KM + (fg == 1 ? 0 : 4)) = kn2;
    }
    LBAR();
    STAMP(1, 3);
    const i32x4 srd_kv = make_srd(ws + WS_KV, 4 * KVCH);
    auto piece_kv = [&](int c, int i) {
        const int p = min(w + 8 * i, 36);
        dma16_sc1(srd_kv, vo_x, c * KVCH + p * 1024, lds0 + (c == 0 ? LDS_KV0 : (c - 1) * KVCH) + p * 1024);
    };
#pragma unroll
    for (int m = 0; m < 9; ++m)
#pragma unroll
        for (int n = 0; n < 4; ++n) acc[m][n] = (f32x4){0.f, 0.f, 0.f, 0.f};
    {
        LAS const char* hb = L + LDS_H + 8 * 8192 + 4 * tg * 1024 + l * 16;
        gemm_step<9, true, 0, 1>(acc, ca, bB, bA, L + LDS_D0 + a_off, hb, [](int) {});
        WAIT_BAR(0);
        if (w == 0 && fresh_lane() == 0) {
            st_agent64(flags + FL_KV + wgi * 16, tag, *(LAS const unsigned*)(L + LDS_KM));
            st_agent64(flags + FL_KV + wgi * 16 + 8, tag, *(LAS const unsigned*)(L + LDS_KM + 4));
        }
        gemm_step<9, false, 5, 2>(acc, ca, bA, bB, L + LDS_D1 + a_off, hb - 1 * 8192, [&](int i) { piece2(2, LDS_D0, i); });
        WAIT_BAR(0);
        gemm_step<9, false, 5, 2>(acc, ca, bB, bA, L + LDS_D0 + a_off, hb - 2 * 8192, [&](int i) { piece2(3, LDS_D1, i); });
        WAIT_BAR(0);
        gemm_step<9, false, 5, 2>(acc, ca, bA, bB, L + LDS_D1 + a_off, hb - 3 * 8192, [&](int i) { piece2(4, LDS_D0, i); });
        WAIT_BAR(0);
        gemm_step<9, false, 10, 2>(acc, ca, bB, bA, L + LDS_D0 + a_off, hb - 4 * 8192, [&](int i) { if (i < 5) piece2(5, LDS_D1, i); else piece2(6, LDS_D2, i - 5); });
        WAIT_BAR(5);
        gemm_step<9, false, 5, 1>(acc, ca, bA, bB, L + LDS_D1 + a_off, hb - 5 * 8192, [&](int i) { piece2(7, LDS_D0, i); });
        WAIT_BAR(5);
        gemm_step<9, false, 5, 1>(acc, ca, bB, bA, L + LDS_D2 + a_off, hb - 6 * 8192, [&](int i) { piece2(8, LDS_D1, i); });
        if (w == 0) {
            const int lk = fresh_lane();
            const __amdgpu_buffer_rsrc_t rk = __builtin_amdgcn_make_buffer_rsrc(flags + FL_KV, 0, 4096, 0x00020000);
            float ma, mb;
            for (unsigned spins = 0;; ++spins) {
                bool ok = true; ma = 0.f; mb = 0.f;
#pragma unroll
                for (int i = 0; i < 4; ++i) {
                    const u32x4 v = __builtin_amdgcn_raw_buffer_load_b128(rk, (i * 64 + lk) * 16, 0, 16);
                    ok = ok && v[0] == tag && v[2] == tag; ma = fmaxf(ma, __uint_as_float(v[1])); mb = fmaxf(mb, __uint_as_float(v[3]));
                }
                if (__all(ok)) break;
                if (spins > (1u << 19)) { __hip_atomic_store((GAS unsigned*)(flags + FL_TMO), tag, RLX_AGENT); break; }
                __builtin_amdgcn_s_sleep(1);
            }
            ma = wave_max(ma); mb = wave_max(mb);
            if (lk == 0) *(LAS float*)(L + LDS_KM + 8) = ma + mb;
        }
        WAIT_BAR(5);
        gemm_step<9, false, 5, 1>(acc, ca, bA, bB, L + LDS_D0 + a_off, hb - 7 * 8192, [&](int i) { piece_kv(0, i); });
        WAIT_BAR(5);
        gemm_step<9, false, 0, 1>(acc, ca, bB, bA, L + LDS_D1 + a_off, hb - 8 * 8192, [](int) {});
        gemm_flush<9>(acc, ca, bA);
        SB(); FENCE();
    }
    STAMP(1, 4);
    const int la = fresh_lane();
#define l la
    const int hh = w >> 2, kh = (w >> 1) & 1, cp = w & 1;
#define h5 (l >> 5)
    bf16x8 q1[2]; u32x2 q2[2];
#pragma unroll
    for (int c = 0; c < 2; ++c) {
        q1[c] = lds_b128(L + LDS_Q1 + (hh * 4 + 2 * cp + c) * 1024 + l * 16);
        q2[c][0] = *(LAS const unsigned*)(L + LDS_Q2 + (hh * 4 + 2 * cp + c) * 128 + (l & 31) * 4);
    }
    u32x4 pk[18];
#pragma unroll
    for (int m = 0; m < 9; ++m)
#pragma unroll
        for (int pr = 0; pr < 2; ++pr) {
            u32x4 o; o[0] = cvtpk(acc[m][2 * pr][0], acc[m][2 * pr][1]); o[1] = cvtpk(acc[m][2 * pr][2], acc[m][2 * pr][3]);
            o[2] = cvtpk(acc[m][2 * pr + 1][0], acc[m][2 * pr + 1][1]); o[3] = cvtpk(acc[m][2 * pr + 1][2], acc[m][2 * pr + 1][3]);
            pk[m * 2 + pr] = o;
        }
    {
        const float kmax2 = *(LAS const float*)(L + LDS_KM + 8);
#pragma unroll
        for (int c = 0; c < 2; ++c) {
            const u32x4 a = __builtin_bit_cast(u32x4, q1[c]); const unsigned t = h5 ? 0u : q2[c][0];
            float n2 = bflo(t) * bflo(t) + bfhi(t) * bfhi(t);
#pragma unroll
            for (int e = 0; e < 4; ++e) n2 += bflo(a[e]) * bflo(a[e]) + bfhi(a[e]) * bfhi(a[e]);
            n2 = half_sum(n2);
            const float cb = sqrtf(n2 * kmax2) * 1.001f + 0.01f;
            const float chi = rbf(cb), clo = cb - chi;
            q2[c][0] = t; q2[c][1] = h5 ? 0u : cvtpk(-chi, -clo);
        }
    }
    if (w == 0 && l < 40) *(LAS u32x4*)(L + LDS_ZERO + l * 16) = (u32x4){0u, 0u, 0u, 0u};
    if (w == 0 && l == 40) *(LAS unsigned*)(L + LDS_CTL) = 0u;
    WAIT_BAR(0);
    STAMP(1, 5);
#undef l
    const int lb = fresh_lane();
#define l lb
    const unsigned kone = h5 ? 0u : 0x3f803f80u;
    const bool vrow = (l & 31) < 19;
    const unsigned k1_base = 8 * kh * 1024 + l * 16, k2_base = KV_K2 + 8 * kh * 128 + (l & 31) * 4;
    const unsigned v_base = vrow ? KV_V + 8 * kh * 1216 + h5 * 304 + (l & 31) * 16 : LDS_ZERO, v_mul = vrow ? 1u : 0u;
    const int tgf = s >> 3, cgp = s & 7, nmt = cgp < 4 ? 5 : 4, m0 = cgp < 4 ? 5 * cgp : 20 + 4 * (cgp - 4);
    const int pr = w & 1, qk = w >> 1;
    const i32x4 srd_wo = make_srd(ws + WS_WO + (size_t)cgp * WOIMG, WOIMG);
    auto piece_wo = [&](int p) { dma16(srd_wo, vo_x, p * 1024, lds0 + wo_lds(p)); };
    const __amdgpu_buffer_rsrc_t rpart = __builtin_amdgcn_make_buffer_rsrc(ws + WS_PART + (size_t)r * 16 * 8 * 18 * 1024, 0, 16 * 8 * 18 * 1024, 0x00020000);
    float own[11];
    u32x4 pl[5][4];

    f32x16 o0, o1, sX, sY; bf16x8 kA, kB, v0A, v1A, v0B, v1B; unsigned k2A, k2B; u32x4 p0, p1; float e3[4];
    unsigned aK, aK2, aV;
#define SLOT(SC, SN, KN, K2N, CN, OP, V1P, OC, V0C) do { float ea[4], eb[4]; \
        SN = MFMA32(KN, q1[CN], (f32x16){}); SB(); \
        ea[0] = EXP2(SC[0]); ea[1] = EXP2(SC[1]); ea[2] = EXP2(SC[2]); ea[3] = EXP2(SC[3]); \
        p1[2] = cvtpk(e3[0], e3[1]); p1[3] = cvtpk(e3[2], e3[3]); SB(); \
        OP = MFMA32(V1P, __builtin_bit_cast(bf16x8, p1), OP); SB(); \
        eb[0] = EXP2(SC[4]); eb[1] = EXP2(SC[5]); eb[2] = EXP2(SC[6]); eb[3] = EXP2(SC[7]); \
        p0[0] = cvtpk(ea[0], ea[1]); p0[1] = cvtpk(ea[2], ea[3]); SB(); \
        SN = MFMA32K8(__builtin_bit_cast(s16x4, ((u32x2){K2N, kone})), __builtin_bit_cast(s16x4, q2[CN]), SN); SB(); \
        ea[0] = EXP2(SC[8]); ea[1] = EXP2(SC[9]); ea[2] = EXP2(SC[10]); ea[3] = EXP2(SC[11]); \
        p0[2] = cvtpk(eb[0], eb[1]); p0[3] = cvtpk(eb[2], eb[3]); SB(); \
        OC = MFMA32(V0C, __builtin_bit_cast(bf16x8, p0), OC); SB(); \
        e3[0] = EXP2(SC[12]); e3[1] = EXP2(SC[13]); e3[2] = EXP2(SC[14]); e3[3] = EXP2(SC[15]); \
        p1[0] = cvtpk(ea[0], ea[1]); p1[1] = cvtpk(ea[2], ea[3]); SB(); } while (0)
#define PAIR(DK, DK2, DV, H0, H1, H2, H3) do { \
        kB = lds_b128(L + aK + 1024); k2B = *(LAS const unsigned*)(L + aK2 + 128); v0B = lds_b128(L + aV + v_mul * 1216); SB(); \
        H0; SLOT(sX, sY, kA, k2A, 1, o1, v1B, o0, v0A); \
        v1B = lds_b128(L + aV + v_mul * 1216 + 608); SB(); \
        H1; SLOT(sY, sX, kB, k2B, 0, o0, v1A, o1, v0A); \
        aK += (DK); aK2 += (DK2); aV += v_mul * (DV); \
        kA = lds_b128(L + aK); k2A = *(LAS const unsigned*)(L + aK2); v0A = lds_b128(L + aV); SB(); \
        H2; SLOT(sX, sY, kB, k2B, 1, o1, v1A, o0, v0B); \
        v1A = lds_b128(L + aV + 608); SB(); \
        H3; SLOT(sY, sX, kA, k2A, 0, o0, v1B, o1, v0B); } while (0)
#define PAIR_IN(H0, H1, H2, H3) PAIR(2048, 256, 2432, H0, H1, H2, H3)
#define PAIR_JUMP(J, H0, H1, H2, H3) PAIR((J) - 6 * 1024, (J) - 6 * 128, (J) - 6 * 1216, H0, H1, H2, H3)
#define NOP_ ((void)0)
#define ATT_INIT() do { o0 = (f32x16){}; o1 = (f32x16){}; aK = k1_base + LDS_KV0; aK2 = k2_base + LDS_KV0; aV = v_base + v_mul * LDS_KV0; \
        kA = lds_b128(L + aK); k2A = *(LAS const unsigned*)(L + aK2); v0A = lds_b128(L + aV); v1A = lds_b128(L + aV + 608); v1B = v1A; \
        sX = MFMA32(kA, q1[0], (f32x16){}); sX = MFMA32K8(__builtin_bit_cast(s16x4, ((u32x2){k2A, kone})), __builtin_bit_cast(s16x4, q2[0]), sX); \
        p1 = (u32x4){0u, 0u, 0u, 0u}; e3[0] = e3[1] = e3[2] = e3[3] = 0.f; } while (0)
#define ATT_FIN() do { p1[2] = cvtpk(e3[0], e3[1]); p1[3] = cvtpk(e3[2], e3[3]); o1 = MFMA32(v1B, __builtin_bit_cast(bf16x8, p1), o1); } while (0)
    constexpr int J01 = 0 - LDS_KV0, J12 = KVCH;
    {
        const __amdgpu_buffer_rsrc_t rp = __builtin_amdgcn_make_buffer_rsrc(ws + WS_PART + (size_t)(r * 16 + s) * 8 * 18 * 1024, 0, 8 * 18 * 1024, 0x00020000);
#define KV(C, I) do { piece_kv(C, I); SB(); } while (0)
#define ST(I) do { __builtin_amdgcn_raw_buffer_store_b128(pk[I], rp, vo_x, (w * 18 + (I)) * 1024, 16); SB(); } while (0)
#define WO(P0, I) do { piece_wo(min((P0) + w + 8 * (I), (P0) + 36)); SB(); } while (0)
#define PL(T, SP) do { const int m_ = m0 + ((T) < nmt ? (T) : 0); \
            pl[T][SP] = __builtin_amdgcn_raw_buffer_load_b128(rpart, vo_x, ((((4 * qk + (SP)) * 8 + tgf * 4 + (m_ & 3)) * 9 + (m_ >> 2)) * 2 + pr) * 1024, 16); SB(); } while (0)
#define PL2(A) do { PL((A) >> 2, (A) & 3); PL(((A) + 1) >> 2, ((A) + 1) & 3); } while (0)
        ATT_INIT();
        PAIR_IN(KV(1, 0), KV(1, 1), KV(1, 2), KV(1, 3));
        PAIR_IN(KV(1, 4), ST(0), KV(2, 0), ST(1));
        PAIR_IN(KV(2, 1), ST(2), KV(2, 2), ST(3));
        WAIT_BAR(7);
        PAIR_JUMP(J01, KV(2, 3), ST(4), KV(2, 4), ST(5));
        PAIR_IN(KV(3, 0), ST(6), KV(3, 1), ST(7));
        PAIR_IN(KV(3, 2), ST(8), KV(3, 3), ST(9));
        PAIR_IN(KV(3, 4), ST(10), NOP_, ST(11));
        WAIT_BAR(12);
        PAIR_JUMP(J12, WO(0, 0), ST(12), WO(0, 1), ST(13));
        PAIR_IN(WO(0, 2), ST(14), WO(0, 3), ST(15));
        PAIR_IN(WO(0, 4), ST(16), NOP_, ST(17));
        PAIR_IN(NOP_, NOP_, NOP_, NOP_);
        WAIT_BAR(0);
        if (w == 0 && fresh_lane() == 0) __hip_atomic_store((GAS unsigned*)(flags + FL_P + wgi * 4), tag, RLX_AGENT);
        PAIR_JUMP(J12, WO(37, 0), WO(37, 1), WO(37, 2), WO(37, 3));
        const unsigned pflag = __hip_atomic_load((GAS unsigned*)(flags + FL_P + (r * 16 + (l & 15)) * 4), RLX_AGENT);
        PAIR_IN(WO(37, 4), NOP_, NOP_, NOP_);
        LBAR();
        if (!__all(pflag == tag)) wait_tags(flags, FL_P + (r * 16 + (l & 15)) * 4, true, tag);
#define WP(I, A) do { WO(74, I); PL((A) >> 2, (A) & 3); } while (0)
        PAIR_IN(WP(0, 0), WP(1, 1), WP(2, 2), WP(3, 3));
        PAIR_IN(WP(4, 4), PL2(5), PL2(7), PL2(9));
        PAIR(0, 0, 0, PL2(11), PL2(13), PL2(15), PL2(17));
        PL(4, 3);
        ATT_FIN();
    }
    STAMP(1, 6);
#undef l
    const int lc = fresh_lane();
#define l lc
    for (int attempt = 0;; ++attempt) {
        if (attempt == 1) {
            const unsigned kone = h5 ? 0u : 0x3f803f80u;
            const bool vrow = (l & 31) < 19;
            const unsigned k1_base = 8 * kh * 1024 + l * 16, k2_base = KV_K2 + 8 * kh * 128 + (l & 31) * 4;
            const unsigned v_base = vrow ? KV_V + 8 * kh * 1216 + h5 * 304 + (l & 31) * 16 : LDS_ZERO, v_mul = vrow ? 1u : 0u;
            WAIT_BAR(0);
#pragma unroll
            for (int c = 0; c < 4; ++c)
#pragma unroll
                for (int i = 0; i < 5; ++i) piece_kv(c, i);
            if (w == 0 && l < 40) *(LAS u32x4*)(L + LDS_ZERO + l * 16) = (u32x4){0u, 0u, 0u, 0u};
            WAIT_BAR(0);
            float m[2] = {-INFINITY, -INFINITY};
            for (int i = 0; i < 32; ++i) {
                const unsigned to = (i >> 3) == 0 ? LDS_KV0 : ((i >> 3) - 1) * KVCH;
                const bf16x8 k1 = lds_b128(L + k1_base + to + (i & 7) * 1024);
                const u32x2 k2 = {*(LAS const unsigned*)(L + k2_base + to + (i & 7) * 128), kone};
#pragma unroll
                for (int c = 0; c < 2; ++c) {
                    const u32x2 qz = {q2[c][0], 0u};
                    f32x16 sc = MFMA32(k1, q1[c], (f32x16){});
                    sc = MFMA32K8(__builtin_bit_cast(s16x4, k2), __builtin_bit_cast(s16x4, qz), sc);
#pragma unroll
                    for (int e = 0; e < 16; ++e) m[c] = fmaxf(m[c], sc[e]);
                }
            }
            LAS float* mx = (LAS float*)(L + LDS_MX);
#pragma unroll
            for (int c = 0; c < 2; ++c) { m[c] = half_max(m[c]); if (l < 32) mx[(w * 2 + c) * 32 + l] = m[c]; }
            LBAR();
#pragma unroll
            for (int c = 0; c < 2; ++c) {
                const float mm = fmaxf(m[c], mx[((w ^ 2) * 2 + c) * 32 + (l & 31)]);
                const float chi = rbf(mm), clo = mm - chi;
                q2[c][1] = h5 ? 0u : cvtpk(-chi, -clo);
            }
            o0 = (f32x16){}; o1 = (f32x16){};
            for (int i = 0; i < 32; ++i) {
                const unsigned to = (i >> 3) == 0 ? LDS_KV0 : ((i >> 3) - 1) * KVCH;
                const bf16x8 k1 = lds_b128(L + k1_base + to + (i & 7) * 1024);
                const u32x2 k2 = {*(LAS const unsigned*)(L + k2_base + to + (i & 7) * 128), kone};
                const unsigned va = v_base + v_mul * (to + (i & 7) * 1216);
                const bf16x8 v0 = lds_b128(L + va), v1 = lds_b128(L + va + 608);
#pragma unroll
                for (int c = 0; c < 2; ++c) {
                    f32x16 sc = MFMA32(k1, q1[c], (f32x16){});
                    sc = MFMA32K8(__builtin_bit_cast(s16x4, k2), __builtin_bit_cast(s16x4, q2[c]), sc);
                    u32x4 pa, pb;
#pragma unroll
                    for (int e = 0; e < 4; ++e) { pa[e] = cvtpk(EXP2(sc[2 * e]), EXP2(sc[2 * e + 1])); pb[e] = cvtpk(EXP2(sc[8 + 2 * e]), EXP2(sc[9 + 2 * e])); }
                    if (c == 0) { o0 = MFMA32(v0, __builtin_bit_cast(bf16x8, pa), o0); o0 = MFMA32(v1, __builtin_bit_cast(bf16x8, pb), o0); }
                    else { o1 = MFMA32(v0, __builtin_bit_cast(bf16x8, pa), o1); o1 = MFMA32(v1, __builtin_bit_cast(bf16x8, pb), o1); }
                }
            }
        }
        LAS float* red = (LAS float*)(L + LDS_RED);
        LBAR();
        if (attempt == 0) {
            piece_wo(111 + w); if (w == 0) piece_wo(119);
        } else {
#pragma unroll
            for (int q = 0; q < 15; ++q) piece_wo(w * 15 + q);
        }
#pragma unroll
        for (int e = 0; e < 11; ++e) { red[(w * 11 + e) * 64 + l] = kh ? o0[e] : o1[e]; own[e] = kh ? o1[e] : o0[e]; }
        LBAR();
#pragma unroll
        for (int e = 0; e < 11; ++e) own[e] += red[((w ^ 2) * 11 + e) * 64 + l];
        const float lsum = half_lo(own[10]);
        if (!(lsum >= 8.67e-19f && lsum < 3.0e38f) && attempt == 0) *(LAS unsigned*)(L + LDS_CTL) = 1u;
        const float rl = 1.0f / lsum;
        LAS char* ot = L + LDS_OT + (32 * (2 * cp + kh) + (l & 31)) * 80 + 40 * hh;
        *(LAS u32x2*)(ot + 8 * h5) = (u32x2){cvtpk(own[0] * rl, own[1] * rl), cvtpk(own[2] * rl, own[3] * rl)};
        *(LAS u32x2*)(ot + 16 + 8 * h5) = (u32x2){cvtpk(own[4] * rl, own[5] * rl), cvtpk(own[6] * rl, own[7] * rl)};
        if (h5 == 0) *(LAS u32x2*)(ot + 32) = (u32x2){cvtpk(own[8] * rl, own[9] * rl), 0u};
        LBAR();
        const unsigned isbad = *(LAS const unsigned*)(L + LDS_CTL);
        if (!(isbad && attempt == 0)) break;
    }
    STAMP(1, 7);
    {
        const __amdgpu_buffer_rsrc_t rw = __builtin_amdgcn_make_buffer_rsrc(ws + WS_O + (size_t)(r * 16 + s) * 128 * 80, 0, 128 * 80, 0x00020000);
        __builtin_amdgcn_raw_buffer_store_b128(*(LAS const u32x4*)(L + LDS_OT + w * 1024 + l * 16), rw, l * 16, w * 1024, 16);
        if (w < 2) __builtin_amdgcn_raw_buffer_store_b128(*(LAS const u32x4*)(L + LDS_OT + (8 + w) * 1024 + l * 16), rw, l * 16, (8 + w) * 1024, 16);
    }
    {
        const int tok = l & 15, g4 = l >> 4, q4 = (l >> 2) & 3, p4 = l & 3;
        u32x2 xv[3];
#pragma unroll
        for (int u = 0; u < 3; ++u) {
            const int tt = min(qk + 4 * u, 2 * nmt - 1);
            xv[u] = *(const u32x2*)(x + (size_t)(128 * r + 64 * tgf + 32 * pr + 16 * (tt & 1) + tok) * DM + 16 * (m0 + (tt >> 1)) + 4 * g4);
        }
        const __amdgpu_buffer_rsrc_t ro = __builtin_amdgcn_make_buffer_rsrc(ws + WS_O + (size_t)r * 16 * 128 * 80, 0, 16 * 128 * 80, 0x00020000);
        u32x4 bf[5][2]; unsigned bo[5];
#pragma unroll
        for (int i = 0; i < 5; ++i) {
            const int kp = 32 * (5 * qk + i) + 8 * g4, sp = kp / 40, d = kp - 40 * sp;
            bo[i] = (sp * 128 + 64 * tgf + 32 * pr + tok) * 80 + d * 2;
#pragma unroll
            for (int nn = 0; nn < 2; ++nn) bf[i][nn] = __builtin_amdgcn_raw_buffer_load_b128(ro, bo[i], nn * 16 * 80, 16);
        }
        SB();
        f32x4 fa[5][2];
#pragma unroll
        for (int t = 0; t < 5; ++t) {
            fa[t][0] = (f32x4){0.f, 0.f, 0.f, 0.f}; fa[t][1] = (f32x4){0.f, 0.f, 0.f, 0.f};
#pragma unroll
            for (int sp = 0; sp < 4; ++sp) {
                const u32x4 p = pl[t][sp];
                fa[t][0][0] += bflo(p[0]); fa[t][0][1] += bfhi(p[0]); fa[t][0][2] += bflo(p[1]); fa[t][0][3] += bfhi(p[1]);
                fa[t][1][0] += bflo(p[2]); fa[t][1][1] += bfhi(p[2]); fa[t][1][2] += bflo(p[3]); fa[t][1][3] += bfhi(p[3]);
            }
        }
        STAMP(1, 8);
        WAIT_BAR(0);
        bf16x8 af[5][5];
        {
            const unsigned ao = (4 * g4 + q4) * 64 + 8 * (p4 & 1);
#pragma unroll
            for (int t = 0; t < 5; ++t)
#pragma unroll
                for (int i = 0; i < 5; ++i) {
                    const int p = ((t < nmt ? t : 0) >> 1) * 40 + (5 * qk + i) * 2;
                    const unsigned sw = ao + 16 * ((2 * (t & 1) + (p4 >> 1)) ^ (2 * (g4 & 1)));
                    const s16x4 lo = lds_tr(L + wo_lds(p) + sw), hi = lds_tr(L + wo_lds(p + 1) + sw);
                    af[t][i] = __builtin_shufflevector(lo, hi, 0, 1, 2, 3, 4, 5, 6, 7);
                }
        }
        LBAR();
        unsigned pend = 0x3ffu;
        for (unsigned spins = 0;; ++spins) {
#pragma unroll
            for (int i = 0; i < 5; ++i)
#pragma unroll
                for (int nn = 0; nn < 2; ++nn)
                    if ((pend & (1u << (2 * i + nn))) && __all(bf[i][nn][0] != O_EMPTY && bf[i][nn][1] != O_EMPTY && bf[i][nn][2] != O_EMPTY && bf[i][nn][3] != O_EMPTY)) pend &= ~(1u << (2 * i + nn));
            if (pend == 0u) break;
            if (spins > (1u << 17)) { __hip_atomic_store((GAS unsigned*)(flags + FL_TMO), tag, RLX_AGENT); break; }
            __builtin_amdgcn_s_sleep(OSLEEP);
#pragma unroll
            for (int i = 0; i < 5; ++i)
#pragma unroll
                for (int nn = 0; nn < 2; ++nn)
                    if (pend & (1u << (2 * i + nn))) bf[i][nn] = __builtin_amdgcn_raw_buffer_load_b128(ro, bo[i], nn * 16 * 80, 16);
        }
        STAMP(1, 9);
#pragma unroll
        for (int i = 0; i < 5; ++i)
#pragma unroll
            for (int t = 0; t < 5; ++t) {
                fa[t][0] = MFMA16(af[t][i], __builtin_bit_cast(bf16x8, bf[i][0]), fa[t][0]); fa[t][1] = MFMA16(af[t][i], __builtin_bit_cast(bf16x8, bf[i][1]), fa[t][1]);
            }
        STAMP(1, 10);
        LAS f32x4* ex = (LAS f32x4*)L;
#pragma unroll
        for (int t = 0; t < 5; ++t) { ex[(w * 10 + 2 * t) * 64 + l] = fa[t][0]; ex[(w * 10 + 2 * t + 1) * 64 + l] = fa[t][1]; }
        LBAR();
#pragma unroll
        for (int u = 0; u < 3; ++u) {
            const int tt = qk + 4 * u;
            if (tt < 2 * nmt) {
                f32x4 v = ex[((0 * 2 + pr) * 10 + tt) * 64 + l];
#pragma unroll
                for (int k = 1; k < 4; ++k) v += ex[((k * 2 + pr) * 10 + tt) * 64 + l];
                const size_t off = (size_t)(128 * r + 64 * tgf + 32 * pr + 16 * (tt & 1) + tok) * DM + 16 * (m0 + (tt >> 1)) + 4 * g4;
                *(u32x2*)(out + off) = (u32x2){cvtpk(v[0] + bflo(xv[u][0]), v[1] + bfhi(xv[u][0])), cvtpk(v[2] + bflo(xv[u][1]), v[3] + bfhi(xv[u][1]))};
            }
        }
    }
#undef l
#undef h5
    STAMP(1, 11); STAMP_END(1);
    asm volatile("fused_block_code_end:");
}

extern "C" void kernel_launch(void* const* d_in, const int* in_sizes, int n_in, void* d_out, int out_size, void* d_ws, size_t ws_size, hipStream_t stream) {
    const u16* x = (const u16*)d_in[0]; const u16* wq = (const u16*)d_in[1]; const u16* wk = (const u16*)d_in[2]; const u16* wv = (const u16*)d_in[3];
    const u16* wo = (const u16*)d_in[4]; const u16* wg = (const u16*)d_in[6]; const u16* wu = (const u16*)d_in[7]; const u16* wd = (const u16*)d_in[8];
    fused_block<<<256, 512, 0, stream>>>(x, wq, wk, wv, wo, wg, wu, wd, (char*)d_ws, (u16*)d_out);
}
```

```cpp
#include <hip/hip_runtime.h>
#include <hip/hip_bf16.h>
#include <stdint.h>
#include <type_traits>

typedef unsigned short u16;
typedef short bf16x8 __attribute__((ext_vector_type(8)));
typedef short s16x4 __attribute__((ext_vector_type(4)));
typedef float f32x4 __attribute__((ext_vector_type(4)));
typedef float f32x16 __attribute__((ext_vector_type(16)));
typedef int i32x4 __attribute__((ext_vector_type(4)));
typedef unsigned u32x4 __attribute__((ext_vector_type(4)));
typedef unsigned u32x2 __attribute__((ext_vector_type(2)));
typedef __bf16 bf16x2_t __attribute__((ext_vector_type(2)));
typedef float f32x2 __attribute__((ext_vector_type(2)));
typedef short v4i16_t __attribute__((ext_vector_type(4)));
extern "C" __device__ unsigned long long amd_dispatch_id() __asm("llvm.amdgcn.dispatch.id");
#define LAS __attribute__((address_space(3)))
#define GAS __attribute__((address_space(1)))
#define DI __device__ __forceinline__

constexpr int SEQ = 2048, DM = 576, NH = 32, DH = 18, FF = 4608;

constexpr int KVCH = 37888;
constexpr int KV_V = 16384, KV_K2 = 35840;
constexpr int WOIMG = 120 * 1024;
constexpr size_t WS_FLAGS = 0;
constexpr size_t WS_XN    = 16384;
constexpr size_t WS_WQX   = WS_XN + (size_t)SEQ * DM * 2;
constexpr size_t WS_WO    = WS_WQX + (size_t)DM * 1024 * 2;
constexpr size_t WS_KV    = WS_WO + (size_t)8 * WOIMG;
constexpr size_t WS_O     = WS_KV + 4 * KVCH;
constexpr size_t WS_PART  = WS_O + (size_t)256 * 128 * 80;
constexpr size_t WS_END   = WS_PART + (size_t)256 * 8 * 18 * 1024;
constexpr int FL_PRO = 0, FL_P = 1024, FL_KV = 2048, FL_TMO = 8192;
constexpr unsigned O_EMPTY = 0xffffffffu;

DI float bflo(unsigned v) { return __uint_as_float(v << 16); }
DI float bfhi(unsigned v) { return __uint_as_float(v & 0xffff0000u); }
DI unsigned cvtpk(float lo, float hi) { return __builtin_bit_cast(unsigned, __builtin_convertvector((f32x2){lo, hi}, bf16x2_t)); }
DI float rbf(float f) { return bflo(cvtpk(f, f)); }
DI i32x4 make_srd(const void* p, unsigned bytes) {
    unsigned long long a = (unsigned long long)p; i32x4 s;
    s.x = __builtin_amdgcn_readfirstlane((unsigned)a); s.y = __builtin_amdgcn_readfirstlane((unsigned)(a >> 32) & 0xffffu);
    s.z = (int)bytes; s.w = 0x00020000; return s;
}
DI void dma16(i32x4 srd, unsigned voff, unsigned soff, unsigned lds_addr) {
    unsigned keep;
    asm volatile("s_mov_b32 %0, m0\n\ts_mov_b32 m0, %4\n\ts_nop 0\n\tbuffer_load_dwordx4 %1, %2, %3 offen lds\n\ts_mov_b32 m0, %0"
                 : "=&s"(keep) : "v"(voff), "s"(srd), "s"(soff), "s"(lds_addr) : "memory");
}
DI void dma16_sc1(i32x4 srd, unsigned voff, unsigned soff, unsigned lds_addr) {
    unsigned keep;
    asm volatile("s_mov_b32 %0, m0\n\ts_mov_b32 m0, %4\n\ts_nop 0\n\tbuffer_load_dwordx4 %1, %2, %3 offen sc1 lds\n\ts_mov_b32 m0, %0"
                 : "=&s"(keep) : "v"(voff), "s"(srd), "s"(soff), "s"(lds_addr) : "memory");
}
DI void dma4(i32x4 srd, unsigned voff, unsigned soff, unsigned lds_addr) {
    unsigned keep;
    asm volatile("s_mov_b32 %0, m0\n\ts_mov_b32 m0, %4\n\ts_nop 0\n\tbuffer_load_dword %1, %2, %3 offen lds\n\ts_mov_b32 m0, %0"
                 : "=&s"(keep) : "v"(voff), "s"(srd), "s"(soff), "s"(lds_addr) : "memory");
}
#define WAIT_BAR(N) asm volatile("s_waitcnt vmcnt(" #N ") lgkmcnt(0)\n\ts_barrier" ::: "memory")
#define LBAR() asm volatile("s_waitcnt lgkmcnt(0)\n\ts_barrier" ::: "memory")
#define SB() __builtin_amdgcn_sched_barrier(0)
#define FENCE() asm volatile("" ::: "memory")
#define PIN4(a) asm volatile("" : "+v"((a)[0]), "+v"((a)[1]), "+v"((a)[2]), "+v"((a)[3]))
#define MFMA16(a, b, c) __builtin_amdgcn_mfma_f32_16x16x32_bf16(a, b, c, 0, 0, 0)
#define MFMA32(a, b, c) __builtin_amdgcn_mfma_f32_32x32x16_bf16(a, b, c, 0, 0, 0)
#define MFMA32K8(a, b, c) __builtin_amdgcn_mfma_f32_32x32x8bf16_1k(a, b, c, 0, 0, 0)
#define EXP2(x) __builtin_amdgcn_exp2f(x)
DI s16x4 lds_tr(LAS const char* p) { return __builtin_bit_cast(s16x4, __builtin_amdgcn_ds_read_tr16_b64_v4i16((LAS v4i16_t*)p)); }
DI bf16x8 lds_b128(LAS const char* p) { return *(LAS const bf16x8*)p; }
DI bf16x8 lds_afrag(LAS const char* p) { const s16x4 lo = lds_tr(p), hi = lds_tr(p + 2048); return __builtin_shufflevector(lo, hi, 0, 1, 2, 3, 4, 5, 6, 7); }
#define RLX_AGENT __ATOMIC_RELAXED, __HIP_MEMORY_SCOPE_AGENT
template <int X> DI float xor_lane(float v) { return __int_as_float(__builtin_amdgcn_ds_swizzle(__float_as_int(v), (X << 10) | 0x1f)); }
DI float half_sum(float v) { auto r = __builtin_amdgcn_permlane32_swap(__float_as_uint(v), __float_as_uint(v), false, false); return __uint_as_float(r[0]) + __uint_as_float(r[1]); }
DI float half_max(float v) { auto r = __builtin_amdgcn_permlane32_swap(__float_as_uint(v), __float_as_uint(v), false, false); return fmaxf(__uint_as_float(r[0]), __uint_as_float(r[1])); }
DI float half_lo(float v) { auto r = __builtin_amdgcn_permlane32_swap(__float_as_uint(v), __float_as_uint(v), false, false); return __uint_as_float(r[0]); }
template <int CTRL, int ROWS> DI float dpp_add(float v) { return v + __int_as_float(__builtin_amdgcn_update_dpp(0, __float_as_int(v), CTRL, ROWS, 0xf, true)); }
DI float wave_sum_dpp(float v) {
    v = dpp_add<0x111, 0xf>(v); v = dpp_add<0x112, 0xf>(v); v = dpp_add<0x114, 0xf>(v); v = dpp_add<0x118, 0xf>(v);
    v = dpp_add<0x142, 0xa>(v); v = dpp_add<0x143, 0xc>(v);
    return __int_as_float(__builtin_amdgcn_readlane(__float_as_int(v), 63));
}
DI float wave_sum(float v) { v += xor_lane<1>(v); v += xor_lane<2>(v); v += xor_lane<4>(v); v += xor_lane<8>(v); v += xor_lane<16>(v); return half_sum(v); }
DI float wave_max(float v) { v = fmaxf(v, xor_lane<1>(v)); v = fmaxf(v, xor_lane<2>(v)); v = fmaxf(v, xor_lane<4>(v)); v = fmaxf(v, xor_lane<8>(v)); v = fmaxf(v, xor_lane<16>(v)); return half_max(v); }
DI int fresh_lane() { int t; asm volatile("v_mbcnt_lo_u32_b32 %0, -1, 0\n\tv_mbcnt_hi_u32_b32 %0, -1, %0" : "=v"(t)); return t; }
DI void st_agent64(void* p, unsigned lo, unsigned hi) { __hip_atomic_store((GAS unsigned long long*)p, ((unsigned long long)hi << 32) | lo, RLX_AGENT); }
DI void wait_tags(const char* flags, unsigned off, bool use, unsigned tag) {
    for (unsigned spins = 0;; ++spins) {
        const unsigned v = use ? __hip_atomic_load((GAS unsigned*)(flags + off), RLX_AGENT) : tag;
        if (__all(v == tag)) break;
        if (spins > (1u << 19)) { __hip_atomic_store((GAS unsigned*)(flags + FL_TMO), tag, RLX_AGENT); break; }
        __builtin_amdgcn_s_sleep(1);
    }
    FENCE();
}
#define STAMP_DECL
#define STAMP(K, i)
#define STAMP_END(K)

#define DMA345 dma16_sc1
#define OSLEEP 8
constexpr int ST1 = 49152;
constexpr int LDS_D0 = 0, LDS_D1 = 49152, LDS_H = 86016, LDS_Q1 = 36864, LDS_Q2 = 45056;
constexpr int LDS_PF = 147456;
constexpr int LDS_KM = 46080;
constexpr int LDS_D2 = 126976;
constexpr int LDS_KV0 = 125952;
constexpr int LDS_OT = 113664;
constexpr int LDS_MX = LDS_OT;
constexpr int LDS_ZERO = LDS_OT + 10240;
constexpr int LDS_CTL = LDS_ZERO + 640;
constexpr int LDS_RED = 2 * KVCH + 9 * 1024;
constexpr int LDS_BYTES = 163840;
DI unsigned wo_lds(int p) { return p < 37 ? LDS_KV0 + p * 1024 : p < 74 ? (p - 37) * 1024 : p < 111 ? KVCH + (p - 74) * 1024 : 2 * KVCH + (p - 111) * 1024; }

template <int NM, bool FIRST, int ND, int DPG, int AD = 3, bool PIN = false, class Dma>
DI void gemm_step(f32x4 (&acc)[10][4], bf16x8 (&ca)[2], bf16x8 (&cb)[4], bf16x8 (&nb)[4], LAS const char* ap, LAS const char* bp, const Dma& dma) {
    bf16x8 a[10];
#pragma unroll
    for (int n = 0; n < 4; ++n) nb[n] = lds_b128(bp + n * 1024);
    a[0] = lds_afrag(ap); a[1] = lds_afrag(ap + 4096); if (AD > 2) a[2] = lds_afrag(ap + 2 * 4096);
    SB();
    int d = 0;
    if (!FIRST) {
#pragma unroll
        for (int k = 0; k < DPG; ++k) if (d < ND) dma(d++);
#pragma unroll
        for (int n = 0; n < 4; ++n) acc[NM - 2][n] = MFMA16(ca[0], cb[n], acc[NM - 2][n]);
        if (PIN) PIN4(acc[NM - 2]); SB();
#pragma unroll
        for (int k = 0; k < DPG; ++k) if (d < ND) dma(d++);
#pragma unroll
        for (int n = 0; n < 4; ++n) acc[NM - 1][n] = MFMA16(ca[1], cb[n], acc[NM - 1][n]);
        if (PIN) PIN4(acc[NM - 1]); SB();
    }
#pragma unroll
    for (int m = 0; m < NM - 2; ++m) {
        if (m + AD < NM) a[m + AD] = lds_afrag(ap + (m + AD) * 4096);
#pragma unroll
        for (int k = 0; k < DPG; ++k) if (d < ND) dma(d++);
#pragma unroll
        for (int n = 0; n < 4; ++n) acc[m][n] = MFMA16(a[m], nb[n], acc[m][n]);
        if (PIN) PIN4(acc[m]); SB();
    }
    ca[0] = a[NM - 2]; ca[1] = a[NM - 1];
}
template <int NM>
DI void gemm_flush(f32x4 (&acc)[10][4], const bf16x8 (&ca)[2], const bf16x8 (&cb)[4]) {
#pragma unroll
    for (int n = 0; n < 4; ++n) acc[NM - 2][n] = MFMA16(ca[0], cb[n], acc[NM - 2][n]);
#pragma unroll
    for (int n = 0; n < 4; ++n) acc[NM - 1][n] = MFMA16(ca[1], cb[n], acc[NM - 1][n]);
}

__global__ __launch_bounds__(512, 2) void fused_block(const u16* __restrict__ x, const u16* __restrict__ wq, const u16* __restrict__ wk, const u16* __restrict__ wv,
                                                       const u16* __restrict__ wo, const u16* __restrict__ wg, const u16* __restrict__ wu, const u16* __restrict__ wd,
                                                       char* __restrict__ ws, u16* __restrict__ out) {
    __shared__ __attribute__((aligned(1024))) char lds[LDS_BYTES];
    const int tid = threadIdx.x, l = tid & 63;
    const int w = __builtin_amdgcn_readfirstlane(tid >> 6);
    const int b = blockIdx.x, s = 2 * (b & 7) + ((b >> 3) & 1), r = b >> 4, wgi = r * 16 + s;
    const int tg = w >> 2, fg = w & 3;
    constexpr unsigned lds0 = 0;
    LAS char* L = (LAS char*)lds;
    char* flags = ws + WS_FLAGS;
    const unsigned tag = (((unsigned)amd_dispatch_id() + 1u) * 0x9E3779B1u) ^ ((unsigned)((unsigned long long)__builtin_amdgcn_dispatch_ptr() >> 6) * 0x85EBCA77u) ^ 0x5bd1e995u;

    const int dmat = w & 1, dR = (w >> 1) & 1, dcg0 = 5 * (w >> 2);
    const int di = l >> 2, dsig = l & 3;
    const int dmi = dsig ^ (2 * dmat + ((di >> 2) & 1)), dkrow = 8 * (di >> 2) + (di & 3);
    const i32x4 srd_w = make_srd(dmat ? wu : wg, DM * FF * 2);
    const i32x4 srd_q = make_srd(ws + WS_WQX, DM * 1024 * 2);
    const i32x4 srd_x = make_srd(ws + WS_XN, SEQ * DM * 2);
    const unsigned vo_w = dkrow * (FF * 2) + 16 * dmi, vo_q = dkrow * 2048 + 16 * dmi, vo_x = l * 16;
    const unsigned dlds = lds0 + dR * 2048 + dmat * 1024;
    auto pieceW = [&](int j, unsigned slot, int cg) { dma16(srd_w, vo_w, (32 * j + 4 * dR) * (FF * 2) + 288 * s * 2 + cg * 64, dlds + slot + cg * 4096); };
    auto pieceQ = [&](int j, unsigned slot) { dma16_sc1(srd_q, vo_q, (32 * j + 4 * dR) * 2048 + (64 * s + 32 * dmat) * 2, dlds + slot + 9 * 4096); };
    auto pieceX = [&](int j, unsigned slot, int n) { dma16_sc1(srd_x, vo_x, ((r * 18 + j) * 8 + n) * 1024, lds0 + slot + 40960 + n * 1024); };
    auto piece1k = [&](int j, unsigned slot, int i, int kind) {
        if (i < 3) { if (kind & 1) pieceW(j, slot, dcg0 + i); }
        else if (i == 3) { if (w == 3) { if (kind & 2) pieceQ(j, slot); } else if (kind & 1) pieceW(j, slot, dcg0 + 3); }
        else if (i == 4) {
            if (w < 3) { if (kind & 1) pieceW(j, slot, 4); } else if (w == 3) { if (kind & 2) pieceX(j, slot, 7); }
            else if (w < 7) { if (kind & 2) pieceQ(j, slot); } else if (kind & 1) pieceW(j, slot, 3);
        } else { if (w == 7) { if (kind & 1) pieceW(j, slot, 4); } else if (kind & 2) pieceX(j, slot, w); }
    };
    auto piece2 = [&](int j, unsigned slot, int i) {
        const int ii = (w >= 4 && i == 4) ? 3 : i;
        const unsigned vo_d = ((vo_w & ~63u) >> 3) + ((vo_w & 48u) << 1);
        dma16(make_srd(wd, FF * DM * 2), vo_d, (288 * s + 32 * j + 4 * dR) * (DM * 2) + 16 * dmat + (dcg0 + ii) * 128, dlds + slot + (dcg0 + ii) * 4096);
    };

    STAMP_DECL;
    STAMP(1, 0);
    const u32x4* xs = (const u32x4*)(x + (size_t)(128 * r + 8 * s + w) * DM);
    const u32x4 v0 = xs[l];
    u32x4 v1 = xs[64 + (l & 7)];
    FENCE();
    if (w == 7) {
        unsigned long long pc; unsigned len;
        asm volatile("s_getpc_b64 %0\n\tcode_here_%=:\n\ts_mov_b32 %1, fused_block_code_end-code_here_%=" : "=s"(pc), "=s"(len));
        i32x4 sc; sc.x = (int)(unsigned)pc; sc.y = (int)((unsigned)(pc >> 32) & 0xffffu); sc.z = (int)len; sc.w = 0x00020000;
        dma4(sc, l < 20 ? (unsigned)(((b >> 3) * 20 + l) * 128) : 0x80000000u, 0, lds0 + LDS_PF);
    }
    {
        unsigned qv[8];
        {
            const int tq = min(tid, 287), k = 36 * r + (tq >> 3), mat = (tq >> 2) & 1, t = tq & 3;
            const u16* wc = ((s & 1) ? wv : wk) + k * DH;
            const u16* wh = wq + (size_t)k * DM + (2 * s + (t >> 1)) * DH;
#pragma unroll
            for (int e = 0; e < 8; ++e) {
                const int rho = mat * 8 + e, cc = (t == 1 ? 0 : 12) + rho - 4;
                const bool isq = (t & 1) == 0 || rho < 2, isc = !isq && rho >= 4 && cc < (t == 1 ? 12 : 18);
                const u16* p = isc ? wc + cc : wh + ((t & 1) == 0 ? rho : 16 + (rho & 1));
                const unsigned val = *p;
                qv[e] = (isq || isc) ? val : 0u;
            }
        }
        const int cgp = s & 7, wp = 4 * (r * 2 + (s >> 3)) + ((min(max(tid, 192), 447) - 192) >> 6);
        const bool wo_act = tid >= 192 && tid < 448 && wp < 120;
        u32x4 wov;
        {
            const int m0 = cgp < 4 ? 5 * cgp : 20 + 4 * (cgp - 4), wpc = min(wp, 119);
            const int pp = wpc / 40, js = (wpc % 40) >> 1, R = wpc & 1, i = l >> 2, ch = (l & 3) ^ (2 * ((i >> 2) & 1));
            const int kp = 32 * js + 8 * (i >> 2) + (i & 3) + 4 * R, sp = kp / 40, d = kp - 40 * sp, hd = d >= 20 ? 1 : 0, dd = d - 20 * hd;
            const int row = 36 * sp + 18 * hd + (dd < 18 ? dd : 17), col = min(16 * (m0 + 2 * pp) + 8 * ch, DM - 8);
            wov = *(const u32x4*)(wo + (size_t)row * DM + col);
        }
        FENCE();
        STAMP(1, 13);
        if (l >= 8) v1 = (u32x4){0u, 0u, 0u, 0u};
        float sm = 0.f;
#pragma unroll
        for (int e = 0; e < 4; ++e) sm += bflo(v0[e]) + bfhi(v0[e]) + bflo(v1[e]) + bfhi(v1[e]);
        const float mean = wave_sum_dpp(sm) * (1.0f / DM);
        float q = 0.f, q1 = 0.f;
#pragma unroll
        for (int e = 0; e < 4; ++e) { const float a = bflo(v0[e]) - mean, c = bfhi(v0[e]) - mean; q += a * a + c * c; }
#pragma unroll
        for (int e = 0; e < 4; ++e) { const float a = bflo(v1[e]) - mean, c = bfhi(v1[e]) - mean; q1 += a * a + c * c; }
        if (l < 8) q += q1;
        const float m = rbf(mean), var = rbf(wave_sum_dpp(q) * (1.0f / DM));
        const float rs = 1.0f / rbf(sqrtf(rbf(var + 1e-5f)));
        LAS char* xst = L + 2 * ST1;
        {
            u32x4 o;
#pragma unroll
            for (int e = 0; e < 4; ++e) o[e] = cvtpk(rbf(bflo(v0[e]) - m) * rs, rbf(bfhi(v0[e]) - m) * rs);
            *(LAS u32x4*)(xst + l * 128 + w * 16) = o;
#pragma unroll
            for (int e = 0; e < 4; ++e) o[e] = cvtpk(rbf(bflo(v1[e]) - m) * rs, rbf(bfhi(v1[e]) - m) * rs);
            if (l < 8) *(LAS u32x4*)(xst + (64 + l) * 128 + w * 16) = o;
        }
        asm volatile("" : "+v"(wov));
        LBAR();
        STAMP(1, 14);
        if (w < 7) {
            const __amdgpu_buffer_rsrc_t rx = __builtin_amdgcn_make_buffer_rsrc(ws + WS_XN, 0, SEQ * DM * 2, 0x00020000);
#pragma unroll
            for (int u = 0; u < 2; ++u) {
                const int i = tid + 448 * u, c = i >> 3, t = i & 7;
                const u32x4 v = *(LAS const u32x4*)(xst + min(i, 575) * 16);
                __builtin_amdgcn_raw_buffer_store_b128(v, rx, i < 576 ? (unsigned)(((r * 18 + (c >> 2)) * 8 + (s >> 1)) * 1024 + ((c & 3) * 16 + 8 * (s & 1) + t) * 16) : 0xfffffff0u, 0, 16);
            }
            {
                u32x4 o; o[0] = qv[0] | (qv[1] << 16); o[1] = qv[2] | (qv[3] << 16); o[2] = qv[4] | (qv[5] << 16); o[3] = qv[6] | (qv[7] << 16);
                const __amdgpu_buffer_rsrc_t rq = __builtin_amdgcn_make_buffer_rsrc(ws + WS_WQX, 0, DM * 1024 * 2, 0x00020000);
                __builtin_amdgcn_raw_buffer_store_b128(o, rq, tid < 288 ? (unsigned)(((36 * r + (tid >> 3)) * 128 + s * 8 + (tid & 7)) * 16) : 0xfffffff0u, 0, 16);
            }
            WAIT_BAR(0);
        } else LBAR();
        STAMP(1, 15);
        if (tid == 0) __hip_atomic_store((GAS unsigned*)(flags + FL_PRO + wgi * 4), tag, RLX_AGENT);
#pragma unroll
        for (int i = 0; i < 6; ++i) piece1k(0, 0, i, 1);
#pragma unroll
        for (int i = 0; i < 6; ++i) piece1k(1, ST1, i, 1);
        if (w == 7) {
            const unsigned o = (l & 7) < 5 ? (unsigned)(((l >> 3) * 16 + r) * (FF * 2) + ((288 * s * 2) & ~127) + (l & 7) * 128) : 0x80000000u;
#pragma unroll
            for (int g = 0; g < 2; ++g) { dma4(make_srd(wg, DM * FF * 2), o, g * 128 * (FF * 2), lds0 + LDS_PF); dma4(srd_w, o, g * 128 * (FF * 2), lds0 + LDS_PF); }
        }
        if (w < 7) {
            const __amdgpu_buffer_rsrc_t rw = __builtin_amdgcn_make_buffer_rsrc(ws + WS_WO, 0, 8 * WOIMG, 0x00020000);
            __builtin_amdgcn_raw_buffer_store_b128(wov, rw, wo_act ? (unsigned)(cgp * WOIMG + wp * 1024 + l * 16) : 0xfffffff0u, 0, 16);
            const __amdgpu_buffer_rsrc_t ro = __builtin_amdgcn_make_buffer_rsrc(ws + WS_O + (size_t)wgi * 128 * 80, 0, 128 * 80, 0x00020000);
            const u32x4 e = (u32x4){O_EMPTY, O_EMPTY, O_EMPTY, O_EMPTY};
            __builtin_amdgcn_raw_buffer_store_b128(e, ro, tid * 16, 0, 16);
            __builtin_amdgcn_raw_buffer_store_b128(e, ro, (tid + 448) * 16, 0, 16);
        }
        STAMP(1, 12);
        if (w == 0) wait_tags(flags, FL_PRO + (l < 16 ? (r * 16 + l) * 4 : ((l - 16) * 16 + s) * 4), l < 32, tag);
        asm volatile("s_barrier" ::: "memory");
#pragma unroll
        for (int i = 3; i < 6; ++i) piece1k(0, 0, i, 2);
#pragma unroll
        for (int i = 3; i < 6; ++i) piece1k(1, ST1, i, 2);
    }
    const int lg = fresh_lane();
    const unsigned a_off = ((lg & 3) >> 1) * 1024 + 16 * (4 * (4 * (lg >> 4) + ((lg >> 2) & 3)) + (fg ^ (2 * ((lg & 3) >> 1) + ((lg >> 4) & 1)))) + 8 * (lg & 1);

    f32x4 acc[10][4];
#pragma unroll
    for (int m = 0; m < 10; ++m)
#pragma unroll
        for (int n = 0; n < 4; ++n) acc[m][n] = (f32x4){0.f, 0.f, 0.f, 0.f};
    bf16x8 ca[2], bA[4], bB[4];
    {
        const unsigned bo = 40960 + tg * 4096 + lg * 16;
        WAIT_BAR(0);
        STAMP(1, 1);
        const bool q3 = w == 3, w4 = w < 3 || w == 7, x4 = w == 3, w5 = w == 7;
        const i32x4 srd3 = q3 ? srd_q : srd_w, srd4 = w4 ? srd_w : x4 ? srd_x : srd_q, srd5 = w5 ? srd_w : srd_x;
        const unsigned vo3 = q3 ? vo_q : vo_w, vo4 = w4 ? vo_w : x4 ? vo_x : vo_q, vo5 = w5 ? vo_w : vo_x;
        const unsigned soW = 4 * dR * (FF * 2) + 288 * s * 2, soQ = 4 * dR * 2048 + (64 * s + 32 * dmat) * 2, soX = r * 18 * 8192;
        const unsigned stW = 32 * FF * 2, stQ = 32 * 2048, stX = 8192;
        const unsigned so3 = q3 ? soQ : soW + (dcg0 + 3) * 64, st3 = q3 ? stQ : stW, lo3 = q3 ? dlds + 9 * 4096 : dlds + (dcg0 + 3) * 4096;
        const int cg4 = w < 3 ? 4 : 3;
        const unsigned so4 = w4 ? soW + cg4 * 64 : x4 ? soX + 7 * 1024 : soQ, st4 = w4 ? stW : x4 ? stX : stQ, lo4 = w4 ? dlds + cg4 * 4096 : x4 ? lds0 + 40960 + 7 * 1024 : dlds + 9 * 4096;
        const unsigned so5 = w5 ? soW + 4 * 64 : soX + w * 1024, st5 = w5 ? stW : stX, lo5 = w5 ? dlds + 4 * 4096 : lds0 + 40960 + w * 1024;
        unsigned vo_pf;
        { const int lp = fresh_lane(); vo_pf = (lp & 7) < 5 ? (unsigned)(((lp >> 3) * 16 + r) * (FF * 2) + ((288 * s * 2) & ~127) + (lp & 7) * 128) : 0x80000000u; }
        auto stage1 = [&](int j, unsigned slot, int i) {
            if (i < 3) dma16(srd_w, vo_w, soW + (dcg0 + i) * 64 + j * stW, dlds + (dcg0 + i) * 4096 + slot);
            else if (i == 3) DMA345(srd3, vo3, so3 + j * st3, lo3 + slot);
            else if (i == 4) DMA345(srd4, vo4, so4 + j * st4, lo4 + slot);
            else if (i == 5) DMA345(srd5, vo5, so5 + j * st5, lo5 + slot);
            else if (w >= 6) {
                const int t = j - 2;
                if (t == 6) { const int lp = fresh_lane(); vo_pf = (w == 7 && (lp & 15) < 9) ? (unsigned)((288 * s + (lp >> 4) * 16 + r) * (DM * 2) + (lp & 15) * 128) : 0x80000000u; }
                const unsigned so = t < 6 ? ((t & 1) ? 0x70000000u : (unsigned)(2 + (t >> 1)) * 128 * (FF * 2)) : t < 11 ? (unsigned)(t - 6) * 64 * (DM * 2) : 0x70000000u;
                dma4(t < 6 ? srd_w : make_srd(wd, FF * DM * 2), vo_pf, so, lds0 + LDS_PF);
            }
        };
        gemm_step<10, true, 7, 1>(acc, ca, bB, bA, L + a_off, L + bo, [&](int i) { stage1(2, 2 * ST1, i); });
        unsigned slot = ST1, slot2 = 0;
        if (w >= 6) { WAIT_BAR(7); } else { WAIT_BAR(6); }
        gemm_step<10, false, 7, 1>(acc, ca, bA, bB, L + slot + a_off, L + slot + bo, [&](int i) { stage1(3, slot2, i); });
        slot = 2 * ST1; slot2 = ST1;
#pragma unroll 1
        for (int j = 2; j < 16; j += 2) {
            if (w >= 6) { WAIT_BAR(8); } else { WAIT_BAR(6); }
            gemm_step<10, false, 7, 1>(acc, ca, bB, bA, L + slot + a_off, L + slot + bo, [&](int i) { stage1(j + 2, slot2, i); });
            slot = (slot == 2 * ST1) ? 0 : slot + ST1; slot2 = (slot2 == 2 * ST1) ? 0 : slot2 + ST1;
            if (w >= 6) { WAIT_BAR(8); } else { WAIT_BAR(6); }
            gemm_step<10, false, 7, 1>(acc, ca, bA, bB, L + slot + a_off, L + slot + bo, [&](int i) { stage1(j + 3, slot2, i); });
            slot = (slot == 2 * ST1) ? 0 : slot + ST1; slot2 = (slot2 == 2 * ST1) ? 0 : slot2 + ST1;
        }
#pragma unroll
        for (int n = 0; n < 4; ++n) bA[n] = bB[n];
#pragma unroll 1
        for (int j = 16; j < 18; ++j) {
            if (j == 16 && w >= 6) { WAIT_BAR(8); } else if (j == 16) { WAIT_BAR(6); } else { WAIT_BAR(5); }
            gemm_step<10, false, 5, 1, 3, true>(acc, ca, bA, bB, L + slot + a_off, L + slot + bo, [&](int i) { piece2(j - 16, j == 16 ? LDS_D0 : LDS_D1, i); });
#pragma unroll
            for (int n = 0; n < 4; ++n) bA[n] = bB[n];
            slot += ST1;
        }
    }
    WAIT_BAR(0);
    STAMP(1, 2);
    gemm_flush<10>(acc, ca, bA);
    auto h_tiles = [&](auto CG0, auto CG1) {
        const int l = fresh_lane(), tok = l & 15, g4 = l >> 4;
#pragma unroll
        for (int cg = CG0; cg < CG1; ++cg)
#pragma unroll
            for (int pr = 0; pr < 2; ++pr) {
                float h[4];
#pragma unroll
                for (int e = 0; e < 4; ++e) {
                    auto sw = __builtin_amdgcn_permlane32_swap(__float_as_uint(acc[cg][2 * pr][e]), __float_as_uint(acc[cg][2 * pr + 1][e]), false, false);
                    h[e] = __int_as_float(max((int)sw[0], 0)) * __uint_as_float(sw[1]);
                }
                const int n = 4 * tg + 2 * pr + (l >> 5);
                u32x2 o; o[0] = cvtpk(h[0], h[1]); o[1] = cvtpk(h[2], h[3]);
                *(LAS u32x2*)(L + LDS_H + ((8 - cg) * 8 + n) * 1024 + (fg * 16 + tok) * 16 + 8 * (g4 & 1)) = o;
            }
    };
    float kn2 = 0.f;
    if ((fg & 1) && tg == (s >> 3)) {
        const int l = fresh_lane(), tok = l & 15, g4 = l >> 4;
        const int e = s >> 1;
        f32x4 cv = acc[9][0];
        if ((e & 3) == 1) cv = acc[9][1]; else if ((e & 3) == 2) cv = acc[9][2]; else if ((e & 3) == 3) cv = acc[9][3];
        const int c0 = (fg == 1 ? 0 : 12) + 4 * (g4 - 1);
        const bool act = g4 >= 1 && c0 < 18;
        const unsigned p0 = cvtpk(cv[0], cv[1]), p1 = cvtpk(cv[2], cv[3]);
        const int kt = 4 * r + (e >> 1);
        char* kvb = ws + WS_KV + ((kt >> 3) & 3) * KVCH; const int lt = 8 * (kt >> 5) + (kt & 7);
        if ((s & 1) == 0) {
            const int key = 16 * (e & 1) + tok;
            if (act) {
                if (c0 < 16) st_agent64(kvb + lt * 1024 + ((c0 >> 3) * 32 + key) * 16 + (c0 & 4) * 2, p0, p1);
                else __hip_atomic_store((GAS unsigned*)(kvb + KV_K2 + lt * 128 + key * 4), p0, RLX_AGENT);
            }
            float n2 = 0.f;
            if (act) { n2 = bflo(p0) * bflo(p0) + bfhi(p0) * bfhi(p0); if (c0 < 16) n2 += bflo(p1) * bflo(p1) + bfhi(p1) * bfhi(p1); }
            kn2 = n2;
        } else {
            if (fg == 1 && l < 2) {
                char* op = kvb + KV_V + lt * 1216 + (e & 1) * 608 + l * 304 + 18 * 16;
                st_agent64(op, 0x3f803f80u, 0x3f803f80u); st_agent64(op + 8, 0x3f803f80u, 0x3f803f80u);
            }
            const int hv = (tok >> 2) & 1, jj = 4 * (tok >> 3) + (tok & 3);
            GAS u16* vb = (GAS u16*)(kvb + KV_V + lt * 1216 + (e & 1) * 608 + hv * 304 + jj * 2);
            if (act) {
                __hip_atomic_store(vb + (c0 + 0) * 8, (u16)(p0 & 0xffff), RLX_AGENT);
                __hip_atomic_store(vb + (c0 + 1) * 8, (u16)(p0 >> 16), RLX_AGENT);
                if (c0 < 16) { __hip_atomic_store(vb + (c0 + 2) * 8, (u16)(p1 & 0xffff), RLX_AGENT);
                               __hip_atomic_store(vb + (c0 + 3) * 8, (u16)(p1 >> 16), RLX_AGENT); }
            }
        }
    }
    h_tiles(std::integral_constant<int, 0>{}, std::integral_constant<int, 9>{});
    {
        const int l = fresh_lane(), tok = l & 15, g4 = l >> 4;
        const float qs = 0.23570226039551584f * 1.4426950408889634f;
        const int hh = fg >> 1;
        unsigned p0[4], p1[4];
#pragma unroll
        for (int n = 0; n < 4; ++n) { p0[n] = cvtpk(acc[9][n][0] * qs, acc[9][n][1] * qs); p1[n] = cvtpk(acc[9][n][2] * qs, acc[9][n][3] * qs); }
        if ((fg & 1) == 0) {
            LAS char* qd = L + LDS_Q1 + (hh * 4 + 2 * tg) * 1024 + ((g4 >> 1) * 32 + tok) * 16 + (g4 & 1) * 8;
#pragma unroll
            for (int n = 0; n < 4; ++n) *(LAS u32x2*)(qd + (n >> 1) * 1024 + (n & 1) * 256) = (u32x2){p0[n], p1[n]};
        } else if (g4 == 0) {
            LAS char* qd = L + LDS_Q2 + (hh * 4 + 2 * tg) * 128 + tok * 4;
#pragma unroll
            for (int n = 0; n < 4; ++n) *(LAS unsigned*)(qd + (n >> 1) * 128 + (n & 1) * 64) = p0[n];
        }
    }
    if ((fg & 1) && tg == (s >> 3)) {
        kn2 += xor_lane<16>(kn2); kn2 = wave_max(half_sum(kn2));
        if (fresh_lane() == 0) *(LAS float*)(L + LDS_KM + (fg == 1 ? 0 : 4)) = kn2;
    }
    LBAR();
    STAMP(1, 3);
    const i32x4 srd_kv = make_srd(ws + WS_KV, 4 * KVCH);
    auto piece_kv = [&](int c, int i) {
        const int p = min(w + 8 * i, 36);
        dma16_sc1(srd_kv, vo_x, c * KVCH + p * 1024, lds0 + (c == 0 ? LDS_KV0 : (c - 1) * KVCH) + p * 1024);
    };
#pragma unroll
    for (int m = 0; m < 9; ++m)
#pragma unroll
        for (int n = 0; n < 4; ++n) acc[m][n] = (f32x4){0.f, 0.f, 0.f, 0.f};
    {
        LAS const char* hb = L + LDS_H + 8 * 8192 + 4 * tg * 1024 + l * 16;
        gemm_step<9, true, 0, 1>(acc, ca, bB, bA, L + LDS_D0 + a_off, hb, [](int) {});
        WAIT_BAR(0);
        if (w == 0 && fresh_lane() == 0) {
            st_agent64(flags + FL_KV + wgi * 16, tag, *(LAS const unsigned*)(L + LDS_KM));
            st_agent64(flags + FL_KV + wgi * 16 + 8, tag, *(LAS const unsigned*)(L + LDS_KM + 4));
        }
        gemm_step<9, false, 5, 2>(acc, ca, bA, bB, L + LDS_D1 + a_off, hb - 1 * 8192, [&](int i) { piece2(2, LDS_D0, i); });
        WAIT_BAR(0);
        gemm_step<9, false, 5, 2>(acc, ca, bB, bA, L + LDS_D0 + a_off, hb - 2 * 8192, [&](int i) { piece2(3, LDS_D1, i); });
        WAIT_BAR(0);
        gemm_step<9, false, 5, 2>(acc, ca, bA, bB, L + LDS_D1 + a_off, hb - 3 * 8192, [&](int i) { piece2(4, LDS_D0, i); });
        WAIT_BAR(0);
        gemm_step<9, false, 10, 2>(acc, ca, bB, bA, L + LDS_D0 + a_off, hb - 4 * 8192, [&](int i) { if (i < 5) piece2(5, LDS_D1, i); else piece2(6, LDS_D2, i - 5); });
        WAIT_BAR(5);
        gemm_step<9, false, 5, 1>(acc, ca, bA, bB, L + LDS_D1 + a_off, hb - 5 * 8192, [&](int i) { piece2(7, LDS_D0, i); });
        WAIT_BAR(5);
        gemm_step<9, false, 5, 1>(acc, ca, bB, bA, L + LDS_D2 + a_off, hb - 6 * 8192, [&](int i) { piece2(8, LDS_D1, i); });
        if (w == 0) {
            const int lk = fresh_lane();
            const __amdgpu_buffer_rsrc_t rk = __builtin_amdgcn_make_buffer_rsrc(flags + FL_KV, 0, 4096, 0x00020000);
            float ma, mb;
            for (unsigned spins = 0;; ++spins) {
                bool ok = true; ma = 0.f; mb = 0.f;
#pragma unroll
                for (int i = 0; i < 4; ++i) {
                    const u32x4 v = __builtin_amdgcn_raw_buffer_load_b128(rk, (i * 64 + lk) * 16, 0, 16);
                    ok = ok && v[0] == tag && v[2] == tag; ma = fmaxf(ma, __uint_as_float(v[1])); mb = fmaxf(mb, __uint_as_float(v[3]));
                }
                if (__all(ok)) break;
                if (spins > (1u << 19)) { __hip_atomic_store((GAS unsigned*)(flags + FL_TMO), tag, RLX_AGENT); break; }
                __builtin_amdgcn_s_sleep(1);
            }
            ma = wave_max(ma); mb = wave_max(mb);
            if (lk == 0) *(LAS float*)(L + LDS_KM + 8) = ma + mb;
        }
        WAIT_BAR(5);
        gemm_step<9, false, 5, 1>(acc, ca, bA, bB, L + LDS_D0 + a_off, hb - 7 * 8192, [&](int i) { piece_kv(0, i); });
        WAIT_BAR(5);
        gemm_step<9, false, 0, 1>(acc, ca, bB, bA, L + LDS_D1 + a_off, hb - 8 * 8192, [](int) {});
        gemm_flush<9>(acc, ca, bA);
        SB(); FENCE();
    }
    STAMP(1, 4);
    const int la = fresh_lane();
#define l la
    const int hh = w >> 2, kh = (w >> 1) & 1, cp = w & 1;
#define h5 (l >> 5)
    bf16x8 q1[2]; u32x2 q2[2];
#pragma unroll
    for (int c = 0; c < 2; ++c) {
        q1[c] = lds_b128(L + LDS_Q1 + (hh * 4 + 2 * cp + c) * 1024 + l * 16);
        q2[c][0] = *(LAS const unsigned*)(L + LDS_Q2 + (hh * 4 + 2 * cp + c) * 128 + (l & 31) * 4);
    }
    u32x4 pk[18];
#pragma unroll
    for (int m = 0; m < 9; ++m)
#pragma unroll
        for (int pr = 0; pr < 2; ++pr) {
            u32x4 o; o[0] = cvtpk(acc[m][2 * pr][0], acc[m][2 * pr][1]); o[1] = cvtpk(acc[m][2 * pr][2], acc[m][2 * pr][3]);
            o[2] = cvtpk(acc[m][2 * pr + 1][0], acc[m][2 * pr + 1][1]); o[3] = cvtpk(acc[m][2 * pr + 1][2], acc[m][2 * pr + 1][3]);
            pk[m * 2 + pr] = o;
        }
    {
        const float kmax2 = *(LAS const float*)(L + LDS_KM + 8);
#pragma unroll
        for (int c = 0; c < 2; ++c) {
            const u32x4 a = __builtin_bit_cast(u32x4, q1[c]); const unsigned t = h5 ? 0u : q2[c][0];
            float n2 = bflo(t) * bflo(t) + bfhi(t) * bfhi(t);
#pragma unroll
            for (int e = 0; e < 4; ++e) n2 += bflo(a[e]) * bflo(a[e]) + bfhi(a[e]) * bfhi(a[e]);
            n2 = half_sum(n2);
            const float cb = sqrtf(n2 * kmax2) * 1.001f + 0.01f;
            const float chi = rbf(cb), clo = cb - chi;
            q2[c][0] = t; q2[c][1] = h5 ? 0u : cvtpk(-chi, -clo);
        }
    }
    if (w == 0 && l < 40) *(LAS u32x4*)(L + LDS_ZERO + l * 16) = (u32x4){0u, 0u, 0u, 0u};
    if (w == 0 && l == 40) *(LAS unsigned*)(L + LDS_CTL) = 0u;
    WAIT_BAR(0);
    STAMP(1, 5);
#undef l
    const int lb = fresh_lane();
#define l lb
    const unsigned kone = h5 ? 0u : 0x3f803f80u;
    const bool vrow = (l & 31) < 19;
    const unsigned k1_base = 8 * kh * 1024 + l * 16, k2_base = KV_K2 + 8 * kh * 128 + (l & 31) * 4;
    const unsigned v_base = vrow ? KV_V + 8 * kh * 1216 + h5 * 304 + (l & 31) * 16 : LDS_ZERO, v_mul = vrow ? 1u : 0u;
    const int tgf = s >> 3, cgp = s & 7, nmt = cgp < 4 ? 5 : 4, m0 = cgp < 4 ? 5 * cgp : 20 + 4 * (cgp - 4);
    const int pr = w & 1, qk = w >> 1;
    const i32x4 srd_wo = make_srd(ws + WS_WO + (size_t)cgp * WOIMG, WOIMG);
    auto piece_wo = [&](int p) { dma16(srd_wo, vo_x, p * 1024, lds0 + wo_lds(p)); };
    const __amdgpu_buffer_rsrc_t rpart = __builtin_amdgcn_make_buffer_rsrc(ws + WS_PART + (size_t)r * 16 * 8 * 18 * 1024, 0, 16 * 8 * 18 * 1024, 0x00020000);
    float own[11];
    u32x4 pl[5][4];

    f32x16 o0, o1, sX, sY; bf16x8 kA, kB, v0A, v1A, v0B, v1B; unsigned k2A, k2B; u32x4 p0, p1; float e3[4];
    unsigned aK, aK2, aV;
#define SLOT(SC, SN, KN, K2N, CN, OP, V1P, OC, V0C) do { float ea[4], eb[4]; \
        SN = MFMA32(KN, q1[CN], (f32x16){}); SB(); \
        ea[0] = EXP2(SC[0]); ea[1] = EXP2(SC[1]); ea[2] = EXP2(SC[2]); ea[3] = EXP2(SC[3]); \
        p1[2] = cvtpk(e3[0], e3[1]); p1[3] = cvtpk(e3[2], e3[3]); SB(); \
        OP = MFMA32(V1P, __builtin_bit_cast(bf16x8, p1), OP); SB(); \
        eb[0] = EXP2(SC[4]); eb[1] = EXP2(SC[5]); eb[2] = EXP2(SC[6]); eb[3] = EXP2(SC[7]); \
        p0[0] = cvtpk(ea[0], ea[1]); p0[1] = cvtpk(ea[2], ea[3]); SB(); \
        SN = MFMA32K8(__builtin_bit_cast(s16x4, ((u32x2){K2N, kone})), __builtin_bit_cast(s16x4, q2[CN]), SN); SB(); \
        ea[0] = EXP2(SC[8]); ea[1] = EXP2(SC[9]); ea[2] = EXP2(SC[10]); ea[3] = EXP2(SC[11]); \
        p0[2] = cvtpk(eb[0], eb[1]); p0[3] = cvtpk(eb[2], eb[3]); SB(); \
        OC = MFMA32(V0C, __builtin_bit_cast(bf16x8, p0), OC); SB(); \
        e3[0] = EXP2(SC[12]); e3[1] = EXP2(SC[13]); e3[2] = EXP2(SC[14]); e3[3] = EXP2(SC[15]); \
        p1[0] = cvtpk(ea[0], ea[1]); p1[1] = cvtpk(ea[2], ea[3]); SB(); } while (0)
#define PAIR(DK, DK2, DV, H0, H1, H2, H3) do { \
        kB = lds_b128(L + aK + 1024); k2B = *(LAS const unsigned*)(L + aK2 + 128); v0B = lds_b128(L + aV + v_mul * 1216); SB(); \
        H0; SLOT(sX, sY, kA, k2A, 1, o1, v1B, o0, v0A); \
        v1B = lds_b128(L + aV + v_mul * 1216 + 608); SB(); \
        H1; SLOT(sY, sX, kB, k2B, 0, o0, v1A, o1, v0A); \
        aK += (DK); aK2 += (DK2); aV += v_mul * (DV); \
        kA = lds_b128(L + aK); k2A = *(LAS const unsigned*)(L + aK2); v0A = lds_b128(L + aV); SB(); \
        H2; SLOT(sX, sY, kB, k2B, 1, o1, v1A, o0, v0B); \
        v1A = lds_b128(L + aV + 608); SB(); \
        H3; SLOT(sY, sX, kA, k2A, 0, o0, v1B, o1, v0B); } while (0)
#define PAIR_IN(H0, H1, H2, H3) PAIR(2048, 256, 2432, H0, H1, H2, H3)
#define PAIR_JUMP(J, H0, H1, H2, H3) PAIR((J) - 6 * 1024, (J) - 6 * 128, (J) - 6 * 1216, H0, H1, H2, H3)
#define NOP_ ((void)0)
#define ATT_INIT() do { o0 = (f32x16){}; o1 = (f32x16){}; aK = k1_base + LDS_KV0; aK2 = k2_base + LDS_KV0; aV = v_base + v_mul * LDS_KV0; \
        kA = lds_b128(L + aK); k2A = *(LAS const unsigned*)(L + aK2); v0A = lds_b128(L + aV); v1A = lds_b128(L + aV + 608); v1B = v1A; \
        sX = MFMA32(kA, q1[0], (f32x16){}); sX = MFMA32K8(__builtin_bit_cast(s16x4, ((u32x2){k2A, kone})), __builtin_bit_cast(s16x4, q2[0]), sX); \
        p1 = (u32x4){0u, 0u, 0u, 0u}; e3[0] = e3[1] = e3[2] = e3[3] = 0.f; } while (0)
#define ATT_FIN() do { p1[2] = cvtpk(e3[0], e3[1]); p1[3] = cvtpk(e3[2], e3[3]); o1 = MFMA32(v1B, __builtin_bit_cast(bf16x8, p1), o1); } while (0)
    constexpr int J01 = 0 - LDS_KV0, J12 = KVCH;
    {
        const __amdgpu_buffer_rsrc_t rp = __builtin_amdgcn_make_buffer_rsrc(ws + WS_PART + (size_t)(r * 16 + s) * 8 * 18 * 1024, 0, 8 * 18 * 1024, 0x00020000);
#define KV(C, I) do { piece_kv(C, I); SB(); } while (0)
#define ST(I) do { __builtin_amdgcn_raw_buffer_store_b128(pk[I], rp, vo_x, (w * 18 + (I)) * 1024, 16); SB(); } while (0)
#define WO(P0, I) do { piece_wo(min((P0) + w + 8 * (I), (P0) + 36)); SB(); } while (0)
#define PL(T, SP) do { const int m_ = m0 + ((T) < nmt ? (T) : 0); \
            pl[T][SP] = __builtin_amdgcn_raw_buffer_load_b128(rpart, vo_x, ((((4 * qk + (SP)) * 8 + tgf * 4 + (m_ & 3)) * 9 + (m_ >> 2)) * 2 + pr) * 1024, 16); SB(); } while (0)
#define PL2(A) do { PL((A) >> 2, (A) & 3); PL(((A) + 1) >> 2, ((A) + 1) & 3); } while (0)
        ATT_INIT();
        PAIR_IN(KV(1, 0), KV(1, 1), KV(1, 2), KV(1, 3));
        PAIR_IN(KV(1, 4), ST(0), KV(2, 0), ST(1));
        PAIR_IN(KV(2, 1), ST(2), KV(2, 2), ST(3));
        WAIT_BAR(7);
        PAIR_JUMP(J01, KV(2, 3), ST(4), KV(2, 4), ST(5));
        PAIR_IN(KV(3, 0), ST(6), KV(3, 1), ST(7));
        PAIR_IN(KV(3, 2), ST(8), KV(3, 3), ST(9));
        PAIR_IN(KV(3, 4), ST(10), NOP_, ST(11));
        WAIT_BAR(12);
        PAIR_JUMP(J12, WO(0, 0), ST(12), WO(0, 1), ST(13));
        PAIR_IN(WO(0, 2), ST(14), WO(0, 3), ST(15));
        PAIR_IN(WO(0, 4), ST(16), NOP_, ST(17));
        PAIR_IN(NOP_, NOP_, NOP_, NOP_);
        WAIT_BAR(0);
        if (w == 0 && fresh_lane() == 0) __hip_atomic_store((GAS unsigned*)(flags + FL_P + wgi * 4), tag, RLX_AGENT);
        PAIR_JUMP(J12, WO(37, 0), WO(37, 1), WO(37, 2), WO(37, 3));
        const unsigned pflag = __hip_atomic_load((GAS unsigned*)(flags + FL_P + (r * 16 + (l & 15)) * 4), RLX_AGENT);
        PAIR_IN(WO(37, 4), NOP_, NOP_, NOP_);
        LBAR();
        if (!__all(pflag == tag)) wait_tags(flags, FL_P + (r * 16 + (l & 15)) * 4, true, tag);
#define WP(I, A) do { WO(74, I); PL((A) >> 2, (A) & 3); } while (0)
        PAIR_IN(WP(0, 0), WP(1, 1), WP(2, 2), WP(3, 3));
        PAIR_IN(WP(4, 4), PL2(5), PL2(7), PL2(9));
        PAIR(0, 0, 0, PL2(11), PL2(13), PL2(15), PL2(17));
        PL(4, 3);
        ATT_FIN();
    }
    STAMP(1, 6);
#undef l
    const int lc = fresh_lane();
#define l lc
    for (int attempt = 0;; ++attempt) {
        if (attempt == 1) {
            const unsigned kone = h5 ? 0u : 0x3f803f80u;
            const bool vrow = (l & 31) < 19;
            const unsigned k1_base = 8 * kh * 1024 + l * 16, k2_base = KV_K2 + 8 * kh * 128 + (l & 31) * 4;
            const unsigned v_base = vrow ? KV_V + 8 * kh * 1216 + h5 * 304 + (l & 31) * 16 : LDS_ZERO, v_mul = vrow ? 1u : 0u;
            WAIT_BAR(0);
#pragma unroll
            for (int c = 0; c < 4; ++c)
#pragma unroll
                for (int i = 0; i < 5; ++i) piece_kv(c, i);
            if (w == 0 && l < 40) *(LAS u32x4*)(L + LDS_ZERO + l * 16) = (u32x4){0u, 0u, 0u, 0u};
            WAIT_BAR(0);
            float m[2] = {-INFINITY, -INFINITY};
            for (int i = 0; i < 32; ++i) {
                const unsigned to = (i >> 3) == 0 ? LDS_KV0 : ((i >> 3) - 1) * KVCH;
                const bf16x8 k1 = lds_b128(L + k1_base + to + (i & 7) * 1024);
                const u32x2 k2 = {*(LAS const unsigned*)(L + k2_base + to + (i & 7) * 128), kone};
#pragma unroll
                for (int c = 0; c < 2; ++c) {
                    const u32x2 qz = {q2[c][0], 0u};
                    f32x16 sc = MFMA32(k1, q1[c], (f32x16){});
                    sc = MFMA32K8(__builtin_bit_cast(s16x4, k2), __builtin_bit_cast(s16x4, qz), sc);
#pragma unroll
                    for (int e = 0; e < 16; ++e) m[c] = fmaxf(m[c], sc[e]);
                }
            }
            LAS float* mx = (LAS float*)(L + LDS_MX);
#pragma unroll
            for (int c = 0; c < 2; ++c) { m[c] = half_max(m[c]); if (l < 32) mx[(w * 2 + c) * 32 + l] = m[c]; }
            LBAR();
#pragma unroll
            for (int c = 0; c < 2; ++c) {
                const float mm = fmaxf(m[c], mx[((w ^ 2) * 2 + c) * 32 + (l & 31)]);
                const float chi = rbf(mm), clo = mm - chi;
                q2[c][1] = h5 ? 0u : cvtpk(-chi, -clo);
            }
            o0 = (f32x16){}; o1 = (f32x16){};
            for (int i = 0; i < 32; ++i) {
                const unsigned to = (i >> 3) == 0 ? LDS_KV0 : ((i >> 3) - 1) * KVCH;
                const bf16x8 k1 = lds_b128(L + k1_base + to + (i & 7) * 1024);
                const u32x2 k2 = {*(LAS const unsigned*)(L + k2_base + to + (i & 7) * 128), kone};
                const unsigned va = v_base + v_mul * (to + (i & 7) * 1216);
                const bf16x8 v0 = lds_b128(L + va), v1 = lds_b128(L + va + 608);
#pragma unroll
                for (int c = 0; c < 2; ++c) {
                    f32x16 sc = MFMA32(k1, q1[c], (f32x16){});
                    sc = MFMA32K8(__builtin_bit_cast(s16x4, k2), __builtin_bit_cast(s16x4, q2[c]), sc);
                    u32x4 pa, pb;
#pragma unroll
                    for (int e = 0; e < 4; ++e) { pa[e] = cvtpk(EXP2(sc[2 * e]), EXP2(sc[2 * e + 1])); pb[e] = cvtpk(EXP2(sc[8 + 2 * e]), EXP2(sc[9 + 2 * e])); }
                    if (c == 0) { o0 = MFMA32(v0, __builtin_bit_cast(bf16x8, pa), o0); o0 = MFMA32(v1, __builtin_bit_cast(bf16x8, pb), o0); }
                    else { o1 = MFMA32(v0, __builtin_bit_cast(bf16x8, pa), o1); o1 = MFMA32(v1, __builtin_bit_cast(bf16x8, pb), o1); }
                }
            }
        }
        LAS float* red = (LAS float*)(L + LDS_RED);
        LBAR();
        if (attempt == 0) {
            piece_wo(111 + w); if (w == 0) piece_wo(119);
        } else {
#pragma unroll
            for (int q = 0; q < 15; ++q) piece_wo(w * 15 + q);
        }
#pragma unroll
        for (int e = 0; e < 11; ++e) { red[(w * 11 + e) * 64 + l] = kh ? o0[e] : o1[e]; own[e] = kh ? o1[e] : o0[e]; }
        LBAR();
#pragma unroll
        for (int e = 0; e < 11; ++e) own[e] += red[((w ^ 2) * 11 + e) * 64 + l];
        const float lsum = half_lo(own[10]);
        if (!(lsum >= 8.67e-19f && lsum < 3.0e38f) && attempt == 0) *(LAS unsigned*)(L + LDS_CTL) = 1u;
        const float rl = 1.0f / lsum;
        LAS char* ot = L + LDS_OT + (32 * (2 * cp + kh) + (l & 31)) * 80 + 40 * hh;
        *(LAS u32x2*)(ot + 8 * h5) = (u32x2){cvtpk(own[0] * rl, own[1] * rl), cvtpk(own[2] * rl, own[3] * rl)};
        *(LAS u32x2*)(ot + 16 + 8 * h5) = (u32x2){cvtpk(own[4] * rl, own[5] * rl), cvtpk(own[6] * rl, own[7] * rl)};
        if (h5 == 0) *(LAS u32x2*)(ot + 32) = (u32x2){cvtpk(own[8] * rl, own[9] * rl), 0u};
        LBAR();
        const unsigned isbad = *(LAS const unsigned*)(L + LDS_CTL);
        if (!(isbad && attempt == 0)) break;
    }
    STAMP(1, 7);
    {
        const __amdgpu_buffer_rsrc_t rw = __builtin_amdgcn_make_buffer_rsrc(ws + WS_O + (size_t)(r * 16 + s) * 128 * 80, 0, 128 * 80, 0x00020000);
        __builtin_amdgcn_raw_buffer_store_b128(*(LAS const u32x4*)(L + LDS_OT + w * 1024 + l * 16), rw, l * 16, w * 1024, 16);
        if (w < 2) __builtin_amdgcn_raw_buffer_store_b128(*(LAS const u32x4*)(L + LDS_OT + (8 + w) * 1024 + l * 16), rw, l * 16, (8 + w) * 1024, 16);
    }
    {
        const int tok = l & 15, g4 = l >> 4, q4 = (l >> 2) & 3, p4 = l & 3;
        const __amdgpu_buffer_rsrc_t ro = __builtin_amdgcn_make_buffer_rsrc(ws + WS_O + (size_t)r * 16 * 128 * 80, 0, 16 * 128 * 80, 0x00020000);
        u32x4 bf[5][2]; unsigned bo[5];
#pragma unroll
        for (int i = 0; i < 5; ++i) {
            const int kp = 32 * (5 * qk + i) + 8 * g4, sp = kp / 40, d = kp - 40 * sp;
            bo[i] = (sp * 128 + 64 * tgf + 32 * pr + tok) * 80 + d * 2;
#pragma unroll
            for (int nn = 0; nn < 2; ++nn) bf[i][nn] = __builtin_amdgcn_raw_buffer_load_b128(ro, bo[i], nn * 16 * 80, 16);
        }
        SB();
        u32x2 xv[3];
#pragma unroll
        for (int u = 0; u < 3; ++u) {
            const int tt = min(qk + 4 * u, 2 * nmt - 1);
            xv[u] = *(const u32x2*)(x + (size_t)(128 * r + 64 * tgf + 32 * pr + 16 * (tt & 1) + tok) * DM + 16 * (m0 + (tt >> 1)) + 4 * g4);
        }
        f32x4 fa[5][2];
#pragma unroll
        for (int t = 0; t < 5; ++t) {
            fa[t][0] = (f32x4){0.f, 0.f, 0.f, 0.f}; fa[t][1] = (f32x4){0.f, 0.f, 0.f, 0.f};
#pragma unroll
            for (int sp = 0; sp < 4; ++sp) {
                const u32x4 p = pl[t][sp];
                fa[t][0][0] += bflo(p[0]); fa[t][0][1] += bfhi(p[0]); fa[t][0][2] += bflo(p[1]); fa[t][0][3] += bfhi(p[1]);
                fa[t][1][0] += bflo(p[2]); fa[t][1][1] += bfhi(p[2]); fa[t][1][2] += bflo(p[3]); fa[t][1][3] += bfhi(p[3]);
            }
        }
#pragma unroll
        for (int t = 0; t < 5; ++t) asm volatile("" : "+v"(fa[t][0]), "+v"(fa[t][1]));
        STAMP(1, 8);
        WAIT_BAR(0);
        bf16x8 af[5][5];
        {
            const unsigned ao = (4 * g4 + q4) * 64 + 8 * (p4 & 1);
#pragma unroll
            for (int t = 0; t < 5; ++t)
#pragma unroll
                for (int i = 0; i < 5; ++i) {
                    const int p = ((t < nmt ? t : 0) >> 1) * 40 + (5 * qk + i) * 2;
                    const unsigned sw = ao + 16 * ((2 * (t & 1) + (p4 >> 1)) ^ (2 * (g4 & 1)));
                    const s16x4 lo = lds_tr(L + wo_lds(p) + sw), hi = lds_tr(L + wo_lds(p + 1) + sw);
                    af[t][i] = __builtin_shufflevector(lo, hi, 0, 1, 2, 3, 4, 5, 6, 7);
                }
        }
        LBAR();
        unsigned pend = 0x3ffu;
        for (unsigned spins = 0;; ++spins) {
#pragma unroll
            for (int i = 0; i < 5; ++i)
#pragma unroll
                for (int nn = 0; nn < 2; ++nn)
                    if ((pend & (1u << (2 * i + nn))) && __all(bf[i][nn][0] != O_EMPTY && bf[i][nn][1] != O_EMPTY && bf[i][nn][2] != O_EMPTY && bf[i][nn][3] != O_EMPTY)) pend &= ~(1u << (2 * i + nn));
            if (pend == 0u) break;
            if (spins > (1u << 17)) { __hip_atomic_store((GAS unsigned*)(flags + FL_TMO), tag, RLX_AGENT); break; }
            __builtin_amdgcn_s_sleep(OSLEEP);
#pragma unroll
            for (int i = 0; i < 5; ++i)
#pragma unroll
                for (int nn = 0; nn < 2; ++nn)
                    if (pend & (1u << (2 * i + nn))) bf[i][nn] = __builtin_amdgcn_raw_buffer_load_b128(ro, bo[i], nn * 16 * 80, 16);
        }
        STAMP(1, 9);
#pragma unroll
        for (int i = 0; i < 5; ++i)
#pragma unroll
            for (int t = 0; t < 5; ++t) {
                fa[t][0] = MFMA16(af[t][i], __builtin_bit_cast(bf16x8, bf[i][0]), fa[t][0]); fa[t][1] = MFMA16(af[t][i], __builtin_bit_cast(bf16x8, bf[i][1]), fa[t][1]);
            }
        STAMP(1, 10);
        LAS f32x4* ex = (LAS f32x4*)L;
#pragma unroll
        for (int t = 0; t < 5; ++t) { ex[(w * 10 + 2 * t) * 64 + l] = fa[t][0]; ex[(w * 10 + 2 * t + 1) * 64 + l] = fa[t][1]; }
        LBAR();
#pragma unroll
        for (int u = 0; u < 3; ++u) {
            const int tt = qk + 4 * u;
            if (tt < 2 * nmt) {
                f32x4 v = ex[((0 * 2 + pr) * 10 + tt) * 64 + l];
#pragma unroll
                for (int k = 1; k < 4; ++k) v += ex[((k * 2 + pr) * 10 + tt) * 64 + l];
                const size_t off = (size_t)(128 * r + 64 * tgf + 32 * pr + 16 * (tt & 1) + tok) * DM + 16 * (m0 + (tt >> 1)) + 4 * g4;
                *(u32x2*)(out + off) = (u32x2){cvtpk(v[0] + bflo(xv[u][0]), v[1] + bfhi(xv[u][0])), cvtpk(v[2] + bflo(xv[u][1]), v[3] + bfhi(xv[u][1]))};
            }
        }
    }
#undef l
#undef h5
    STAMP(1, 11); STAMP_END(1);
    asm volatile("fused_block_code_end:");
}

extern "C" void kernel_launch(void* const* d_in, const int* in_sizes, int n_in, void* d_out, int out_size, void* d_ws, size_t ws_size, hipStream_t stream) {
    const u16* x = (const u16*)d_in[0]; const u16* wq = (const u16*)d_in[1]; const u16* wk = (const u16*)d_in[2]; const u16* wv = (const u16*)d_in[3];
    const u16* wo = (const u16*)d_in[4]; const u16* wg = (const u16*)d_in[6]; const u16* wu = (const u16*)d_in[7]; const u16* wd = (const u16*)d_in[8];
    fused_block<<<256, 512, 0, stream>>>(x, wq, wk, wv, wo, wg, wu, wd, (char*)d_ws, (u16*)d_out);
}
```

```cpp
#include <hip/hip_runtime.h>
#include <hip/hip_bf16.h>
#include <stdint.h>
#include <type_traits>

typedef unsigned short u16;
typedef short bf16x8 __attribute__((ext_vector_type(8)));
typedef short s16x4 __attribute__((ext_vector_type(4)));
typedef float f32x4 __attribute__((ext_vector_type(4)));
typedef float f32x16 __attribute__((ext_vector_type(16)));
typedef int i32x4 __attribute__((ext_vector_type(4)));
typedef unsigned u32x4 __attribute__((ext_vector_type(4)));
typedef unsigned u32x2 __attribute__((ext_vector_type(2)));
typedef __bf16 bf16x2_t __attribute__((ext_vector_type(2)));
typedef float f32x2 __attribute__((ext_vector_type(2)));
typedef short v4i16_t __attribute__((ext_vector_type(4)));
extern "C" __device__ unsigned long long amd_dispatch_id() __asm("llvm.amdgcn.dispatch.id");
#define LAS __attribute__((address_space(3)))
#define GAS __attribute__((address_space(1)))
#define DI __device__ __forceinline__

constexpr int SEQ = 2048, DM = 576, NH = 32, DH = 18, FF = 4608;

constexpr int KVCH = 37888;
constexpr int KV_V = 16384, KV_K2 = 35840;
constexpr int WOIMG = 120 * 1024;
constexpr size_t WS_FLAGS = 0;
constexpr size_t WS_XN    = 16384;
constexpr size_t WS_WQX   = WS_XN + (size_t)SEQ * DM * 2;
constexpr size_t WS_WO    = WS_WQX + (size_t)DM * 1024 * 2;
constexpr size_t WS_KV    = WS_WO + (size_t)8 * WOIMG;
constexpr size_t WS_O     = WS_KV + 4 * KVCH;
constexpr size_t WS_PART  = WS_O + (size_t)256 * 128 * 80;
constexpr size_t WS_END   = WS_PART + (size_t)256 * 8 * 18 * 1024;
constexpr int FL_PRO = 0, FL_P = 1024, FL_KV = 2048, FL_TMO = 8192;
constexpr unsigned O_EMPTY = 0xffffffffu;

DI float bflo(unsigned v) { return __uint_as_float(v << 16); }
DI float bfhi(unsigned v) { return __uint_as_float(v & 0xffff0000u); }
DI unsigned cvtpk(float lo, float hi) { return __builtin_bit_cast(unsigned, __builtin_convertvector((f32x2){lo, hi}, bf16x2_t)); }
DI float rbf(float f) { return bflo(cvtpk(f, f)); }
DI i32x4 make_srd(const void* p, unsigned bytes) {
    unsigned long long a = (unsigned long long)p; i32x4 s;
    s.x = __builtin_amdgcn_readfirstlane((unsigned)a); s.y = __builtin_amdgcn_readfirstlane((unsigned)(a >> 32) & 0xffffu);
    s.z = (int)bytes; s.w = 0x00020000; return s;
}
DI void dma16(i32x4 srd, unsigned voff, unsigned soff, unsigned lds_addr) {
    unsigned keep;
    asm volatile("s_mov_b32 %0, m0\n\ts_mov_b32 m0, %4\n\ts_nop 0\n\tbuffer_load_dwordx4 %1, %2, %3 offen lds\n\ts_mov_b32 m0, %0"
                 : "=&s"(keep) : "v"(voff), "s"(srd), "s"(soff), "s"(lds_addr) : "memory");
}
DI void dma16_sc1(i32x4 srd, unsigned voff, unsigned soff, unsigned lds_addr) {
    unsigned keep;
    asm volatile("s_mov_b32 %0, m0\n\ts_mov_b32 m0, %4\n\ts_nop 0\n\tbuffer_load_dwordx4 %1, %2, %3 offen sc1 lds\n\ts_mov_b32 m0, %0"
                 : "=&s"(keep) : "v"(voff), "s"(srd), "s"(soff), "s"(lds_addr) : "memory");
}
DI void dma4(i32x4 srd, unsigned voff, unsigned soff, unsigned lds_addr) {
    unsigned keep;
    asm volatile("s_mov_b32 %0, m0\n\ts_mov_b32 m0, %4\n\ts_nop 0\n\tbuffer_load_dword %1, %2, %3 offen lds\n\ts_mov_b32 m0, %0"
                 : "=&s"(keep) : "v"(voff), "s"(srd), "s"(soff), "s"(lds_addr) : "memory");
}
#define WAIT_BAR(N) asm volatile("s_waitcnt vmcnt(" #N ") lgkmcnt(0)\n\ts_barrier" ::: "memory")
#define LBAR() asm volatile("s_waitcnt lgkmcnt(0)\n\ts_barrier" ::: "memory")
#define SB() __builtin_amdgcn_sched_barrier(0)
#define FENCE() asm volatile("" ::: "memory")
#define PIN4(a) asm volatile("" : "+v"((a)[0]), "+v"((a)[1]), "+v"((a)[2]), "+v"((a)[3]))
#define MFMA16(a, b, c) __builtin_amdgcn_mfma_f32_16x16x32_bf16(a, b, c, 0, 0, 0)
#define MFMA32(a, b, c) __builtin_amdgcn_mfma_f32_32x32x16_bf16(a, b, c, 0, 0, 0)
#define MFMA32K8(a, b, c) __builtin_amdgcn_mfma_f32_32x32x8bf16_1k(a, b, c, 0, 0, 0)
#define EXP2(x) __builtin_amdgcn_exp2f(x)
DI s16x4 lds_tr(LAS const char* p) { return __builtin_bit_cast(s16x4, __builtin_amdgcn_ds_read_tr16_b64_v4i16((LAS v4i16_t*)p)); }
DI bf16x8 lds_b128(LAS const char* p) { return *(LAS const bf16x8*)p; }
DI bf16x8 lds_afrag(LAS const char* p) { const s16x4 lo = lds_tr(p), hi = lds_tr(p + 2048); return __builtin_shufflevector(lo, hi, 0, 1, 2, 3, 4, 5, 6, 7); }
#define RLX_AGENT __ATOMIC_RELAXED, __HIP_MEMORY_SCOPE_AGENT
template <int X> DI float xor_lane(float v) { return __int_as_float(__builtin_amdgcn_ds_swizzle(__float_as_int(v), (X << 10) | 0x1f)); }
DI float half_sum(float v) { auto r = __builtin_amdgcn_permlane32_swap(__float_as_uint(v), __float_as_uint(v), false, false); return __uint_as_float(r[0]) + __uint_as_float(r[1]); }
DI float half_max(float v) { auto r = __builtin_amdgcn_permlane32_swap(__float_as_uint(v), __float_as_uint(v), false, false); return fmaxf(__uint_as_float(r[0]), __uint_as_float(r[1])); }
DI float half_lo(float v) { auto r = __builtin_amdgcn_permlane32_swap(__float_as_uint(v), __float_as_uint(v), false, false); return __uint_as_float(r[0]); }
template <int CTRL, int ROWS> DI float dpp_add(float v) { return v + __int_as_float(__builtin_amdgcn_update_dpp(0, __float_as_int(v), CTRL, ROWS, 0xf, true)); }
DI float wave_sum_dpp(float v) {
    v = dpp_add<0x111, 0xf>(v); v = dpp_add<0x112, 0xf>(v); v = dpp_add<0x114, 0xf>(v); v = dpp_add<0x118, 0xf>(v);
    v = dpp_add<0x142, 0xa>(v); v = dpp_add<0x143, 0xc>(v);
    return __int_as_float(__builtin_amdgcn_readlane(__float_as_int(v), 63));
}
DI float wave_sum(float v) { v += xor_lane<1>(v); v += xor_lane<2>(v); v += xor_lane<4>(v); v += xor_lane<8>(v); v += xor_lane<16>(v); return half_sum(v); }
DI float wave_max(float v) { v = fmaxf(v, xor_lane<1>(v)); v = fmaxf(v, xor_lane<2>(v)); v = fmaxf(v, xor_lane<4>(v)); v = fmaxf(v, xor_lane<8>(v)); v = fmaxf(v, xor_lane<16>(v)); return half_max(v); }
DI int fresh_lane() { int t; asm volatile("v_mbcnt_lo_u32_b32 %0, -1, 0\n\tv_mbcnt_hi_u32_b32 %0, -1, %0" : "=v"(t)); return t; }
DI void st_agent64(void* p, unsigned lo, unsigned hi) { __hip_atomic_store((GAS unsigned long long*)p, ((unsigned long long)hi << 32) | lo, RLX_AGENT); }
DI void wait_tags(const char* flags, unsigned off, bool use, unsigned tag) {
    for (unsigned spins = 0;; ++spins) {
        const unsigned v = use ? __hip_atomic_load((GAS unsigned*)(flags + off), RLX_AGENT) : tag;
        if (__all(v == tag)) break;
        if (spins > (1u << 19)) { __hip_atomic_store((GAS unsigned*)(flags + FL_TMO), tag, RLX_AGENT); break; }
        __builtin_amdgcn_s_sleep(1);
    }
    FENCE();
}

#define DMA345 dma16_sc1
constexpr int ST1 = 49152;
constexpr int LDS_D0 = 0, LDS_D1 = 49152, LDS_H = 86016, LDS_Q1 = 36864, LDS_Q2 = 45056;
constexpr int LDS_PF = 147456;
constexpr int LDS_KM = 46080;
constexpr int LDS_D2 = 126976;
constexpr int LDS_KV0 = 125952;
constexpr int LDS_OT = 113664;
constexpr int LDS_MX = LDS_OT;
constexpr int LDS_ZERO = LDS_OT + 10240;
constexpr int LDS_CTL = LDS_ZERO + 640;
constexpr int LDS_RED = 2 * KVCH + 9 * 1024;
constexpr int LDS_BYTES = 163840;
DI unsigned wo_lds(int p) { return p < 37 ? LDS_KV0 + p * 1024 : p < 74 ? (p - 37) * 1024 : p < 111 ? KVCH + (p - 74) * 1024 : 2 * KVCH + (p - 111) * 1024; }

template <int NM, bool FIRST, int ND, int DPG, int AD = 3, bool PIN = false, class Dma>
DI void gemm_step(f32x4 (&acc)[10][4], bf16x8 (&ca)[2], bf16x8 (&cb)[4], bf16x8 (&nb)[4], LAS const char* ap, LAS const char* bp, const Dma& dma) {
    bf16x8 a[10];
#pragma unroll
    for (int n = 0; n < 4; ++n) nb[n] = lds_b128(bp + n * 1024);
    a[0] = lds_afrag(ap); a[1] = lds_afrag(ap + 4096); if (AD > 2) a[2] = lds_afrag(ap + 2 * 4096);
    SB();
    int d = 0;
    if (!FIRST) {
#pragma unroll
        for (int k = 0; k < DPG; ++k) if (d < ND) dma(d++);
#pragma unroll
        for (int n = 0; n < 4; ++n) acc[NM - 2][n] = MFMA16(ca[0], cb[n], acc[NM - 2][n]);
        if (PIN) PIN4(acc[NM - 2]); SB();
#pragma unroll
        for (int k = 0; k < DPG; ++k) if (d < ND) dma(d++);
#pragma unroll
        for (int n = 0; n < 4; ++n) acc[NM - 1][n] = MFMA16(ca[1], cb[n], acc[NM - 1][n]);
        if (PIN) PIN4(acc[NM - 1]); SB();
    }
#pragma unroll
    for (int m = 0; m < NM - 2; ++m) {
        if (m + AD < NM) a[m + AD] = lds_afrag(ap + (m + AD) * 4096);
#pragma unroll
        for (int k = 0; k < DPG; ++k) if (d < ND) dma(d++);
#pragma unroll
        for (int n = 0; n < 4; ++n) acc[m][n] = MFMA16(a[m], nb[n], acc[m][n]);
        if (PIN) PIN4(acc[m]); SB();
    }
    ca[0] = a[NM - 2]; ca[1] = a[NM - 1];
}
template <int NM>
DI void gemm_flush(f32x4 (&acc)[10][4], const bf16x8 (&ca)[2], const bf16x8 (&cb)[4]) {
#pragma unroll
    for (int n = 0; n < 4; ++n) acc[NM - 2][n] = MFMA16(ca[0], cb[n], acc[NM - 2][n]);
#pragma unroll
    for (int n = 0; n < 4; ++n) acc[NM - 1][n] = MFMA16(ca[1], cb[n], acc[NM - 1][n]);
}

__global__ __launch_bounds__(512, 2) void fused_block(const u16* __restrict__ x, const u16* __restrict__ wq, const u16* __restrict__ wk, const u16* __restrict__ wv,
                                                       const u16* __restrict__ wo, const u16* __restrict__ wg, const u16* __restrict__ wu, const u16* __restrict__ wd,
                                                       char* __restrict__ ws, u16* __restrict__ out) {
    __shared__ __attribute__((aligned(1024))) char lds[LDS_BYTES];
    const int tid = threadIdx.x, l = tid & 63;
    const int w = __builtin_amdgcn_readfirstlane(tid >> 6);
    const int b = blockIdx.x, s = 2 * (b & 7) + ((b >> 3) & 1), r = b >> 4, wgi = r * 16 + s;
    const int tg = w >> 2, fg = w & 3;
    constexpr unsigned lds0 = 0;
    LAS char* L = (LAS char*)lds;
    char* flags = ws + WS_FLAGS;
    const unsigned tag = (((unsigned)amd_dispatch_id() + 1u) * 0x9E3779B1u) ^ ((unsigned)((unsigned long long)__builtin_amdgcn_dispatch_ptr() >> 6) * 0x85EBCA77u) ^ 0x5bd1e995u;

    const int dmat = w & 1, dR = (w >> 1) & 1, dcg0 = 5 * (w >> 2);
    const int di = l >> 2, dsig = l & 3;
    const int dmi = dsig ^ (2 * dmat + ((di >> 2) & 1)), dkrow = 8 * (di >> 2) + (di & 3);
    const i32x4 srd_w = make_srd(dmat ? wu : wg, DM * FF * 2);
    const i32x4 srd_q = make_srd(ws + WS_WQX, DM * 1024 * 2);
    const i32x4 srd_x = make_srd(ws + WS_XN, SEQ * DM * 2);
    const unsigned vo_w = dkrow * (FF * 2) + 16 * dmi, vo_q = dkrow * 2048 + 16 * dmi, vo_x = l * 16;
    const unsigned dlds = lds0 + dR * 2048 + dmat * 1024;
    auto pieceW = [&](int j, unsigned slot, int cg) { dma16(srd_w, vo_w, (32 * j + 4 * dR) * (FF * 2) + 288 * s * 2 + cg * 64, dlds + slot + cg * 4096); };
    auto pieceQ = [&](int j, unsigned slot) { dma16_sc1(srd_q, vo_q, (32 * j + 4 * dR) * 2048 + (64 * s + 32 * dmat) * 2, dlds + slot + 9 * 4096); };
    auto pieceX = [&](int j, unsigned slot, int n) { dma16_sc1(srd_x, vo_x, ((r * 18 + j) * 8 + n) * 1024, lds0 + slot + 40960 + n * 1024); };
    auto piece1k = [&](int j, unsigned slot, int i, int kind) {
        if (i < 3) { if (kind & 1) pieceW(j, slot, dcg0 + i); }
        else if (i == 3) { if (w == 3) { if (kind & 2) pieceQ(j, slot); } else if (kind & 1) pieceW(j, slot, dcg0 + 3); }
        else if (i == 4) {
            if (w < 3) { if (kind & 1) pieceW(j, slot, 4); } else if (w == 3) { if (kind & 2) pieceX(j, slot, 7); }
            else if (w < 7) { if (kind & 2) pieceQ(j, slot); } else if (kind & 1) pieceW(j, slot, 3);
        } else { if (w == 7) { if (kind & 1) pieceW(j, slot, 4); } else if (kind & 2) pieceX(j, slot, w); }
    };
    auto piece2 = [&](int j, unsigned slot, int i) {
        const int ii = (w >= 4 && i == 4) ? 3 : i;
        const unsigned vo_d = ((vo_w & ~63u) >> 3) + ((vo_w & 48u) << 1);
        dma16(make_srd(wd, FF * DM * 2), vo_d, (288 * s + 32 * j + 4 * dR) * (DM * 2) + 16 * dmat + (dcg0 + ii) * 128, dlds + slot + (dcg0 + ii) * 4096);
    };

    const u32x4* xs = (const u32x4*)(x + (size_t)(128 * r + 8 * s + w) * DM);
    const u32x4 v0 = xs[l];
    u32x4 v1 = xs[64 + (l & 7)];
    FENCE();
    if (w == 7) {
        unsigned long long pc; unsigned len;
        asm volatile("s_getpc_b64 %0\n\tcode_here_%=:\n\ts_mov_b32 %1, fused_block_code_end-code_here_%=" : "=s"(pc), "=s"(len));
        i32x4 sc; sc.x = (int)(unsigned)pc; sc.y = (int)((unsigned)(pc >> 32) & 0xffffu); sc.z = (int)len; sc.w = 0x00020000;
        dma4(sc, l < 20 ? (unsigned)(((b >> 3) * 20 + l) * 128) : 0x80000000u, 0, lds0 + LDS_PF);
    }
    {
        unsigned qv[8];
        {
            const int tq = min(tid, 287), k = 36 * r + (tq >> 3), mat = (tq >> 2) & 1, t = tq & 3;
            const u16* wc = ((s & 1) ? wv : wk) + k * DH;
            const u16* wh = wq + (size_t)k * DM + (2 * s + (t >> 1)) * DH;
#pragma unroll
            for (int e = 0; e < 8; ++e) {
                const int rho = mat * 8 + e, cc = (t == 1 ? 0 : 12) + rho - 4;
                const bool isq = (t & 1) == 0 || rho < 2, isc = !isq && rho >= 4 && cc < (t == 1 ? 12 : 18);
                const u16* p = isc ? wc + cc : wh + ((t & 1) == 0 ? rho : 16 + (rho & 1));
                const unsigned val = *p;
                qv[e] = (isq || isc) ? val : 0u;
            }
        }
        const int cgp = s & 7, wp = 4 * (r * 2 + (s >> 3)) + ((min(max(tid, 192), 447) - 192) >> 6);
        const bool wo_act = tid >= 192 && tid < 448 && wp < 120;
        u32x4 wov;
        {
            const int m0 = cgp < 4 ? 5 * cgp : 20 + 4 * (cgp - 4), wpc = min(wp, 119);
            const int pp = wpc / 40, js = (wpc % 40) >> 1, R = wpc & 1, i = l >> 2, ch = (l & 3) ^ (2 * ((i >> 2) & 1));
            const int kp = 32 * js + 8 * (i >> 2) + (i & 3) + 4 * R, sp = kp / 40, d = kp - 40 * sp, hd = d >= 20 ? 1 : 0, dd = d - 20 * hd;
            const int row = 36 * sp + 18 * hd + (dd < 18 ? dd : 17), col = min(16 * (m0 + 2 * pp) + 8 * ch, DM - 8);
            wov = *(const u32x4*)(wo + (size_t)row * DM + col);
        }
        FENCE();
        if (l >= 8) v1 = (u32x4){0u, 0u, 0u, 0u};
        float sm = 0.f;
#pragma unroll
        for (int e = 0; e < 4; ++e) sm += bflo(v0[e]) + bfhi(v0[e]) + bflo(v1[e]) + bfhi(v1[e]);
        const float mean = wave_sum_dpp(sm) * (1.0f / DM);
        float q = 0.f, q1 = 0.f;
#pragma unroll
        for (int e = 0; e < 4; ++e) { const float a = bflo(v0[e]) - mean, c = bfhi(v0[e]) - mean; q += a * a + c * c; }
#pragma unroll
        for (int e = 0; e < 4; ++e) { const float a = bflo(v1[e]) - mean, c = bfhi(v1[e]) - mean; q1 += a * a + c * c; }
        if (l < 8) q += q1;
        const float m = rbf(mean), var = rbf(wave_sum_dpp(q) * (1.0f / DM));
        const float rs = 1.0f / rbf(sqrtf(rbf(var + 1e-5f)));
        LAS char* xst = L + 2 * ST1;
        {
            u32x4 o;
#pragma unroll
            for (int e = 0; e < 4; ++e) o[e] = cvtpk(rbf(bflo(v0[e]) - m) * rs, rbf(bfhi(v0[e]) - m) * rs);
            *(LAS u32x4*)(xst + l * 128 + w * 16) = o;
#pragma unroll
            for (int e = 0; e < 4; ++e) o[e] = cvtpk(rbf(bflo(v1[e]) - m) * rs, rbf(bfhi(v1[e]) - m) * rs);
            if (l < 8) *(LAS u32x4*)(xst + (64 + l) * 128 + w * 16) = o;
        }
        asm volatile("" : "+v"(wov));
        LBAR();
        if (w < 7) {
            const __amdgpu_buffer_rsrc_t rx = __builtin_amdgcn_make_buffer_rsrc(ws + WS_XN, 0, SEQ * DM * 2, 0x00020000);
#pragma unroll
            for (int u = 0; u < 2; ++u) {
                const int i = tid + 448 * u, c = i >> 3, t = i & 7;
                const u32x4 v = *(LAS const u32x4*)(xst + min(i, 575) * 16);
                __builtin_amdgcn_raw_buffer_store_b128(v, rx, i < 576 ? (unsigned)(((r * 18 + (c >> 2)) * 8 + (s >> 1)) * 1024 + ((c & 3) * 16 + 8 * (s & 1) + t) * 16) : 0xfffffff0u, 0, 16);
            }
            {
                u32x4 o; o[0] = qv[0] | (qv[1] << 16); o[1] = qv[2] | (qv[3] << 16); o[2] = qv[4] | (qv[5] << 16); o[3] = qv[6] | (qv[7] << 16);
                const __amdgpu_buffer_rsrc_t rq = __builtin_amdgcn_make_buffer_rsrc(ws + WS_WQX, 0, DM * 1024 * 2, 0x00020000);
                __builtin_amdgcn_raw_buffer_store_b128(o, rq, tid < 288 ? (unsigned)(((36 * r + (tid >> 3)) * 128 + s * 8 + (tid & 7)) * 16) : 0xfffffff0u, 0, 16);
            }
            WAIT_BAR(0);
        } else {
            const unsigned o = ((l & 7) < 5 && l < 32) ? (unsigned)((4 * r + (l >> 3)) * (FF * 2) + ((288 * s * 2) & ~127) + (l & 7) * 128) : 0x80000000u;
            dma4(make_srd(wg, DM * FF * 2), o, 0, lds0 + LDS_PF); dma4(srd_w, o, 0, lds0 + LDS_PF);
            LBAR();
        }
        if (tid == 0) __hip_atomic_store((GAS unsigned*)(flags + FL_PRO + wgi * 4), tag, RLX_AGENT);
#pragma unroll
        for (int i = 0; i < 6; ++i) piece1k(0, 0, i, 1);
#pragma unroll
        for (int i = 0; i < 6; ++i) piece1k(1, ST1, i, 1);
        if (w < 7) {
            const __amdgpu_buffer_rsrc_t rw = __builtin_amdgcn_make_buffer_rsrc(ws + WS_WO, 0, 8 * WOIMG, 0x00020000);
            __builtin_amdgcn_raw_buffer_store_b128(wov, rw, wo_act ? (unsigned)(cgp * WOIMG + wp * 1024 + l * 16) : 0xfffffff0u, 0, 16);
            const __amdgpu_buffer_rsrc_t ro = __builtin_amdgcn_make_buffer_rsrc(ws + WS_O + (size_t)wgi * 128 * 80, 0, 128 * 80, 0x00020000);
            const u32x4 e = (u32x4){O_EMPTY, O_EMPTY, O_EMPTY, O_EMPTY};
            __builtin_amdgcn_raw_buffer_store_b128(e, ro, tid * 16, 0, 16);
            __builtin_amdgcn_raw_buffer_store_b128(e, ro, (tid + 448) * 16, 0, 16);
        }
        if (w == 0) wait_tags(flags, FL_PRO + (l < 16 ? (r * 16 + l) * 4 : ((l - 16) * 16 + s) * 4), l < 32, tag);
        asm volatile("s_barrier" ::: "memory");
#pragma unroll
        for (int i = 3; i < 6; ++i) piece1k(0, 0, i, 2);
#pragma unroll
        for (int i = 3; i < 6; ++i) piece1k(1, ST1, i, 2);
        if (w == 7) {
            const unsigned o = (l & 7) < 5 ? (unsigned)(((l >> 3) * 16 + r) * (FF * 2) + ((288 * s * 2) & ~127) + (l & 7) * 128) : 0x80000000u;
#pragma unroll
            for (int g = 0; g < 2; ++g) { dma4(make_srd(wg, DM * FF * 2), o, g * 128 * (FF * 2), lds0 + LDS_PF); dma4(srd_w, o, g * 128 * (FF * 2), lds0 + LDS_PF); }
        }
    }
    const int lg = fresh_lane();
    const unsigned a_off = ((lg & 3) >> 1) * 1024 + 16 * (4 * (4 * (lg >> 4) + ((lg >> 2) & 3)) + (fg ^ (2 * ((lg & 3) >> 1) + ((lg >> 4) & 1)))) + 8 * (lg & 1);

    f32x4 acc[10][4];
#pragma unroll
    for (int m = 0; m < 10; ++m)
#pragma unroll
        for (int n = 0; n < 4; ++n) acc[m][n] = (f32x4){0.f, 0.f, 0.f, 0.f};
    bf16x8 ca[2], bA[4], bB[4];
    {
        const unsigned bo = 40960 + tg * 4096 + lg * 16;
        if (w == 7) { WAIT_BAR(4); } else { WAIT_BAR(0); }
        const bool q3 = w == 3, w4 = w < 3 || w == 7, x4 = w == 3, w5 = w == 7;
        const i32x4 srd3 = q3 ? srd_q : srd_w, srd4 = w4 ? srd_w : x4 ? srd_x : srd_q, srd5 = w5 ? srd_w : srd_x;
        const unsigned vo3 = q3 ? vo_q : vo_w, vo4 = w4 ? vo_w : x4 ? vo_x : vo_q, vo5 = w5 ? vo_w : vo_x;
        const unsigned soW = 4 * dR * (FF * 2) + 288 * s * 2, soQ = 4 * dR * 2048 + (64 * s + 32 * dmat) * 2, soX = r * 18 * 8192;
        const unsigned stW = 32 * FF * 2, stQ = 32 * 2048, stX = 8192;
        const unsigned so3 = q3 ? soQ : soW + (dcg0 + 3) * 64, st3 = q3 ? stQ : stW, lo3 = q3 ? dlds + 9 * 4096 : dlds + (dcg0 + 3) * 4096;
        const int cg4 = w < 3 ? 4 : 3;
        const unsigned so4 = w4 ? soW + cg4 * 64 : x4 ? soX + 7 * 1024 : soQ, st4 = w4 ? stW : x4 ? stX : stQ, lo4 = w4 ? dlds + cg4 * 4096 : x4 ? lds0 + 40960 + 7 * 1024 : dlds + 9 * 4096;
        const unsigned so5 = w5 ? soW + 4 * 64 : soX + w * 1024, st5 = w5 ? stW : stX, lo5 = w5 ? dlds + 4 * 4096 : lds0 + 40960 + w * 1024;
        unsigned vo_pf;
        { const int lp = fresh_lane(); vo_pf = (lp & 7) < 5 ? (unsigned)(((lp >> 3) * 16 + r) * (FF * 2) + ((288 * s * 2) & ~127) + (lp & 7) * 128) : 0x80000000u; }
        auto stage1 = [&](int j, unsigned slot, int i) {
            if (i < 3) dma16(srd_w, vo_w, soW + (dcg0 + i) * 64 + j * stW, dlds + (dcg0 + i) * 4096 + slot);
            else if (i == 3) DMA345(srd3, vo3, so3 + j * st3, lo3 + slot);
            else if (i == 4) DMA345(srd4, vo4, so4 + j * st4, lo4 + slot);
            else if (i == 5) DMA345(srd5, vo5, so5 + j * st5, lo5 + slot);
            else if (w >= 6) {
                const int t = j - 2;
                if (t == 6) { const int lp = fresh_lane(); vo_pf = (w == 7 && (lp & 15) < 9) ? (unsigned)((288 * s + (lp >> 4) * 16 + r) * (DM * 2) + (lp & 15) * 128) : 0x80000000u; }
                const unsigned so = t < 6 ? ((t & 1) ? 0x70000000u : (unsigned)(2 + (t >> 1)) * 128 * (FF * 2)) : t < 11 ? (unsigned)(t - 6) * 64 * (DM * 2) : 0x70000000u;
                dma4(t < 6 ? srd_w : make_srd(wd, FF * DM * 2), vo_pf, so, lds0 + LDS_PF);
            }
        };
        gemm_step<10, true, 7, 1>(acc, ca, bB, bA, L + a_off, L + bo, [&](int i) { stage1(2, 2 * ST1, i); });
        unsigned slot = ST1, slot2 = 0;
        if (w >= 6) { WAIT_BAR(7); } else { WAIT_BAR(6); }
        gemm_step<10, false, 7, 1>(acc, ca, bA, bB, L + slot + a_off, L + slot + bo, [&](int i) { stage1(3, slot2, i); });
        slot = 2 * ST1; slot2 = ST1;
#pragma unroll 1
        for (int j = 2; j < 16; j += 2) {
            if (w >= 6) { WAIT_BAR(8); } else { WAIT_BAR(6); }
            gemm_step<10, false, 7, 1>(acc, ca, bB, bA, L + slot + a_off, L + slot + bo, [&](int i) { stage1(j + 2, slot2, i); });
            slot = (slot == 2 * ST1) ? 0 : slot + ST1; slot2 = (slot2 == 2 * ST1) ? 0 : slot2 + ST1;
            if (w >= 6) { WAIT_BAR(8); } else { WAIT_BAR(6); }
            gemm_step<10, false, 7, 1>(acc, ca, bA, bB, L + slot + a_off, L + slot + bo, [&](int i) { stage1(j + 3, slot2, i); });
            slot = (slot == 2 * ST1) ? 0 : slot + ST1; slot2 = (slot2 == 2 * ST1) ? 0 : slot2 + ST1;
        }
#pragma unroll
        for (int n = 0; n < 4; ++n) bA[n] = bB[n];
#pragma unroll 1
        for (int j = 16; j < 18; ++j) {
            if (j == 16 && w >= 6) { WAIT_BAR(8); } else if (j == 16) { WAIT_BAR(6); } else { WAIT_BAR(5); }
            gemm_step<10, false, 5, 1, 3, true>(acc, ca, bA, bB, L + slot + a_off, L + slot + bo, [&](int i) { piece2(j - 16, j == 16 ? LDS_D0 : LDS_D1, i); });
#pragma unroll
            for (int n = 0; n < 4; ++n) bA[n] = bB[n];
            slot += ST1;
        }
    }
    WAIT_BAR(0);
    gemm_flush<10>(acc, ca, bA);
    auto h_tiles = [&](auto CG0, auto CG1) {
        const int l = fresh_lane(), tok = l & 15, g4 = l >> 4;
#pragma unroll
        for (int cg = CG0; cg < CG1; ++cg)
#pragma unroll
            for (int pr = 0; pr < 2; ++pr) {
                float h[4];
#pragma unroll
                for (int e = 0; e < 4; ++e) {
                    auto sw = __builtin_amdgcn_permlane32_swap(__float_as_uint(acc[cg][2 * pr][e]), __float_as_uint(acc[cg][2 * pr + 1][e]), false, false);
                    h[e] = __int_as_float(max((int)sw[0], 0)) * __uint_as_float(sw[1]);
                }
                const int n = 4 * tg + 2 * pr + (l >> 5);
                u32x2 o; o[0] = cvtpk(h[0], h[1]); o[1] = cvtpk(h[2], h[3]);
                *(LAS u32x2*)(L + LDS_H + ((8 - cg) * 8 + n) * 1024 + (fg * 16 + tok) * 16 + 8 * (g4 & 1)) = o;
            }
    };
    float kn2 = 0.f;
    if ((fg & 1) && tg == (s >> 3)) {
        const int l = fresh_lane(), tok = l & 15, g4 = l >> 4;
        const int e = s >> 1;
        f32x4 cv = acc[9][0];
        if ((e & 3) == 1) cv = acc[9][1]; else if ((e & 3) == 2) cv = acc[9][2]; else if ((e & 3) == 3) cv = acc[9][3];
        const int c0 = (fg == 1 ? 0 : 12) + 4 * (g4 - 1);
        const bool act = g4 >= 1 && c0 < 18;
        const unsigned p0 = cvtpk(cv[0], cv[1]), p1 = cvtpk(cv[2], cv[3]);
        const int kt = 4 * r + (e >> 1);
        char* kvb = ws + WS_KV + ((kt >> 3) & 3) * KVCH; const int lt = 8 * (kt >> 5) + (kt & 7);
        if ((s & 1) == 0) {
            const int key = 16 * (e & 1) + tok;
            if (act) {
                if (c0 < 16) st_agent64(kvb + lt * 1024 + ((c0 >> 3) * 32 + key) * 16 + (c0 & 4) * 2, p0, p1);
                else __hip_atomic_store((GAS unsigned*)(kvb + KV_K2 + lt * 128 + key * 4), p0, RLX_AGENT);
            }
            float n2 = 0.f;
            if (act) { n2 = bflo(p0) * bflo(p0) + bfhi(p0) * bfhi(p0); if (c0 < 16) n2 += bflo(p1) * bflo(p1) + bfhi(p1) * bfhi(p1); }
            kn2 = n2;
        } else {
            if (fg == 1 && l < 2) {
                char* op = kvb + KV_V + lt * 1216 + (e & 1) * 608 + l * 304 + 18 * 16;
                st_agent64(op, 0x3f803f80u, 0x3f803f80u); st_agent64(op + 8, 0x3f803f80u, 0x3f803f80u);
            }
            const int hv = (tok >> 2) & 1, jj = 4 * (tok >> 3) + (tok & 3);
            GAS u16* vb = (GAS u16*)(kvb + KV_V + lt * 1216 + (e & 1) * 608 + hv * 304 + jj * 2);
            if (act) {
                __hip_atomic_store(vb + (c0 + 0) * 8, (u16)(p0 & 0xffff), RLX_AGENT);
                __hip_atomic_store(vb + (c0 + 1) * 8, (u16)(p0 >> 16), RLX_AGENT);
                if (c0 < 16) { __hip_atomic_store(vb + (c0 + 2) * 8, (u16)(p1 & 0xffff), RLX_AGENT);
                               __hip_atomic_store(vb + (c0 + 3) * 8, (u16)(p1 >> 16), RLX_AGENT); }
            }
        }
    }
    h_tiles(std::integral_constant<int, 0>{}, std::integral_constant<int, 9>{});
    {
        const int l = fresh_lane(), tok = l & 15, g4 = l >> 4;
        const float qs = 0.23570226039551584f * 1.4426950408889634f;
        const int hh = fg >> 1;
        unsigned p0[4], p1[4];
#pragma unroll
        for (int n = 0; n < 4; ++n) { p0[n] = cvtpk(acc[9][n][0] * qs, acc[9][n][1] * qs); p1[n] = cvtpk(acc[9][n][2] * qs, acc[9][n][3] * qs); }
        if ((fg & 1) == 0) {
            LAS char* qd = L + LDS_Q1 + (hh * 4 + 2 * tg) * 1024 + ((g4 >> 1) * 32 + tok) * 16 + (g4 & 1) * 8;
#pragma unroll
            for (int n = 0; n < 4; ++n) *(LAS u32x2*)(qd + (n >> 1) * 1024 + (n & 1) * 256) = (u32x2){p0[n], p1[n]};
        } else if (g4 == 0) {
            LAS char* qd = L + LDS_Q2 + (hh * 4 + 2 * tg) * 128 + tok * 4;
#pragma unroll
            for (int n = 0; n < 4; ++n) *(LAS unsigned*)(qd + (n >> 1) * 128 + (n & 1) * 64) = p0[n];
        }
    }
    if ((fg & 1) && tg == (s >> 3)) {
        kn2 += xor_lane<16>(kn2); kn2 = wave_max(half_sum(kn2));
        if (fresh_lane() == 0) *(LAS float*)(L + LDS_KM + (fg == 1 ? 0 : 4)) = kn2;
    }
    LBAR();
    const i32x4 srd_kv = make_srd(ws + WS_KV, 4 * KVCH);
    auto piece_kv = [&](int c, int i) {
        const int p = min(w + 8 * i, 36);
        dma16_sc1(srd_kv, vo_x, c * KVCH + p * 1024, lds0 + (c == 0 ? LDS_KV0 : (c - 1) * KVCH) + p * 1024);
    };
#pragma unroll
    for (int m = 0; m < 9; ++m)
#pragma unroll
        for (int n = 0; n < 4; ++n) acc[m][n] = (f32x4){0.f, 0.f, 0.f, 0.f};
    {
        LAS const char* hb = L + LDS_H + 8 * 8192 + 4 * tg * 1024 + l * 16;
        gemm_step<9, true, 0, 1>(acc, ca, bB, bA, L + LDS_D0 + a_off, hb, [](int) {});
        WAIT_BAR(0);
        if (w == 0 && fresh_lane() == 0) {
            st_agent64(flags + FL_KV + wgi * 16, tag, *(LAS const unsigned*)(L + LDS_KM));
            st_agent64(flags + FL_KV + wgi * 16 + 8, tag, *(LAS const unsigned*)(L + LDS_KM + 4));
        }
        gemm_step<9, false, 5, 2>(acc, ca, bA, bB, L + LDS_D1 + a_off, hb - 1 * 8192, [&](int i) { piece2(2, LDS_D0, i); });
        WAIT_BAR(0);
        gemm_step<9, false, 5, 2>(acc, ca, bB, bA, L + LDS_D0 + a_off, hb - 2 * 8192, [&](int i) { piece2(3, LDS_D1, i); });
        WAIT_BAR(0);
        gemm_step<9, false, 5, 2>(acc, ca, bA, bB, L + LDS_D1 + a_off, hb - 3 * 8192, [&](int i) { piece2(4, LDS_D0, i); });
        WAIT_BAR(0);
        gemm_step<9, false, 10, 2>(acc, ca, bB, bA, L + LDS_D0 + a_off, hb - 4 * 8192, [&](int i) { if (i < 5) piece2(5, LDS_D1, i); else piece2(6, LDS_D2, i - 5); });
        WAIT_BAR(5);
        gemm_step<9, false, 5, 1>(acc, ca, bA, bB, L + LDS_D1 + a_off, hb - 5 * 8192, [&](int i) { piece2(7, LDS_D0, i); });
        WAIT_BAR(5);
        gemm_step<9, false, 5, 1>(acc, ca, bB, bA, L + LDS_D2 + a_off, hb - 6 * 8192, [&](int i) { piece2(8, LDS_D1, i); });
        if (w == 0) {
            const int lk = fresh_lane();
            const __amdgpu_buffer_rsrc_t rk = __builtin_amdgcn_make_buffer_rsrc(flags + FL_KV, 0, 4096, 0x00020000);
            float ma, mb;
            for (unsigned spins = 0;; ++spins) {
                bool ok = true; ma = 0.f; mb = 0.f;
#pragma unroll
                for (int i = 0; i < 4; ++i) {
                    const u32x4 v = __builtin_amdgcn_raw_buffer_load_b128(rk, (i * 64 + lk) * 16, 0, 16);
                    ok = ok && v[0] == tag && v[2] == tag; ma = fmaxf(ma, __uint_as_float(v[1])); mb = fmaxf(mb, __uint_as_float(v[3]));
                }
                if (__all(ok)) break;
                if (spins > (1u << 19)) { __hip_atomic_store((GAS unsigned*)(flags + FL_TMO), tag, RLX_AGENT); break; }
                __builtin_amdgcn_s_sleep(1);
            }
            ma = wave_max(ma); mb = wave_max(mb);
            if (lk == 0) *(LAS float*)(L + LDS_KM + 8) = ma + mb;
        }
        WAIT_BAR(5);
        gemm_step<9, false, 5, 1>(acc, ca, bA, bB, L + LDS_D0 + a_off, hb - 7 * 8192, [&](int i) { piece_kv(0, i); });
        WAIT_BAR(5);
        gemm_step<9, false, 0, 1>(acc, ca, bB, bA, L + LDS_D1 + a_off, hb - 8 * 8192, [](int) {});
        gemm_flush<9>(acc, ca, bA);
        SB(); FENCE();
    }
    const int la = fresh_lane();
#define l la
    const int hh = w >> 2, kh = (w >> 1) & 1, cp = w & 1;
#define h5 (l >> 5)
    bf16x8 q1[2]; u32x2 q2[2];
#pragma unroll
    for (int c = 0; c < 2; ++c) {
        q1[c] = lds_b128(L + LDS_Q1 + (hh * 4 + 2 * cp + c) * 1024 + l * 16);
        q2[c][0] = *(LAS const unsigned*)(L + LDS_Q2 + (hh * 4 + 2 * cp + c) * 128 + (l & 31) * 4);
    }
    u32x4 pk[18];
#pragma unroll
    for (int m = 0; m < 9; ++m)
#pragma unroll
        for (int pr = 0; pr < 2; ++pr) {
            u32x4 o; o[0] = cvtpk(acc[m][2 * pr][0], acc[m][2 * pr][1]); o[1] = cvtpk(acc[m][2 * pr][2], acc[m][2 * pr][3]);
            o[2] = cvtpk(acc[m][2 * pr + 1][0], acc[m][2 * pr + 1][1]); o[3] = cvtpk(acc[m][2 * pr + 1][2], acc[m][2 * pr + 1][3]);
            pk[m * 2 + pr] = o;
        }
    {
        const float kmax2 = *(LAS const float*)(L + LDS_KM + 8);
#pragma unroll
        for (int c = 0; c < 2; ++c) {
            const u32x4 a = __builtin_bit_cast(u32x4, q1[c]); const unsigned t = h5 ? 0u : q2[c][0];
            float n2 = bflo(t) * bflo(t) + bfhi(t) * bfhi(t);
#pragma unroll
            for (int e = 0; e < 4; ++e) n2 += bflo(a[e]) * bflo(a[e]) + bfhi(a[e]) * bfhi(a[e]);
            n2 = half_sum(n2);
            const float cb = sqrtf(n2 * kmax2) * 1.001f + 0.01f;
            const float chi = rbf(cb), clo = cb - chi;
            q2[c][0] = t; q2[c][1] = h5 ? 0u : cvtpk(-chi, -clo);
        }
    }
    if (w == 0 && l < 40) *(LAS u32x4*)(L + LDS_ZERO + l * 16) = (u32x4){0u, 0u, 0u, 0u};
    if (w == 0 && l == 40) *(LAS unsigned*)(L + LDS_CTL) = 0u;
    WAIT_BAR(0);
#undef l
    const int lb = fresh_lane();
#define l lb
    const unsigned kone = h5 ? 0u : 0x3f803f80u;
    const bool vrow = (l & 31) < 19;
    const unsigned k1_base = 8 * kh * 1024 + l * 16, k2_base = KV_K2 + 8 * kh * 128 + (l & 31) * 4;
    const unsigned v_base = vrow ? KV_V + 8 * kh * 1216 + h5 * 304 + (l & 31) * 16 : LDS_ZERO, v_mul = vrow ? 1u : 0u;
    const int tgf = s >> 3, cgp = s & 7, nmt = cgp < 4 ? 5 : 4, m0 = cgp < 4 ? 5 * cgp : 20 + 4 * (cgp - 4);
    const int pr = w & 1, qk = w >> 1;
    const i32x4 srd_wo = make_srd(ws + WS_WO + (size_t)cgp * WOIMG, WOIMG);
    auto piece_wo = [&](int p) { dma16_sc1(srd_wo, vo_x, p * 1024, lds0 + wo_lds(p)); };
    const __amdgpu_buffer_rsrc_t rpart = __builtin_amdgcn_make_buffer_rsrc(ws + WS_PART + (size_t)r * 16 * 8 * 18 * 1024, 0, 16 * 8 * 18 * 1024, 0x00020000);
    float own[11];
    u32x4 pl[5][4];

    f32x16 o0, o1, sX, sY; bf16x8 kA, kB, v0A, v1A, v0B, v1B; unsigned k2A, k2B; u32x4 p0, p1; float e3[4];
    unsigned aK, aK2, aV;
#define SLOT(SC, SN, KN, K2N, CN, OP, V1P, OC, V0C) do { float ea[4], eb[4]; \
        SN = MFMA32(KN, q1[CN], (f32x16){}); SB(); \
        p1[2] = cvtpk(e3[0], e3[1]); p1[3] = cvtpk(e3[2], e3[3]); SB(); \
        ea[0] = EXP2(SC[0]); ea[1] = EXP2(SC[1]); ea[2] = EXP2(SC[2]); ea[3] = EXP2(SC[3]); SB(); \
        OP = MFMA32(V1P, __builtin_bit_cast(bf16x8, p1), OP); SB(); \
        p0[0] = cvtpk(ea[0], ea[1]); p0[1] = cvtpk(ea[2], ea[3]); SB(); \
        eb[0] = EXP2(SC[4]); eb[1] = EXP2(SC[5]); eb[2] = EXP2(SC[6]); eb[3] = EXP2(SC[7]); SB(); \
        SN = MFMA32K8(__builtin_bit_cast(s16x4, ((u32x2){K2N, kone})), __builtin_bit_cast(s16x4, q2[CN]), SN); SB(); \
        p0[2] = cvtpk(eb[0], eb[1]); p0[3] = cvtpk(eb[2], eb[3]); SB(); \
        ea[0] = EXP2(SC[8]); ea[1] = EXP2(SC[9]); ea[2] = EXP2(SC[10]); ea[3] = EXP2(SC[11]); SB(); \
        OC = MFMA32(V0C, __builtin_bit_cast(bf16x8, p0), OC); SB(); \
        p1[0] = cvtpk(ea[0], ea[1]); p1[1] = cvtpk(ea[2], ea[3]); SB(); \
        e3[0] = EXP2(SC[12]); e3[1] = EXP2(SC[13]); e3[2] = EXP2(SC[14]); e3[3] = EXP2(SC[15]); SB(); } while (0)
#define PAIR(DK, DK2, DV, H0, H1, H2, H3) do { \
        kB = lds_b128(L + aK + 1024); k2B = *(LAS const unsigned*)(L + aK2 + 128); v0B = lds_b128(L + aV + v_mul * 1216); SB(); \
        H0; SLOT(sX, sY, kA, k2A, 1, o1, v1B, o0, v0A); \
        v1B = lds_b128(L + aV + v_mul * 1216 + 608); SB(); \
        H1; SLOT(sY, sX, kB, k2B, 0, o0, v1A, o1, v0A); \
        aK += (DK); aK2 += (DK2); aV += v_mul * (DV); \
        kA = lds_b128(L + aK); k2A = *(LAS const unsigned*)(L + aK2); v0A = lds_b128(L + aV); SB(); \
        H2; SLOT(sX, sY, kB, k2B, 1, o1, v1A, o0, v0B); \
        v1A = lds_b128(L + aV + 608); SB(); \
        H3; SLOT(sY, sX, kA, k2A, 0, o0, v1B, o1, v0B); } while (0)
#define PAIR_IN(H0, H1, H2, H3) PAIR(2048, 256, 2432, H0, H1, H2, H3)
#define PAIR_JUMP(J, H0, H1, H2, H3) PAIR((J) - 6 * 1024, (J) - 6 * 128, (J) - 6 * 1216, H0, H1, H2, H3)
#define NOP_ ((void)0)
#define ATT_INIT() do { o0 = (f32x16){}; o1 = (f32x16){}; aK = k1_base + LDS_KV0; aK2 = k2_base + LDS_KV0; aV = v_base + v_mul * LDS_KV0; \
        kA = lds_b128(L + aK); k2A = *(LAS const unsigned*)(L + aK2); v0A = lds_b128(L + aV); v1A = lds_b128(L + aV + 608); v1B = v1A; \
        sX = MFMA32(kA, q1[0], (f32x16){}); sX = MFMA32K8(__builtin_bit_cast(s16x4, ((u32x2){k2A, kone})), __builtin_bit_cast(s16x4, q2[0]), sX); \
        p1 = (u32x4){0u, 0u, 0u, 0u}; e3[0] = e3[1] = e3[2] = e3[3] = 0.f; } while (0)
#define ATT_FIN() do { p1[2] = cvtpk(e3[0], e3[1]); p1[3] = cvtpk(e3[2], e3[3]); o1 = MFMA32(v1B, __builtin_bit_cast(bf16x8, p1), o1); } while (0)
    constexpr int J01 = 0 - LDS_KV0, J12 = KVCH;
    {
        const __amdgpu_buffer_rsrc_t rp = __builtin_amdgcn_make_buffer_rsrc(ws + WS_PART + (size_t)(r * 16 + s) * 8 * 18 * 1024, 0, 8 * 18 * 1024, 0x00020000);
#define KV(C, I) do { piece_kv(C, I); SB(); } while (0)
#define ST(I) do { __builtin_amdgcn_raw_buffer_store_b128(pk[I], rp, vo_x, (w * 18 + (I)) * 1024, 16); SB(); } while (0)
#define WO(P0, I) do { piece_wo(min((P0) + w + 8 * (I), (P0) + 36)); SB(); } while (0)
#define PL(T, SP) do { const int m_ = m0 + ((T) < nmt ? (T) : 0); \
            pl[T][SP] = __builtin_amdgcn_raw_buffer_load_b128(rpart, vo_x, ((((4 * qk + (SP)) * 8 + tgf * 4 + (m_ & 3)) * 9 + (m_ >> 2)) * 2 + pr) * 1024, 16); SB(); } while (0)
#define PL2(A) do { PL((A) >> 2, (A) & 3); PL(((A) + 1) >> 2, ((A) + 1) & 3); } while (0)
        ATT_INIT();
        PAIR_IN(KV(1, 0), KV(1, 1), KV(1, 2), KV(1, 3));
        PAIR_IN(KV(1, 4), ST(0), KV(2, 0), ST(1));
        PAIR_IN(KV(2, 1), ST(2), KV(2, 2), ST(3));
        WAIT_BAR(7);
        PAIR_JUMP(J01, KV(2, 3), ST(4), KV(2, 4), ST(5));
        PAIR_IN(KV(3, 0), ST(6), KV(3, 1), ST(7));
        PAIR_IN(KV(3, 2), ST(8), KV(3, 3), ST(9));
        PAIR_IN(KV(3, 4), ST(10), NOP_, ST(11));
        WAIT_BAR(12);
        PAIR_JUMP(J12, WO(0, 0), ST(12), WO(0, 1), ST(13));
        PAIR_IN(WO(0, 2), ST(14), WO(0, 3), ST(15));
        PAIR_IN(WO(0, 4), ST(16), NOP_, ST(17));
        PAIR_IN(NOP_, NOP_, NOP_, NOP_);
        WAIT_BAR(0);
        if (w == 0 && fresh_lane() == 0) __hip_atomic_store((GAS unsigned*)(flags + FL_P + wgi * 4), tag, RLX_AGENT);
        PAIR_JUMP(J12, WO(37, 0), WO(37, 1), WO(37, 2), WO(37, 3));
        const unsigned pflag = __hip_atomic_load((GAS unsigned*)(flags + FL_P + (r * 16 + (l & 15)) * 4), RLX_AGENT);
        PAIR_IN(WO(37, 4), NOP_, NOP_, NOP_);
        LBAR();
        if (!__all(pflag == tag)) wait_tags(flags, FL_P + (r * 16 + (l & 15)) * 4, true, tag);
#define WP(I, A) do { WO(74, I); PL((A) >> 2, (A) & 3); } while (0)
        PAIR_IN(WP(0, 0), WP(1, 1), WP(2, 2), WP(3, 3));
        PAIR_IN(WP(4, 4), PL2(5), PL2(7), PL2(9));
        PAIR(0, 0, 0, PL2(11), PL2(13), PL2(15), PL2(17));
        PL(4, 3);
        ATT_FIN();
    }
#undef l
    const int lc = fresh_lane();
#define l lc
    for (int attempt = 0;; ++attempt) {
        if (attempt == 1) {
            const unsigned kone = h5 ? 0u : 0x3f803f80u;
            const bool vrow = (l & 31) < 19;
            const unsigned k1_base = 8 * kh * 1024 + l * 16, k2_base = KV_K2 + 8 * kh * 128 + (l & 31) * 4;
            const unsigned v_base = vrow ? KV_V + 8 * kh * 1216 + h5 * 304 + (l & 31) * 16 : LDS_ZERO, v_mul = vrow ? 1u : 0u;
            WAIT_BAR(0);
#pragma unroll
            for (int c = 0; c < 4; ++c)
#pragma unroll
                for (int i = 0; i < 5; ++i) piece_kv(c, i);
            if (w == 0 && l < 40) *(LAS u32x4*)(L + LDS_ZERO + l * 16) = (u32x4){0u, 0u, 0u, 0u};
            WAIT_BAR(0);
            float m[2] = {-INFINITY, -INFINITY};
            for (int i = 0; i < 32; ++i) {
                const unsigned to = (i >> 3) == 0 ? LDS_KV0 : ((i >> 3) - 1) * KVCH;
                const bf16x8 k1 = lds_b128(L + k1_base + to + (i & 7) * 1024);
                const u32x2 k2 = {*(LAS const unsigned*)(L + k2_base + to + (i & 7) * 128), kone};
#pragma unroll
                for (int c = 0; c < 2; ++c) {
                    const u32x2 qz = {q2[c][0], 0u};
                    f32x16 sc = MFMA32(k1, q1[c], (f32x16){});
                    sc = MFMA32K8(__builtin_bit_cast(s16x4, k2), __builtin_bit_cast(s16x4, qz), sc);
#pragma unroll
                    for (int e = 0; e < 16; ++e) m[c] = fmaxf(m[c], sc[e]);
                }
            }
            LAS float* mx = (LAS float*)(L + LDS_MX);
#pragma unroll
            for (int c = 0; c < 2; ++c) { m[c] = half_max(m[c]); if (l < 32) mx[(w * 2 + c) * 32 + l] = m[c]; }
            LBAR();
#pragma unroll
            for (int c = 0; c < 2; ++c) {
                const float mm = fmaxf(m[c], mx[((w ^ 2) * 2 + c) * 32 + (l & 31)]);
                const float chi = rbf(mm), clo = mm - chi;
                q2[c][1] = h5 ? 0u : cvtpk(-chi, -clo);
            }
            o0 = (f32x16){}; o1 = (f32x16){};
            for (int i = 0; i < 32; ++i) {
                const unsigned to = (i >> 3) == 0 ? LDS_KV0 : ((i >> 3) - 1) * KVCH;
                const bf16x8 k1 = lds_b128(L + k1_base + to + (i & 7) * 1024);
                const u32x2 k2 = {*(LAS const unsigned*)(L + k2_base + to + (i & 7) * 128), kone};
                const unsigned va = v_base + v_mul * (to + (i & 7) * 1216);
                const bf16x8 v0 = lds_b128(L + va), v1 = lds_b128(L + va + 608);
#pragma unroll
                for (int c = 0; c < 2; ++c) {
                    f32x16 sc = MFMA32(k1, q1[c], (f32x16){});
                    sc = MFMA32K8(__builtin_bit_cast(s16x4, k2), __builtin_bit_cast(s16x4, q2[c]), sc);
                    u32x4 pa, pb;
#pragma unroll
                    for (int e = 0; e < 4; ++e) { pa[e] = cvtpk(EXP2(sc[2 * e]), EXP2(sc[2 * e + 1])); pb[e] = cvtpk(EXP2(sc[8 + 2 * e]), EXP2(sc[9 + 2 * e])); }
                    if (c == 0) { o0 = MFMA32(v0, __builtin_bit_cast(bf16x8, pa), o0); o0 = MFMA32(v1, __builtin_bit_cast(bf16x8, pb), o0); }
                    else { o1 = MFMA32(v0, __builtin_bit_cast(bf16x8, pa), o1); o1 = MFMA32(v1, __builtin_bit_cast(bf16x8, pb), o1); }
                }
            }
        }
        LAS float* red = (LAS float*)(L + LDS_RED);
        LBAR();
        if (attempt == 0) {
            piece_wo(111 + w); if (w == 0) piece_wo(119);
        } else {
#pragma unroll
            for (int q = 0; q < 15; ++q) piece_wo(w * 15 + q);
        }
#pragma unroll
        for (int e = 0; e < 11; ++e) { red[(w * 11 + e) * 64 + l] = kh ? o0[e] : o1[e]; own[e] = kh ? o1[e] : o0[e]; }
        LBAR();
#pragma unroll
        for (int e = 0; e < 11; ++e) own[e] += red[((w ^ 2) * 11 + e) * 64 + l];
        const float lsum = half_lo(own[10]);
        if (!(lsum >= 8.67e-19f && lsum < 3.0e38f) && attempt == 0) *(LAS unsigned*)(L + LDS_CTL) = 1u;
        const float rl = 1.0f / lsum;
        LAS char* ot = L + LDS_OT + (32 * (2 * cp + kh) + (l & 31)) * 80 + 40 * hh;
        *(LAS u32x2*)(ot + 8 * h5) = (u32x2){cvtpk(own[0] * rl, own[1] * rl), cvtpk(own[2] * rl, own[3] * rl)};
        *(LAS u32x2*)(ot + 16 + 8 * h5) = (u32x2){cvtpk(own[4] * rl, own[5] * rl), cvtpk(own[6] * rl, own[7] * rl)};
        if (h5 == 0) *(LAS u32x2*)(ot + 32) = (u32x2){cvtpk(own[8] * rl, own[9] * rl), 0u};
        LBAR();
        const unsigned isbad = *(LAS const unsigned*)(L + LDS_CTL);
        if (!(isbad && attempt == 0)) break;
    }
    {
        const __amdgpu_buffer_rsrc_t rw = __builtin_amdgcn_make_buffer_rsrc(ws + WS_O + (size_t)(r * 16 + s) * 128 * 80, 0, 128 * 80, 0x00020000);
        __builtin_amdgcn_raw_buffer_store_b128(*(LAS const u32x4*)(L + LDS_OT + w * 1024 + l * 16), rw, l * 16, w * 1024, 16);
        if (w < 2) __builtin_amdgcn_raw_buffer_store_b128(*(LAS const u32x4*)(L + LDS_OT + (8 + w) * 1024 + l * 16), rw, l * 16, (8 + w) * 1024, 16);
    }
    {
        const int tok = l & 15, g4 = l >> 4, q4 = (l >> 2) & 3, p4 = l & 3;
        const __amdgpu_buffer_rsrc_t ro = __builtin_amdgcn_make_buffer_rsrc(ws + WS_O + (size_t)r * 16 * 128 * 80, 0, 16 * 128 * 80, 0x00020000);
        u32x4 bf[5][2]; unsigned bo[5];
#pragma unroll
        for (int i = 0; i < 5; ++i) {
            const int kp = 32 * (5 * qk + i) + 8 * g4, sp = kp / 40, d = kp - 40 * sp;
            bo[i] = (sp * 128 + 64 * tgf + 32 * pr + tok) * 80 + d * 2;
#pragma unroll
            for (int nn = 0; nn < 2; ++nn) bf[i][nn] = __builtin_amdgcn_raw_buffer_load_b128(ro, bo[i], nn * 16 * 80, 16);
        }
        SB();
        u32x2 xv[3];
#pragma unroll
        for (int u = 0; u < 3; ++u) {
            const int tt = min(qk + 4 * u, 2 * nmt - 1);
            xv[u] = *(const u32x2*)(x + (size_t)(128 * r + 64 * tgf + 32 * pr + 16 * (tt & 1) + tok) * DM + 16 * (m0 + (tt >> 1)) + 4 * g4);
        }
        f32x4 fa[5][2];
#pragma unroll
        for (int t = 0; t < 5; ++t) {
            fa[t][0] = (f32x4){0.f, 0.f, 0.f, 0.f}; fa[t][1] = (f32x4){0.f, 0.f, 0.f, 0.f};
#pragma unroll
            for (int sp = 0; sp < 4; ++sp) {
                const u32x4 p = pl[t][sp];
                fa[t][0][0] += bflo(p[0]); fa[t][0][1] += bfhi(p[0]); fa[t][0][2] += bflo(p[1]); fa[t][0][3] += bfhi(p[1]);
                fa[t][1][0] += bflo(p[2]); fa[t][1][1] += bfhi(p[2]); fa[t][1][2] += bflo(p[3]); fa[t][1][3] += bfhi(p[3]);
            }
        }
#pragma unroll
        for (int t = 0; t < 5; ++t) asm volatile("" : "+v"(fa[t][0]), "+v"(fa[t][1]));
        WAIT_BAR(0);
        bf16x8 af[5][5];
        {
            const unsigned ao = (4 * g4 + q4) * 64 + 8 * (p4 & 1);
#pragma unroll
            for (int t = 0; t < 5; ++t)
#pragma unroll
                for (int i = 0; i < 5; ++i) {
                    const int p = ((t < nmt ? t : 0) >> 1) * 40 + (5 * qk + i) * 2;
                    const unsigned sw = ao + 16 * ((2 * (t & 1) + (p4 >> 1)) ^ (2 * (g4 & 1)));
                    const s16x4 lo = lds_tr(L + wo_lds(p) + sw), hi = lds_tr(L + wo_lds(p + 1) + sw);
                    af[t][i] = __builtin_shufflevector(lo, hi, 0, 1, 2, 3, 4, 5, 6, 7);
                }
        }
        LBAR();
        unsigned pend = 0x3ffu;
        for (unsigned spins = 0;; ++spins) {
#pragma unroll
            for (int i = 0; i < 5; ++i)
#pragma unroll
                for (int nn = 0; nn < 2; ++nn)
                    if ((pend & (1u << (2 * i + nn))) && __all(bf[i][nn][0] != O_EMPTY && bf[i][nn][1] != O_EMPTY && bf[i][nn][2] != O_EMPTY && bf[i][nn][3] != O_EMPTY)) pend &= ~(1u << (2 * i + nn));
            if (pend == 0u) break;
            if (spins > (1u << 17)) { __hip_atomic_store((GAS unsigned*)(flags + FL_TMO), tag, RLX_AGENT); break; }
            __builtin_amdgcn_s_sleep(8);
#pragma unroll
            for (int i = 0; i < 5; ++i)
#pragma unroll
                for (int nn = 0; nn < 2; ++nn)
                    if (pend & (1u << (2 * i + nn))) bf[i][nn] = __builtin_amdgcn_raw_buffer_load_b128(ro, bo[i], nn * 16 * 80, 16);
        }
#pragma unroll
        for (int i = 0; i < 5; ++i)
#pragma unroll
            for (int t = 0; t < 5; ++t) {
                fa[t][0] = MFMA16(af[t][i], __builtin_bit_cast(bf16x8, bf[i][0]), fa[t][0]); fa[t][1] = MFMA16(af[t][i], __builtin_bit_cast(bf16x8, bf[i][1]), fa[t][1]);
            }
        LAS f32x4* ex = (LAS f32x4*)L;
#pragma unroll
        for (int t = 0; t < 5; ++t) { ex[(w * 10 + 2 * t) * 64 + l] = fa[t][0]; ex[(w * 10 + 2 * t + 1) * 64 + l] = fa[t][1]; }
        LBAR();
#pragma unroll
        for (int u = 0; u < 3; ++u) {
            const int tt = qk + 4 * u;
            if (tt < 2 * nmt) {
                f32x4 v = ex[((0 * 2 + pr) * 10 + tt) * 64 + l];
#pragma unroll
                for (int k = 1; k < 4; ++k) v += ex[((k * 2 + pr) * 10 + tt) * 64 + l];
                const size_t off = (size_t)(128 * r + 64 * tgf + 32 * pr + 16 * (tt & 1) + tok) * DM + 16 * (m0 + (tt >> 1)) + 4 * g4;
                *(u32x2*)(out + off) = (u32x2){cvtpk(v[0] + bflo(xv[u][0]), v[1] + bfhi(xv[u][0])), cvtpk(v[2] + bflo(xv[u][1]), v[3] + bfhi(xv[u][1]))};
            }
        }
    }
#undef l
#undef h5
    asm volatile("fused_block_code_end:");
}

extern "C" void kernel_launch(void* const* d_in, const int* in_sizes, int n_in, void* d_out, int out_size, void* d_ws, size_t ws_size, hipStream_t stream) {
    const u16* x = (const u16*)d_in[0]; const u16* wq = (const u16*)d_in[1]; const u16* wk = (const u16*)d_in[2]; const u16* wv = (const u16*)d_in[3];
    const u16* wo = (const u16*)d_in[4]; const u16* wg = (const u16*)d_in[6]; const u16* wu = (const u16*)d_in[7]; const u16* wd = (const u16*)d_in[8];
    fused_block<<<256, 512, 0, stream>>>(x, wq, wk, wv, wo, wg, wu, wd, (char*)d_ws, (u16*)d_out);
}
```

```cpp
#include <hip/hip_runtime.h>
#include <hip/hip_bf16.h>
#include <stdint.h>
#include <type_traits>

typedef unsigned short u16;
typedef short bf16x8 __attribute__((ext_vector_type(8)));
typedef short s16x4 __attribute__((ext_vector_type(4)));
typedef float f32x4 __attribute__((ext_vector_type(4)));
typedef float f32x16 __attribute__((ext_vector_type(16)));
typedef int i32x4 __attribute__((ext_vector_type(4)));
typedef unsigned u32x4 __attribute__((ext_vector_type(4)));
typedef unsigned u32x2 __attribute__((ext_vector_type(2)));
typedef __bf16 bf16x2_t __attribute__((ext_vector_type(2)));
typedef float f32x2 __attribute__((ext_vector_type(2)));
typedef short v4i16_t __attribute__((ext_vector_type(4)));
extern "C" __device__ unsigned long long amd_dispatch_id() __asm("llvm.amdgcn.dispatch.id");
#define LAS __attribute__((address_space(3)))
#define GAS __attribute__((address_space(1)))
#define DI __device__ __forceinline__

constexpr int SEQ = 2048, DM = 576, NH = 32, DH = 18, FF = 4608;

constexpr int KVCH = 37888;
constexpr int KV_V = 16384, KV_K2 = 35840;
constexpr int WOIMG = 120 * 1024;
constexpr size_t WS_FLAGS = 0;
constexpr size_t WS_XN    = 16384;
constexpr size_t WS_WQX   = WS_XN + (size_t)SEQ * DM * 2;
constexpr size_t WS_WO    = WS_WQX + (size_t)DM * 1024 * 2;
constexpr size_t WS_KV    = WS_WO + (size_t)8 * WOIMG;
constexpr size_t WS_O     = WS_KV + 4 * KVCH;
constexpr size_t WS_PART  = WS_O + (size_t)256 * 128 * 80;
constexpr size_t WS_END   = WS_PART + (size_t)256 * 8 * 18 * 1024;
constexpr int FL_PRO = 0, FL_P = 1024, FL_KV = 2048, FL_TMO = 8192;
constexpr unsigned O_EMPTY = 0xffffffffu;

DI float bflo(unsigned v) { return __uint_as_float(v << 16); }
DI float bfhi(unsigned v) { return __uint_as_float(v & 0xffff0000u); }
DI unsigned cvtpk(float lo, float hi) { return __builtin_bit_cast(unsigned, __builtin_convertvector((f32x2){lo, hi}, bf16x2_t)); }
DI float rbf(float f) { return bflo(cvtpk(f, f)); }
DI i32x4 make_srd(const void* p, unsigned bytes) {
    unsigned long long a = (unsigned long long)p; i32x4 s;
    s.x = __builtin_amdgcn_readfirstlane((unsigned)a); s.y = __builtin_amdgcn_readfirstlane((unsigned)(a >> 32) & 0xffffu);
    s.z = (int)bytes; s.w = 0x00020000; return s;
}
DI void dma16(i32x4 srd, unsigned voff, unsigned soff, unsigned lds_addr) {
    unsigned keep;
    asm volatile("s_mov_b32 %0, m0\n\ts_mov_b32 m0, %4\n\ts_nop 0\n\tbuffer_load_dwordx4 %1, %2, %3 offen lds\n\ts_mov_b32 m0, %0"
                 : "=&s"(keep) : "v"(voff), "s"(srd), "s"(soff), "s"(lds_addr) : "memory");
}
DI void dma16_sc1(i32x4 srd, unsigned voff, unsigned soff, unsigned lds_addr) {
    unsigned keep;
    asm volatile("s_mov_b32 %0, m0\n\ts_mov_b32 m0, %4\n\ts_nop 0\n\tbuffer_load_dwordx4 %1, %2, %3 offen sc1 lds\n\ts_mov_b32 m0, %0"
                 : "=&s"(keep) : "v"(voff), "s"(srd), "s"(soff), "s"(lds_addr) : "memory");
}
DI void dma4(i32x4 srd, unsigned voff, unsigned soff, unsigned lds_addr) {
    unsigned keep;
    asm volatile("s_mov_b32 %0, m0\n\ts_mov_b32 m0, %4\n\ts_nop 0\n\tbuffer_load_dword %1, %2, %3 offen lds\n\ts_mov_b32 m0, %0"
                 : "=&s"(keep) : "v"(voff), "s"(srd), "s"(soff), "s"(lds_addr) : "memory");
}
#define WAIT_BAR(N) asm volatile("s_waitcnt vmcnt(" #N ") lgkmcnt(0)\n\ts_barrier" ::: "memory")
#define LBAR() asm volatile("s_waitcnt lgkmcnt(0)\n\ts_barrier" ::: "memory")
#define SB() __builtin_amdgcn_sched_barrier(0)
#define FENCE() asm volatile("" ::: "memory")
#define PIN4(a) asm volatile("" : "+v"((a)[0]), "+v"((a)[1]), "+v"((a)[2]), "+v"((a)[3]))
#define MFMA16(a, b, c) __builtin_amdgcn_mfma_f32_16x16x32_bf16(a, b, c, 0, 0, 0)
#define MFMA32(a, b, c) __builtin_amdgcn_mfma_f32_32x32x16_bf16(a, b, c, 0, 0, 0)
#define MFMA32K8(a, b, c) __builtin_amdgcn_mfma_f32_32x32x8bf16_1k(a, b, c, 0, 0, 0)
#define EXP2(x) __builtin_amdgcn_exp2f(x)
DI s16x4 lds_tr(LAS const char* p) { return __builtin_bit_cast(s16x4, __builtin_amdgcn_ds_read_tr16_b64_v4i16((LAS v4i16_t*)p)); }
DI bf16x8 lds_b128(LAS const char* p) { return *(LAS const bf16x8*)p; }
DI bf16x8 lds_afrag(LAS const char* p) { const s16x4 lo = lds_tr(p), hi = lds_tr(p + 2048); return __builtin_shufflevector(lo, hi, 0, 1, 2, 3, 4, 5, 6, 7); }
#define RLX_AGENT __ATOMIC_RELAXED, __HIP_MEMORY_SCOPE_AGENT
template <int X> DI float xor_lane(float v) { return __int_as_float(__builtin_amdgcn_ds_swizzle(__float_as_int(v), (X << 10) | 0x1f)); }
DI float half_sum(float v) { auto r = __builtin_amdgcn_permlane32_swap(__float_as_uint(v), __float_as_uint(v), false, false); return __uint_as_float(r[0]) + __uint_as_float(r[1]); }
DI float half_max(float v) { auto r = __builtin_amdgcn_permlane32_swap(__float_as_uint(v), __float_as_uint(v), false, false); return fmaxf(__uint_as_float(r[0]), __uint_as_float(r[1])); }
DI float half_lo(float v) { auto r = __builtin_amdgcn_permlane32_swap(__float_as_uint(v), __float_as_uint(v), false, false); return __uint_as_float(r[0]); }
template <int CTRL, int ROWS> DI float dpp_add(float v) { return v + __int_as_float(__builtin_amdgcn_update_dpp(0, __float_as_int(v), CTRL, ROWS, 0xf, true)); }
DI float wave_sum_dpp(float v) {
    v = dpp_add<0x111, 0xf>(v); v = dpp_add<0x112, 0xf>(v); v = dpp_add<0x114, 0xf>(v); v = dpp_add<0x118, 0xf>(v);
    v = dpp_add<0x142, 0xa>(v); v = dpp_add<0x143, 0xc>(v);
    return __int_as_float(__builtin_amdgcn_readlane(__float_as_int(v), 63));
}
DI float wave_sum(float v) { v += xor_lane<1>(v); v += xor_lane<2>(v); v += xor_lane<4>(v); v += xor_lane<8>(v); v += xor_lane<16>(v); return half_sum(v); }
DI float wave_max(float v) { v = fmaxf(v, xor_lane<1>(v)); v = fmaxf(v, xor_lane<2>(v)); v = fmaxf(v, xor_lane<4>(v)); v = fmaxf(v, xor_lane<8>(v)); v = fmaxf(v, xor_lane<16>(v)); return half_max(v); }
DI int fresh_lane() { int t; asm volatile("v_mbcnt_lo_u32_b32 %0, -1, 0\n\tv_mbcnt_hi_u32_b32 %0, -1, %0" : "=v"(t)); return t; }
DI void st_agent64(void* p, unsigned lo, unsigned hi) { __hip_atomic_store((GAS unsigned long long*)p, ((unsigned long long)hi << 32) | lo, RLX_AGENT); }
DI void wait_tags(const char* flags, unsigned off, bool use, unsigned tag) {
    for (unsigned spins = 0;; ++spins) {
        const unsigned v = use ? __hip_atomic_load((GAS unsigned*)(flags + off), RLX_AGENT) : tag;
        if (__all(v == tag)) break;
        if (spins > (1u << 19)) { __hip_atomic_store((GAS unsigned*)(flags + FL_TMO), tag, RLX_AGENT); break; }
        __builtin_amdgcn_s_sleep(1);
    }
    FENCE();
}

#define DMA345 dma16_sc1
constexpr int ST1 = 49152;
constexpr int LDS_D0 = 0, LDS_D1 = 49152, LDS_H = 86016, LDS_Q1 = 36864, LDS_Q2 = 45056;
constexpr int LDS_PF = 147456;
constexpr int LDS_KM = 46080;
constexpr int LDS_D2 = 126976;
constexpr int LDS_KV0 = 125952;
constexpr int LDS_OT = 113664;
constexpr int LDS_MX = LDS_OT;
constexpr int LDS_ZERO = LDS_OT + 10240;
constexpr int LDS_CTL = LDS_ZERO + 640;
constexpr int LDS_RED = 2 * KVCH + 9 * 1024;
constexpr int LDS_BYTES = 163840;
DI unsigned wo_lds(int p) { return p < 37 ? LDS_KV0 + p * 1024 : p < 74 ? (p - 37) * 1024 : p < 111 ? KVCH + (p - 74) * 1024 : 2 * KVCH + (p - 111) * 1024; }

template <int NM, bool FIRST, int ND, int DPG, int AD = 3, bool PIN = false, class Dma>
DI void gemm_step(f32x4 (&acc)[10][4], bf16x8 (&ca)[2], bf16x8 (&cb)[4], bf16x8 (&nb)[4], LAS const char* ap, LAS const char* bp, const Dma& dma) {
    bf16x8 a[10];
#pragma unroll
    for (int n = 0; n < 4; ++n) nb[n] = lds_b128(bp + n * 1024);
    a[0] = lds_afrag(ap); a[1] = lds_afrag(ap + 4096); if (AD > 2) a[2] = lds_afrag(ap + 2 * 4096);
    SB();
    int d = 0;
    if (!FIRST) {
#pragma unroll
        for (int k = 0; k < DPG; ++k) if (d < ND) dma(d++);
#pragma unroll
        for (int n = 0; n < 4; ++n) acc[NM - 2][n] = MFMA16(ca[0], cb[n], acc[NM - 2][n]);
        if (PIN) PIN4(acc[NM - 2]); SB();
#pragma unroll
        for (int k = 0; k < DPG; ++k) if (d < ND) dma(d++);
#pragma unroll
        for (int n = 0; n < 4; ++n) acc[NM - 1][n] = MFMA16(ca[1], cb[n], acc[NM - 1][n]);
        if (PIN) PIN4(acc[NM - 1]); SB();
    }
#pragma unroll
    for (int m = 0; m < NM - 2; ++m) {
        if (m + AD < NM) a[m + AD] = lds_afrag(ap + (m + AD) * 4096);
#pragma unroll
        for (int k = 0; k < DPG; ++k) if (d < ND) dma(d++);
#pragma unroll
        for (int n = 0; n < 4; ++n) acc[m][n] = MFMA16(a[m], nb[n], acc[m][n]);
        if (PIN) PIN4(acc[m]); SB();
    }
    ca[0] = a[NM - 2]; ca[1] = a[NM - 1];
}
template <int NM>
DI void gemm_flush(f32x4 (&acc)[10][4], const bf16x8 (&ca)[2], const bf16x8 (&cb)[4]) {
#pragma unroll
    for (int n = 0; n < 4; ++n) acc[NM - 2][n] = MFMA16(ca[0], cb[n], acc[NM - 2][n]);
#pragma unroll
    for (int n = 0; n < 4; ++n) acc[NM - 1][n] = MFMA16(ca[1], cb[n], acc[NM - 1][n]);
}

__global__ __launch_bounds__(512, 2) void fused_block(const u16* __restrict__ x, const u16* __restrict__ wq, const u16* __restrict__ wk, const u16* __restrict__ wv,
                                                       const u16* __restrict__ wo, const u16* __restrict__ wg, const u16* __restrict__ wu, const u16* __restrict__ wd,
                                                       char* __restrict__ ws, u16* __restrict__ out) {
    __shared__ __attribute__((aligned(1024))) char lds[LDS_BYTES];
    const int tid = threadIdx.x, l = tid & 63;
    const int w = __builtin_amdgcn_readfirstlane(tid >> 6);
    const int b = blockIdx.x, s = 2 * (b & 7) + ((b >> 3) & 1), r = b >> 4, wgi = r * 16 + s;
    const int tg = w >> 2, fg = w & 3;
    constexpr unsigned lds0 = 0;
    LAS char* L = (LAS char*)lds;
    char* flags = ws + WS_FLAGS;
    const unsigned tag = (((unsigned)amd_dispatch_id() + 1u) * 0x9E3779B1u) ^ ((unsigned)((unsigned long long)__builtin_amdgcn_dispatch_ptr() >> 6) * 0x85EBCA77u) ^ 0x5bd1e995u;

    const int dmat = w & 1, dR = (w >> 1) & 1, dcg0 = 5 * (w >> 2);
    const int di = l >> 2, dsig = l & 3;
    const int dmi = dsig ^ (2 * dmat + ((di >> 2) & 1)), dkrow = 8 * (di >> 2) + (di & 3);
    const i32x4 srd_w = make_srd(dmat ? wu : wg, DM * FF * 2);
    const i32x4 srd_q = make_srd(ws + WS_WQX, DM * 1024 * 2);
    const i32x4 srd_x = make_srd(ws + WS_XN, SEQ * DM * 2);
    const unsigned vo_w = dkrow * (FF * 2) + 16 * dmi, vo_q = dkrow * 2048 + 16 * dmi, vo_x = l * 16;
    const unsigned dlds = lds0 + dR * 2048 + dmat * 1024;
    auto pieceW = [&](int j, unsigned slot, int cg) { dma16(srd_w, vo_w, (32 * j + 4 * dR) * (FF * 2) + 288 * s * 2 + cg * 64, dlds + slot + cg * 4096); };
    auto pieceQ = [&](int j, unsigned slot) { dma16_sc1(srd_q, vo_q, (32 * j + 4 * dR) * 2048 + (64 * s + 32 * dmat) * 2, dlds + slot + 9 * 4096); };
    auto pieceX = [&](int j, unsigned slot, int n) { dma16_sc1(srd_x, vo_x, ((r * 18 + j) * 8 + n) * 1024, lds0 + slot + 40960 + n * 1024); };
    auto piece1k = [&](int j, unsigned slot, int i, int kind) {
        if (i < 3) { if (kind & 1) pieceW(j, slot, dcg0 + i); }
        else if (i == 3) { if (w == 3) { if (kind & 2) pieceQ(j, slot); } else if (kind & 1) pieceW(j, slot, dcg0 + 3); }
        else if (i == 4) {
            if (w < 3) { if (kind & 1) pieceW(j, slot, 4); } else if (w == 3) { if (kind & 2) pieceX(j, slot, 7); }
            else if (w < 7) { if (kind & 2) pieceQ(j, slot); } else if (kind & 1) pieceW(j, slot, 3);
        } else { if (w == 7) { if (kind & 1) pieceW(j, slot, 4); } else if (kind & 2) pieceX(j, slot, w); }
    };
    auto piece2 = [&](int j, unsigned slot, int i) {
        const int ii = (w >= 4 && i == 4) ? 3 : i;
        const unsigned vo_d = ((vo_w & ~63u) >> 3) + ((vo_w & 48u) << 1);
        dma16(make_srd(wd, FF * DM * 2), vo_d, (288 * s + 32 * j + 4 * dR) * (DM * 2) + 16 * dmat + (dcg0 + ii) * 128, dlds + slot + (dcg0 + ii) * 4096);
    };

    const u32x4* xs = (const u32x4*)(x + (size_t)(128 * r + 8 * s + w) * DM);
    const u32x4 v0 = xs[l];
    u32x4 v1 = xs[64 + (l & 7)];
    FENCE();
    if (w == 7) {
        unsigned long long pc; unsigned len;
        asm volatile("s_getpc_b64 %0\n\tcode_here_%=:\n\ts_mov_b32 %1, fused_block_code_end-code_here_%=" : "=s"(pc), "=s"(len));
        i32x4 sc; sc.x = (int)(unsigned)pc; sc.y = (int)((unsigned)(pc >> 32) & 0xffffu); sc.z = (int)len; sc.w = 0x00020000;
        dma4(sc, l < 20 ? (unsigned)(((b >> 3) * 20 + l) * 128) : 0x80000000u, 0, lds0 + LDS_PF);
    }
    {
        unsigned qv[4];
        {
            const int tq = min(tid, 287), k = 36 * r + (tq >> 3), mat = (tq >> 2) & 1, t = tq & 3;
            const bool odd = t & 1;
            const __amdgpu_buffer_rsrc_t rq_ = __builtin_amdgcn_make_buffer_rsrc((void*)wq, 0, DM * DM * 2, 0x00020000);
            const __amdgpu_buffer_rsrc_t rc_ = __builtin_amdgcn_make_buffer_rsrc((void*)((s & 1) ? wv : wk), 0, DM * DH * 2, 0x00020000);
            const u32x4 a = __builtin_amdgcn_raw_buffer_load_b128(rq_, (k * DM + (2 * s + (t >> 1)) * DH + (odd ? 16 : 8 * mat)) * 2, 0, 0);
            const u32x4 c = __builtin_amdgcn_raw_buffer_load_b128(rc_, (k * DH + (t == 1 ? 4 * mat : 12 + 4 * mat)) * 2, 0, 0);
            qv[0] = (!odd || mat == 0) ? a[0] : c[0];
            qv[1] = !odd ? a[1] : (mat == 1 && t == 1) ? c[1] : 0u;
            qv[2] = !odd ? a[2] : mat == 0 ? c[0] : t == 1 ? c[2] : 0u;
            qv[3] = !odd ? a[3] : mat == 0 ? c[1] : t == 1 ? c[3] : 0u;
        }
        FENCE();
        if (l >= 8) v1 = (u32x4){0u, 0u, 0u, 0u};
        float sm = 0.f;
#pragma unroll
        for (int e = 0; e < 4; ++e) sm += bflo(v0[e]) + bfhi(v0[e]) + bflo(v1[e]) + bfhi(v1[e]);
        const float mean = wave_sum_dpp(sm) * (1.0f / DM);
        float q = 0.f, q1 = 0.f;
#pragma unroll
        for (int e = 0; e < 4; ++e) { const float a = bflo(v0[e]) - mean, c = bfhi(v0[e]) - mean; q += a * a + c * c; }
#pragma unroll
        for (int e = 0; e < 4; ++e) { const float a = bflo(v1[e]) - mean, c = bfhi(v1[e]) - mean; q1 += a * a + c * c; }
        if (l < 8) q += q1;
        const float m = rbf(mean), var = rbf(wave_sum_dpp(q) * (1.0f / DM));
        const float rs = 1.0f / rbf(sqrtf(rbf(var + 1e-5f)));
        LAS char* xst = L + 2 * ST1;
        {
            u32x4 o;
#pragma unroll
            for (int e = 0; e < 4; ++e) o[e] = cvtpk(rbf(bflo(v0[e]) - m) * rs, rbf(bfhi(v0[e]) - m) * rs);
            *(LAS u32x4*)(xst + l * 128 + w * 16) = o;
#pragma unroll
            for (int e = 0; e < 4; ++e) o[e] = cvtpk(rbf(bflo(v1[e]) - m) * rs, rbf(bfhi(v1[e]) - m) * rs);
            if (l < 8) *(LAS u32x4*)(xst + (64 + l) * 128 + w * 16) = o;
        }
        LBAR();
        if (w < 7) {
            const __amdgpu_buffer_rsrc_t rx = __builtin_amdgcn_make_buffer_rsrc(ws + WS_XN, 0, SEQ * DM * 2, 0x00020000);
#pragma unroll
            for (int u = 0; u < 2; ++u) {
                const int i = tid + 448 * u, c = i >> 3, t = i & 7;
                const u32x4 v = *(LAS const u32x4*)(xst + min(i, 575) * 16);
                __builtin_amdgcn_raw_buffer_store_b128(v, rx, i < 576 ? (unsigned)(((r * 18 + (c >> 2)) * 8 + (s >> 1)) * 1024 + ((c & 3) * 16 + 8 * (s & 1) + t) * 16) : 0xfffffff0u, 0, 16);
            }
            {
                const u32x4 o = (u32x4){qv[0], qv[1], qv[2], qv[3]};
                const __amdgpu_buffer_rsrc_t rq = __builtin_amdgcn_make_buffer_rsrc(ws + WS_WQX, 0, DM * 1024 * 2, 0x00020000);
                __builtin_amdgcn_raw_buffer_store_b128(o, rq, tid < 288 ? (unsigned)(((36 * r + (tid >> 3)) * 128 + s * 8 + (tid & 7)) * 16) : 0xfffffff0u, 0, 16);
            }
            WAIT_BAR(0);
        } else {
            const unsigned o = ((l & 7) < 5 && l < 32) ? (unsigned)((4 * r + (l >> 3)) * (FF * 2) + ((288 * s * 2) & ~127) + (l & 7) * 128) : 0x80000000u;
            dma4(make_srd(wg, DM * FF * 2), o, 0, lds0 + LDS_PF); dma4(srd_w, o, 0, lds0 + LDS_PF);
            LBAR();
        }
        if (tid == 0) __hip_atomic_store((GAS unsigned*)(flags + FL_PRO + wgi * 4), tag, RLX_AGENT);
#pragma unroll
        for (int i = 0; i < 6; ++i) piece1k(0, 0, i, 1);
#pragma unroll
        for (int i = 0; i < 6; ++i) piece1k(1, ST1, i, 1);
        if (w == 0) wait_tags(flags, FL_PRO + (l < 16 ? (r * 16 + l) * 4 : ((l - 16) * 16 + s) * 4), l < 32, tag);
        asm volatile("s_barrier" ::: "memory");
#pragma unroll
        for (int i = 3; i < 6; ++i) piece1k(0, 0, i, 2);
#pragma unroll
        for (int i = 3; i < 6; ++i) piece1k(1, ST1, i, 2);
        if (w == 7) {
            const unsigned o = (l & 7) < 5 ? (unsigned)(((l >> 3) * 16 + r) * (FF * 2) + ((288 * s * 2) & ~127) + (l & 7) * 128) : 0x80000000u;
#pragma unroll
            for (int g = 0; g < 2; ++g) { dma4(make_srd(wg, DM * FF * 2), o, g * 128 * (FF * 2), lds0 + LDS_PF); dma4(srd_w, o, g * 128 * (FF * 2), lds0 + LDS_PF); }
        }
    }
    const int lg = fresh_lane();
    const unsigned a_off = ((lg & 3) >> 1) * 1024 + 16 * (4 * (4 * (lg >> 4) + ((lg >> 2) & 3)) + (fg ^ (2 * ((lg & 3) >> 1) + ((lg >> 4) & 1)))) + 8 * (lg & 1);

    f32x4 acc[10][4];
#pragma unroll
    for (int m = 0; m < 10; ++m)
#pragma unroll
        for (int n = 0; n < 4; ++n) acc[m][n] = (f32x4){0.f, 0.f, 0.f, 0.f};
    bf16x8 ca[2], bA[4], bB[4];
    {
        const unsigned bo = 40960 + tg * 4096 + lg * 16;
        if (w == 7) { WAIT_BAR(4); } else { WAIT_BAR(0); }
        const bool q3 = w == 3, w4 = w < 3 || w == 7, x4 = w == 3, w5 = w == 7;
        const i32x4 srd3 = q3 ? srd_q : srd_w, srd4 = w4 ? srd_w : x4 ? srd_x : srd_q, srd5 = w5 ? srd_w : srd_x;
        const unsigned vo3 = q3 ? vo_q : vo_w, vo4 = w4 ? vo_w : x4 ? vo_x : vo_q, vo5 = w5 ? vo_w : vo_x;
        const unsigned soW = 4 * dR * (FF * 2) + 288 * s * 2, soQ = 4 * dR * 2048 + (64 * s + 32 * dmat) * 2, soX = r * 18 * 8192;
        const unsigned stW = 32 * FF * 2, stQ = 32 * 2048, stX = 8192;
        const unsigned so3 = q3 ? soQ : soW + (dcg0 + 3) * 64, st3 = q3 ? stQ : stW, lo3 = q3 ? dlds + 9 * 4096 : dlds + (dcg0 + 3) * 4096;
        const int cg4 = w < 3 ? 4 : 3;
        const unsigned so4 = w4 ? soW + cg4 * 64 : x4 ? soX + 7 * 1024 : soQ, st4 = w4 ? stW : x4 ? stX : stQ, lo4 = w4 ? dlds + cg4 * 4096 : x4 ? lds0 + 40960 + 7 * 1024 : dlds + 9 * 4096;
        const unsigned so5 = w5 ? soW + 4 * 64 : soX + w * 1024, st5 = w5 ? stW : stX, lo5 = w5 ? dlds + 4 * 4096 : lds0 + 40960 + w * 1024;
        unsigned vo_pf;
        { const int lp = fresh_lane(); vo_pf = (lp & 7) < 5 ? (unsigned)(((lp >> 3) * 16 + r) * (FF * 2) + ((288 * s * 2) & ~127) + (lp & 7) * 128) : 0x80000000u; }
        auto stage1 = [&](int j, unsigned slot, int i) {
            if (i < 3) dma16(srd_w, vo_w, soW + (dcg0 + i) * 64 + j * stW, dlds + (dcg0 + i) * 4096 + slot);
            else if (i == 3) DMA345(srd3, vo3, so3 + j * st3, lo3 + slot);
            else if (i == 4) DMA345(srd4, vo4, so4 + j * st4, lo4 + slot);
            else if (i == 5) DMA345(srd5, vo5, so5 + j * st5, lo5 + slot);
            else if (w >= 6) {
                const int t = j - 2;
                if (t == 6) { const int lp = fresh_lane(); vo_pf = (w == 7 && (lp & 15) < 9) ? (unsigned)((288 * s + (lp >> 4) * 16 + r) * (DM * 2) + (lp & 15) * 128) : 0x80000000u; }
                const unsigned so = t < 6 ? ((t & 1) ? 0x70000000u : (unsigned)(2 + (t >> 1)) * 128 * (FF * 2)) : t < 11 ? (unsigned)(t - 6) * 64 * (DM * 2) : 0x70000000u;
                dma4(t < 6 ? srd_w : make_srd(wd, FF * DM * 2), vo_pf, so, lds0 + LDS_PF);
            }
        };
        gemm_step<10, true, 7, 1>(acc, ca, bB, bA, L + a_off, L + bo, [&](int i) { stage1(2, 2 * ST1, i); });
        unsigned slot = ST1, slot2 = 0;
        if (w >= 6) { WAIT_BAR(7); } else { WAIT_BAR(6); }
        gemm_step<10, false, 7, 1>(acc, ca, bA, bB, L + slot + a_off, L + slot + bo, [&](int i) { stage1(3, slot2, i); });
        slot = 2 * ST1; slot2 = ST1;
#pragma unroll 1
        for (int j = 2; j < 16; j += 2) {
            if (w >= 6) { WAIT_BAR(8); } else { WAIT_BAR(6); }
            gemm_step<10, false, 7, 1>(acc, ca, bB, bA, L + slot + a_off, L + slot + bo, [&](int i) { stage1(j + 2, slot2, i); });
            slot = (slot == 2 * ST1) ? 0 : slot + ST1; slot2 = (slot2 == 2 * ST1) ? 0 : slot2 + ST1;
            if (w >= 6) { WAIT_BAR(8); } else { WAIT_BAR(6); }
            gemm_step<10, false, 7, 1>(acc, ca, bA, bB, L + slot + a_off, L + slot + bo, [&](int i) { stage1(j + 3, slot2, i); });
            slot = (slot == 2 * ST1) ? 0 : slot + ST1; slot2 = (slot2 == 2 * ST1) ? 0 : slot2 + ST1;
        }
#pragma unroll
        for (int n = 0; n < 4; ++n) bA[n] = bB[n];
#pragma unroll 1
        for (int j = 16; j < 18; ++j) {
            if (j == 16 && w >= 6) { WAIT_BAR(8); } else if (j == 16) { WAIT_BAR(6); } else { WAIT_BAR(5); }
            gemm_step<10, false, 5, 1, 3, true>(acc, ca, bA, bB, L + slot + a_off, L + slot + bo, [&](int i) { piece2(j - 16, j == 16 ? LDS_D0 : LDS_D1, i); });
#pragma unroll
            for (int n = 0; n < 4; ++n) bA[n] = bB[n];
            slot += ST1;
        }
    }
    WAIT_BAR(0);
    gemm_flush<10>(acc, ca, bA);
    u32x4 wov; bool wo_act; unsigned wo_off;
    {
        const int l = fresh_lane(), tw = w * 64 + l;
        const int cgp = s & 7, wp = 4 * (r * 2 + (s >> 3)) + ((min(max(tw, 192), 447) - 192) >> 6);
        wo_act = tw >= 192 && tw < 448 && wp < 120;
        const int m0 = cgp < 4 ? 5 * cgp : 20 + 4 * (cgp - 4), wpc = min(wp, 119);
        const int pp = wpc / 40, js = (wpc % 40) >> 1, R = wpc & 1, i = l >> 2, ch = (l & 3) ^ (2 * ((i >> 2) & 1));
        const int kp = 32 * js + 8 * (i >> 2) + (i & 3) + 4 * R, sp = kp / 40, d = kp - 40 * sp, hd = d >= 20 ? 1 : 0, dd = d - 20 * hd;
        const int row = 36 * sp + 18 * hd + (dd < 18 ? dd : 17), col = min(16 * (m0 + 2 * pp) + 8 * ch, DM - 8);
        wov = *(const u32x4*)(wo + (size_t)row * DM + col);
        wo_off = wo_act ? (unsigned)(cgp * WOIMG + wp * 1024 + l * 16) : 0xfffffff0u;
    }
    auto h_tiles = [&](auto CG0, auto CG1) {
        const int l = fresh_lane(), tok = l & 15, g4 = l >> 4;
#pragma unroll
        for (int cg = CG0; cg < CG1; ++cg)
#pragma unroll
            for (int pr = 0; pr < 2; ++pr) {
                float h[4];
#pragma unroll
                for (int e = 0; e < 4; ++e) {
                    auto sw = __builtin_amdgcn_permlane32_swap(__float_as_uint(acc[cg][2 * pr][e]), __float_as_uint(acc[cg][2 * pr + 1][e]), false, false);
                    h[e] = __int_as_float(max((int)sw[0], 0)) * __uint_as_float(sw[1]);
                }
                const int n = 4 * tg + 2 * pr + (l >> 5);
                u32x2 o; o[0] = cvtpk(h[0], h[1]); o[1] = cvtpk(h[2], h[3]);
                *(LAS u32x2*)(L + LDS_H + ((8 - cg) * 8 + n) * 1024 + (fg * 16 + tok) * 16 + 8 * (g4 & 1)) = o;
            }
    };
    float kn2 = 0.f;
    if ((fg & 1) && tg == (s >> 3)) {
        const int l = fresh_lane(), tok = l & 15, g4 = l >> 4;
        const int e = s >> 1;
        f32x4 cv = acc[9][0];
        if ((e & 3) == 1) cv = acc[9][1]; else if ((e & 3) == 2) cv = acc[9][2]; else if ((e & 3) == 3) cv = acc[9][3];
        const int c0 = (fg == 1 ? 0 : 12) + 4 * (g4 - 1);
        const bool act = g4 >= 1 && c0 < 18;
        const unsigned p0 = cvtpk(cv[0], cv[1]), p1 = cvtpk(cv[2], cv[3]);
        const int kt = 4 * r + (e >> 1);
        char* kvb = ws + WS_KV + ((kt >> 3) & 3) * KVCH; const int lt = 8 * (kt >> 5) + (kt & 7);
        if ((s & 1) == 0) {
            const int key = 16 * (e & 1) + tok;
            if (act) {
                if (c0 < 16) st_agent64(kvb + lt * 1024 + ((c0 >> 3) * 32 + key) * 16 + (c0 & 4) * 2, p0, p1);
                else __hip_atomic_store((GAS unsigned*)(kvb + KV_K2 + lt * 128 + key * 4), p0, RLX_AGENT);
            }
            float n2 = 0.f;
            if (act) { n2 = bflo(p0) * bflo(p0) + bfhi(p0) * bfhi(p0); if (c0 < 16) n2 += bflo(p1) * bflo(p1) + bfhi(p1) * bfhi(p1); }
            kn2 = n2;
        } else {
            if (fg == 1 && l < 2) {
                char* op = kvb + KV_V + lt * 1216 + (e & 1) * 608 + l * 304 + 18 * 16;
                st_agent64(op, 0x3f803f80u, 0x3f803f80u); st_agent64(op + 8, 0x3f803f80u, 0x3f803f80u);
            }
            const int hv = (tok >> 2) & 1, jj = 4 * (tok >> 3) + (tok & 3);
            GAS u16* vb = (GAS u16*)(kvb + KV_V + lt * 1216 + (e & 1) * 608 + hv * 304 + jj * 2);
            if (act) {
                __hip_atomic_store(vb + (c0 + 0) * 8, (u16)(p0 & 0xffff), RLX_AGENT);
                __hip_atomic_store(vb + (c0 + 1) * 8, (u16)(p0 >> 16), RLX_AGENT);
                if (c0 < 16) { __hip_atomic_store(vb + (c0 + 2) * 8, (u16)(p1 & 0xffff), RLX_AGENT);
                               __hip_atomic_store(vb + (c0 + 3) * 8, (u16)(p1 >> 16), RLX_AGENT); }
            }
        }
    }
    h_tiles(std::integral_constant<int, 0>{}, std::integral_constant<int, 9>{});
    {
        const int l = fresh_lane(), tok = l & 15, g4 = l >> 4;
        const float qs = 0.23570226039551584f * 1.4426950408889634f;
        const int hh = fg >> 1;
        unsigned p0[4], p1[4];
#pragma unroll
        for (int n = 0; n < 4; ++n) { p0[n] = cvtpk(acc[9][n][0] * qs, acc[9][n][1] * qs); p1[n] = cvtpk(acc[9][n][2] * qs, acc[9][n][3] * qs); }
        if ((fg & 1) == 0) {
            LAS char* qd = L + LDS_Q1 + (hh * 4 + 2 * tg) * 1024 + ((g4 >> 1) * 32 + tok) * 16 + (g4 & 1) * 8;
#pragma unroll
            for (int n = 0; n < 4; ++n) *(LAS u32x2*)(qd + (n >> 1) * 1024 + (n & 1) * 256) = (u32x2){p0[n], p1[n]};
        } else if (g4 == 0) {
            LAS char* qd = L + LDS_Q2 + (hh * 4 + 2 * tg) * 128 + tok * 4;
#pragma unroll
            for (int n = 0; n < 4; ++n) *(LAS unsigned*)(qd + (n >> 1) * 128 + (n & 1) * 64) = p0[n];
        }
    }
    {
        const int l = fresh_lane(), tw = w * 64 + l;
        const __amdgpu_buffer_rsrc_t rw = __builtin_amdgcn_make_buffer_rsrc(ws + WS_WO, 0, 8 * WOIMG, 0x00020000);
        __builtin_amdgcn_raw_buffer_store_b128(wov, rw, wo_off, 0, 16);
        const __amdgpu_buffer_rsrc_t ro = __builtin_amdgcn_make_buffer_rsrc(ws + WS_O + (size_t)wgi * 128 * 80, 0, 128 * 80, 0x00020000);
        const u32x4 e = (u32x4){O_EMPTY, O_EMPTY, O_EMPTY, O_EMPTY};
        __builtin_amdgcn_raw_buffer_store_b128(e, ro, tw * 16, 0, 16);
        __builtin_amdgcn_raw_buffer_store_b128(e, ro, (tw + 512) * 16, 0, 16);
    }
    if ((fg & 1) && tg == (s >> 3)) {
        kn2 += xor_lane<16>(kn2); kn2 = wave_max(half_sum(kn2));
        if (fresh_lane() == 0) *(LAS float*)(L + LDS_KM + (fg == 1 ? 0 : 4)) = kn2;
    }
    LBAR();
    const i32x4 srd_kv = make_srd(ws + WS_KV, 4 * KVCH);
    auto piece_kv = [&](int c, int i) {
        const int p = min(w + 8 * i, 36);
        dma16_sc1(srd_kv, vo_x, c * KVCH + p * 1024, lds0 + (c == 0 ? LDS_KV0 : (c - 1) * KVCH) + p * 1024);
    };
#pragma unroll
    for (int m = 0; m < 9; ++m)
#pragma unroll
        for (int n = 0; n < 4; ++n) acc[m][n] = (f32x4){0.f, 0.f, 0.f, 0.f};
    {
        LAS const char* hb = L + LDS_H + 8 * 8192 + 4 * tg * 1024 + l * 16;
        gemm_step<9, true, 0, 1>(acc, ca, bB, bA, L + LDS_D0 + a_off, hb, [](int) {});
        WAIT_BAR(0);
        if (w == 0 && fresh_lane() == 0) {
            st_agent64(flags + FL_KV + wgi * 16, tag, *(LAS const unsigned*)(L + LDS_KM));
            st_agent64(flags + FL_KV + wgi * 16 + 8, tag, *(LAS const unsigned*)(L + LDS_KM + 4));
        }
        gemm_step<9, false, 5, 2>(acc, ca, bA, bB, L + LDS_D1 + a_off, hb - 1 * 8192, [&](int i) { piece2(2, LDS_D0, i); });
        WAIT_BAR(0);
        gemm_step<9, false, 5, 2>(acc, ca, bB, bA, L + LDS_D0 + a_off, hb - 2 * 8192, [&](int i) { piece2(3, LDS_D1, i); });
        WAIT_BAR(0);
        gemm_step<9, false, 5, 2>(acc, ca, bA, bB, L + LDS_D1 + a_off, hb - 3 * 8192, [&](int i) { piece2(4, LDS_D0, i); });
        WAIT_BAR(0);
        gemm_step<9, false, 10, 2>(acc, ca, bB, bA, L + LDS_D0 + a_off, hb - 4 * 8192, [&](int i) { if (i < 5) piece2(5, LDS_D1, i); else piece2(6, LDS_D2, i - 5); });
        WAIT_BAR(5);
        gemm_step<9, false, 5, 1>(acc, ca, bA, bB, L + LDS_D1 + a_off, hb - 5 * 8192, [&](int i) { piece2(7, LDS_D0, i); });
        WAIT_BAR(5);
        gemm_step<9, false, 5, 1>(acc, ca, bB, bA, L + LDS_D2 + a_off, hb - 6 * 8192, [&](int i) { piece2(8, LDS_D1, i); });
        if (w == 0) {
            const int lk = fresh_lane();
            const __amdgpu_buffer_rsrc_t rk = __builtin_amdgcn_make_buffer_rsrc(flags + FL_KV, 0, 4096, 0x00020000);
            float ma, mb;
            for (unsigned spins = 0;; ++spins) {
                bool ok = true; ma = 0.f; mb = 0.f;
#pragma unroll
                for (int i = 0; i < 4; ++i) {
                    const u32x4 v = __builtin_amdgcn_raw_buffer_load_b128(rk, (i * 64 + lk) * 16, 0, 16);
                    ok = ok && v[0] == tag && v[2] == tag; ma = fmaxf(ma, __uint_as_float(v[1])); mb = fmaxf(mb, __uint_as_float(v[3]));
                }
                if (__all(ok)) break;
                if (spins > (1u << 19)) { __hip_atomic_store((GAS unsigned*)(flags + FL_TMO), tag, RLX_AGENT); break; }
                __builtin_amdgcn_s_sleep(1);
            }
            ma = wave_max(ma); mb = wave_max(mb);
            if (lk == 0) *(LAS float*)(L + LDS_KM + 8) = ma + mb;
        }
        WAIT_BAR(5);
        gemm_step<9, false, 5, 1>(acc, ca, bA, bB, L + LDS_D0 + a_off, hb - 7 * 8192, [&](int i) { piece_kv(0, i); });
        WAIT_BAR(5);
        gemm_step<9, false, 0, 1>(acc, ca, bB, bA, L + LDS_D1 + a_off, hb - 8 * 8192, [](int) {});
        gemm_flush<9>(acc, ca, bA);
        SB(); FENCE();
    }
    const int la = fresh_lane();
#define l la
    const int hh = w >> 2, kh = (w >> 1) & 1, cp = w & 1;
#define h5 (l >> 5)
    bf16x8 q1[2]; u32x2 q2[2];
#pragma unroll
    for (int c = 0; c < 2; ++c) {
        q1[c] = lds_b128(L + LDS_Q1 + (hh * 4 + 2 * cp + c) * 1024 + l * 16);
        q2[c][0] = *(LAS const unsigned*)(L + LDS_Q2 + (hh * 4 + 2 * cp + c) * 128 + (l & 31) * 4);
    }
    u32x4 pk[18];
#pragma unroll
    for (int m = 0; m < 9; ++m)
#pragma unroll
        for (int pr = 0; pr < 2; ++pr) {
            u32x4 o; o[0] = cvtpk(acc[m][2 * pr][0], acc[m][2 * pr][1]); o[1] = cvtpk(acc[m][2 * pr][2], acc[m][2 * pr][3]);
            o[2] = cvtpk(acc[m][2 * pr + 1][0], acc[m][2 * pr + 1][1]); o[3] = cvtpk(acc[m][2 * pr + 1][2], acc[m][2 * pr + 1][3]);
            pk[m * 2 + pr] = o;
        }
    {
        const float kmax2 = *(LAS const float*)(L + LDS_KM + 8);
#pragma unroll
        for (int c = 0; c < 2; ++c) {
            const u32x4 a = __builtin_bit_cast(u32x4, q1[c]); const unsigned t = h5 ? 0u : q2[c][0];
            float n2 = bflo(t) * bflo(t) + bfhi(t) * bfhi(t);
#pragma unroll
            for (int e = 0; e < 4; ++e) n2 += bflo(a[e]) * bflo(a[e]) + bfhi(a[e]) * bfhi(a[e]);
            n2 = half_sum(n2);
            const float cb = sqrtf(n2 * kmax2) * 1.001f + 0.01f;
            const float chi = rbf(cb), clo = cb - chi;
            q2[c][0] = t; q2[c][1] = h5 ? 0u : cvtpk(-chi, -clo);
        }
    }
    if (w == 0 && l < 40) *(LAS u32x4*)(L + LDS_ZERO + l * 16) = (u32x4){0u, 0u, 0u, 0u};
    if (w == 0 && l == 40) *(LAS unsigned*)(L + LDS_CTL) = 0u;
    WAIT_BAR(0);
#undef l
    const int lb = fresh_lane();
#define l lb
    const unsigned kone = h5 ? 0u : 0x3f803f80u;
    const bool vrow = (l & 31) < 19;
    const unsigned k1_base = 8 * kh * 1024 + l * 16, k2_base = KV_K2 + 8 * kh * 128 + (l & 31) * 4;
    const unsigned v_base = vrow ? KV_V + 8 * kh * 1216 + h5 * 304 + (l & 31) * 16 : LDS_ZERO, v_mul = vrow ? 1u : 0u;
    const int tgf = s >> 3, cgp = s & 7, nmt = cgp < 4 ? 5 : 4, m0 = cgp < 4 ? 5 * cgp : 20 + 4 * (cgp - 4);
    const int pr = w & 1, qk = w >> 1;
    const i32x4 srd_wo = make_srd(ws + WS_WO + (size_t)cgp * WOIMG, WOIMG);
    auto piece_wo = [&](int p) { dma16_sc1(srd_wo, vo_x, p * 1024, lds0 + wo_lds(p)); };
    const __amdgpu_buffer_rsrc_t rpart = __builtin_amdgcn_make_buffer_rsrc(ws + WS_PART + (size_t)r * 16 * 8 * 18 * 1024, 0, 16 * 8 * 18 * 1024, 0x00020000);
    float own[11];
    u32x4 pl[5][4];

    f32x16 o0, o1, sX, sY; bf16x8 kA, kB, v0A, v1A, v0B, v1B; unsigned k2A, k2B; u32x4 p0, p1; float e3[4];
    unsigned aK, aK2, aV;
#define SLOT(SC, SN, KN, K2N, CN, OP, V1P, OC, V0C) do { float ea[4], eb[4]; \
        SN = MFMA32(KN, q1[CN], (f32x16){}); SB(); \
        p1[2] = cvtpk(e3[0], e3[1]); p1[3] = cvtpk(e3[2], e3[3]); SB(); \
        ea[0] = EXP2(SC[0]); ea[1] = EXP2(SC[1]); ea[2] = EXP2(SC[2]); ea[3] = EXP2(SC[3]); SB(); \
        OP = MFMA32(V1P, __builtin_bit_cast(bf16x8, p1), OP); SB(); \
        p0[0] = cvtpk(ea[0], ea[1]); p0[1] = cvtpk(ea[2], ea[3]); SB(); \
        eb[0] = EXP2(SC[4]); eb[1] = EXP2(SC[5]); eb[2] = EXP2(SC[6]); eb[3] = EXP2(SC[7]); SB(); \
        SN = MFMA32K8(__builtin_bit_cast(s16x4, ((u32x2){K2N, kone})), __builtin_bit_cast(s16x4, q2[CN]), SN); SB(); \
        p0[2] = cvtpk(eb[0], eb[1]); p0[3] = cvtpk(eb[2], eb[3]); SB(); \
        ea[0] = EXP2(SC[8]); ea[1] = EXP2(SC[9]); ea[2] = EXP2(SC[10]); ea[3] = EXP2(SC[11]); SB(); \
        OC = MFMA32(V0C, __builtin_bit_cast(bf16x8, p0), OC); SB(); \
        p1[0] = cvtpk(ea[0], ea[1]); p1[1] = cvtpk(ea[2], ea[3]); SB(); \
        e3[0] = EXP2(SC[12]); e3[1] = EXP2(SC[13]); e3[2] = EXP2(SC[14]); e3[3] = EXP2(SC[15]); SB(); } while (0)
#define PAIR(DK, DK2, DV, H0, H1, H2, H3) do { \
        kB = lds_b128(L + aK + 1024); k2B = *(LAS const unsigned*)(L + aK2 + 128); v0B = lds_b128(L + aV + v_mul * 1216); SB(); \
        H0; SLOT(sX, sY, kA, k2A, 1, o1, v1B, o0, v0A); \
        v1B = lds_b128(L + aV + v_mul * 1216 + 608); SB(); \
        H1; SLOT(sY, sX, kB, k2B, 0, o0, v1A, o1, v0A); \
        aK += (DK); aK2 += (DK2); aV += v_mul * (DV); \
        kA = lds_b128(L + aK); k2A = *(LAS const unsigned*)(L + aK2); v0A = lds_b128(L + aV); SB(); \
        H2; SLOT(sX, sY, kB, k2B, 1, o1, v1A, o0, v0B); \
        v1A = lds_b128(L + aV + 608); SB(); \
        H3; SLOT(sY, sX, kA, k2A, 0, o0, v1B, o1, v0B); } while (0)
#define PAIR_IN(H0, H1, H2, H3) PAIR(2048, 256, 2432, H0, H1, H2, H3)
#define PAIR_JUMP(J, H0, H1, H2, H3) PAIR((J) - 6 * 1024, (J) - 6 * 128, (J) - 6 * 1216, H0, H1, H2, H3)
#define NOP_ ((void)0)
#define ATT_INIT() do { o0 = (f32x16){}; o1 = (f32x16){}; aK = k1_base + LDS_KV0; aK2 = k2_base + LDS_KV0; aV = v_base + v_mul * LDS_KV0; \
        kA = lds_b128(L + aK); k2A = *(LAS const unsigned*)(L + aK2); v0A = lds_b128(L + aV); v1A = lds_b128(L + aV + 608); v1B = v1A; \
        sX = MFMA32(kA, q1[0], (f32x16){}); sX = MFMA32K8(__builtin_bit_cast(s16x4, ((u32x2){k2A, kone})), __builtin_bit_cast(s16x4, q2[0]), sX); \
        p1 = (u32x4){0u, 0u, 0u, 0u}; e3[0] = e3[1] = e3[2] = e3[3] = 0.f; } while (0)
#define ATT_FIN() do { p1[2] = cvtpk(e3[0], e3[1]); p1[3] = cvtpk(e3[2], e3[3]); o1 = MFMA32(v1B, __builtin_bit_cast(bf16x8, p1), o1); } while (0)
    constexpr int J01 = 0 - LDS_KV0, J12 = KVCH;
    {
        const __amdgpu_buffer_rsrc_t rp = __builtin_amdgcn_make_buffer_rsrc(ws + WS_PART + (size_t)(r * 16 + s) * 8 * 18 * 1024, 0, 8 * 18 * 1024, 0x00020000);
#define KV(C, I) do { piece_kv(C, I); SB(); } while (0)
#define ST(I) do { __builtin_amdgcn_raw_buffer_store_b128(pk[I], rp, vo_x, (w * 18 + (I)) * 1024, 16); SB(); } while (0)
#define WO(P0, I) do { piece_wo(min((P0) + w + 8 * (I), (P0) + 36)); SB(); } while (0)
#define PL(T, SP) do { const int m_ = m0 + ((T) < nmt ? (T) : 0); \
            pl[T][SP] = __builtin_amdgcn_raw_buffer_load_b128(rpart, vo_x, ((((4 * qk + (SP)) * 8 + tgf * 4 + (m_ & 3)) * 9 + (m_ >> 2)) * 2 + pr) * 1024, 16); SB(); } while (0)
#define PL2(A) do { PL((A) >> 2, (A) & 3); PL(((A) + 1) >> 2, ((A) + 1) & 3); } while (0)
        ATT_INIT();
        PAIR_IN(KV(1, 0), KV(1, 1), KV(1, 2), KV(1, 3));
        PAIR_IN(KV(1, 4), ST(0), KV(2, 0), ST(1));
        PAIR_IN(KV(2, 1), ST(2), KV(2, 2), ST(3));
        WAIT_BAR(7);
        PAIR_JUMP(J01, KV(2, 3), ST(4), KV(2, 4), ST(5));
        PAIR_IN(KV(3, 0), ST(6), KV(3, 1), ST(7));
        PAIR_IN(KV(3, 2), ST(8), KV(3, 3), ST(9));
        PAIR_IN(KV(3, 4), ST(10), NOP_, ST(11));
        WAIT_BAR(12);
        PAIR_JUMP(J12, WO(0, 0), ST(12), WO(0, 1), ST(13));
        PAIR_IN(WO(0, 2), ST(14), WO(0, 3), ST(15));
        PAIR_IN(WO(0, 4), ST(16), NOP_, ST(17));
        PAIR_IN(NOP_, NOP_, NOP_, NOP_);
        WAIT_BAR(0);
        if (w == 0 && fresh_lane() == 0) __hip_atomic_store((GAS unsigned*)(flags + FL_P + wgi * 4), tag, RLX_AGENT);
        PAIR_JUMP(J12, WO(37, 0), WO(37, 1), WO(37, 2), WO(37, 3));
        const unsigned pflag = __hip_atomic_load((GAS unsigned*)(flags + FL_P + (r * 16 + (l & 15)) * 4), RLX_AGENT);
        PAIR_IN(WO(37, 4), NOP_, NOP_, NOP_);
        LBAR();
        if (!__all(pflag == tag)) wait_tags(flags, FL_P + (r * 16 + (l & 15)) * 4, true, tag);
#define WP(I, A) do { WO(74, I); PL((A) >> 2, (A) & 3); } while (0)
        PAIR_IN(WP(0, 0), WP(1, 1), WP(2, 2), WP(3, 3));
        PAIR_IN(WP(4, 4), PL2(5), PL2(7), PL2(9));
        PAIR(0, 0, 0, PL2(11), PL2(13), PL2(15), PL2(17));
        PL(4, 3);
        ATT_FIN();
    }
#undef l
    const int lc = fresh_lane();
#define l lc
    for (int attempt = 0;; ++attempt) {
        if (attempt == 1) {
            const unsigned kone = h5 ? 0u : 0x3f803f80u;
            const bool vrow = (l & 31) < 19;
            const unsigned k1_base = 8 * kh * 1024 + l * 16, k2_base = KV_K2 + 8 * kh * 128 + (l & 31) * 4;
            const unsigned v_base = vrow ? KV_V + 8 * kh * 1216 + h5 * 304 + (l & 31) * 16 : LDS_ZERO, v_mul = vrow ? 1u : 0u;
            WAIT_BAR(0);
#pragma unroll
            for (int c = 0; c < 4; ++c)
#pragma unroll
                for (int i = 0; i < 5; ++i) piece_kv(c, i);
            if (w == 0 && l < 40) *(LAS u32x4*)(L + LDS_ZERO + l * 16) = (u32x4){0u, 0u, 0u, 0u};
            WAIT_BAR(0);
            float m[2] = {-INFINITY, -INFINITY};
            for (int i = 0; i < 32; ++i) {
                const unsigned to = (i >> 3) == 0 ? LDS_KV0 : ((i >> 3) - 1) * KVCH;
                const bf16x8 k1 = lds_b128(L + k1_base + to + (i & 7) * 1024);
                const u32x2 k2 = {*(LAS const unsigned*)(L + k2_base + to + (i & 7) * 128), kone};
#pragma unroll
                for (int c = 0; c < 2; ++c) {
                    const u32x2 qz = {q2[c][0], 0u};
                    f32x16 sc = MFMA32(k1, q1[c], (f32x16){});
                    sc = MFMA32K8(__builtin_bit_cast(s16x4, k2), __builtin_bit_cast(s16x4, qz), sc);
#pragma unroll
                    for (int e = 0; e < 16; ++e) m[c] = fmaxf(m[c], sc[e]);
                }
            }
            LAS float* mx = (LAS float*)(L + LDS_MX);
#pragma unroll
            for (int c = 0; c < 2; ++c) { m[c] = half_max(m[c]); if (l < 32) mx[(w * 2 + c) * 32 + l] = m[c]; }
            LBAR();
#pragma unroll
            for (int c = 0; c < 2; ++c) {
                const float mm = fmaxf(m[c], mx[((w ^ 2) * 2 + c) * 32 + (l & 31)]);
                const float chi = rbf(mm), clo = mm - chi;
                q2[c][1] = h5 ? 0u : cvtpk(-chi, -clo);
            }
            o0 = (f32x16){}; o1 = (f32x16){};
            for (int i = 0; i < 32; ++i) {
                const unsigned to = (i >> 3) == 0 ? LDS_KV0 : ((i >> 3) - 1) * KVCH;
                const bf16x8 k1 = lds_b128(L + k1_base + to + (i & 7) * 1024);
                const u32x2 k2 = {*(LAS const unsigned*)(L + k2_base + to + (i & 7) * 128), kone};
                const unsigned va = v_base + v_mul * (to + (i & 7) * 1216);
                const bf16x8 v0 = lds_b128(L + va), v1 = lds_b128(L + va + 608);
#pragma unroll
                for (int c = 0; c < 2; ++c) {
                    f32x16 sc = MFMA32(k1, q1[c], (f32x16){});
                    sc = MFMA32K8(__builtin_bit_cast(s16x4, k2), __builtin_bit_cast(s16x4, q2[c]), sc);
                    u32x4 pa, pb;
#pragma unroll
                    for (int e = 0; e < 4; ++e) { pa[e] = cvtpk(EXP2(sc[2 * e]), EXP2(sc[2 * e + 1])); pb[e] = cvtpk(EXP2(sc[8 + 2 * e]), EXP2(sc[9 + 2 * e])); }
                    if (c == 0) { o0 = MFMA32(v0, __builtin_bit_cast(bf16x8, pa), o0); o0 = MFMA32(v1, __builtin_bit_cast(bf16x8, pb), o0); }
                    else { o1 = MFMA32(v0, __builtin_bit_cast(bf16x8, pa), o1); o1 = MFMA32(v1, __builtin_bit_cast(bf16x8, pb), o1); }
                }
            }
        }
        LAS float* red = (LAS float*)(L + LDS_RED);
        LBAR();
        if (attempt == 0) {
            piece_wo(111 + w); if (w == 0) piece_wo(119);
        } else {
#pragma unroll
            for (int q = 0; q < 15; ++q) piece_wo(w * 15 + q);
        }
#pragma unroll
        for (int e = 0; e < 11; ++e) { red[(w * 11 + e) * 64 + l] = kh ? o0[e] : o1[e]; own[e] = kh ? o1[e] : o0[e]; }
        LBAR();
#pragma unroll
        for (int e = 0; e < 11; ++e) own[e] += red[((w ^ 2) * 11 + e) * 64 + l];
        const float lsum = half_lo(own[10]);
        if (!(lsum >= 8.67e-19f && lsum < 3.0e38f) && attempt == 0) *(LAS unsigned*)(L + LDS_CTL) = 1u;
        const float rl = 1.0f / lsum;
        LAS char* ot = L + LDS_OT + (32 * (2 * cp + kh) + (l & 31)) * 80 + 40 * hh;
        *(LAS u32x2*)(ot + 8 * h5) = (u32x2){cvtpk(own[0] * rl, own[1] * rl), cvtpk(own[2] * rl, own[3] * rl)};
        *(LAS u32x2*)(ot + 16 + 8 * h5) = (u32x2){cvtpk(own[4] * rl, own[5] * rl), cvtpk(own[6] * rl, own[7] * rl)};
        if (h5 == 0) *(LAS u32x2*)(ot + 32) = (u32x2){cvtpk(own[8] * rl, own[9] * rl), 0u};
        LBAR();
        const unsigned isbad = *(LAS const unsigned*)(L + LDS_CTL);
        if (!(isbad && attempt == 0)) break;
    }
    {
        const __amdgpu_buffer_rsrc_t rw = __builtin_amdgcn_make_buffer_rsrc(ws + WS_O + (size_t)(r * 16 + s) * 128 * 80, 0, 128 * 80, 0x00020000);
        __builtin_amdgcn_raw_buffer_store_b128(*(LAS const u32x4*)(L + LDS_OT + w * 1024 + l * 16), rw, l * 16, w * 1024, 16);
        if (w < 2) __builtin_amdgcn_raw_buffer_store_b128(*(LAS const u32x4*)(L + LDS_OT + (8 + w) * 1024 + l * 16), rw, l * 16, (8 + w) * 1024, 16);
    }
    {
        const int tok = l & 15, g4 = l >> 4, q4 = (l >> 2) & 3, p4 = l & 3;
        const __amdgpu_buffer_rsrc_t ro = __builtin_amdgcn_make_buffer_rsrc(ws + WS_O + (size_t)r * 16 * 128 * 80, 0, 16 * 128 * 80, 0x00020000);
        u32x4 bf[5][2]; unsigned bo[5];
#pragma unroll
        for (int i = 0; i < 5; ++i) {
            const int kp = 32 * (5 * qk + i) + 8 * g4, sp = kp / 40, d = kp - 40 * sp;
            bo[i] = (sp * 128 + 64 * tgf + 32 * pr + tok) * 80 + d * 2;
#pragma unroll
            for (int nn = 0; nn < 2; ++nn) bf[i][nn] = __builtin_amdgcn_raw_buffer_load_b128(ro, bo[i], nn * 16 * 80, 16);
        }
        SB();
        u32x2 xv[3];
#pragma unroll
        for (int u = 0; u < 3; ++u) {
            const int tt = min(qk + 4 * u, 2 * nmt - 1);
            xv[u] = *(const u32x2*)(x + (size_t)(128 * r + 64 * tgf + 32 * pr + 16 * (tt & 1) + tok) * DM + 16 * (m0 + (tt >> 1)) + 4 * g4);
        }
        f32x4 fa[5][2];
#pragma unroll
        for (int t = 0; t < 5; ++t) {
            fa[t][0] = (f32x4){0.f, 0.f, 0.f, 0.f}; fa[t][1] = (f32x4){0.f, 0.f, 0.f, 0.f};
#pragma unroll
            for (int sp = 0; sp < 4; ++sp) {
                const u32x4 p = pl[t][sp];
                fa[t][0][0] += bflo(p[0]); fa[t][0][1] += bfhi(p[0]); fa[t][0][2] += bflo(p[1]); fa[t][0][3] += bfhi(p[1]);
                fa[t][1][0] += bflo(p[2]); fa[t][1][1] += bfhi(p[2]); fa[t][1][2] += bflo(p[3]); fa[t][1][3] += bfhi(p[3]);
            }
        }
#pragma unroll
        for (int t = 0; t < 5; ++t) asm volatile("" : "+v"(fa[t][0]), "+v"(fa[t][1]));
        WAIT_BAR(0);
        bf16x8 af[5][5];
        {
            const unsigned ao = (4 * g4 + q4) * 64 + 8 * (p4 & 1);
#pragma unroll
            for (int t = 0; t < 5; ++t)
#pragma unroll
                for (int i = 0; i < 5; ++i) {
                    const int p = ((t < nmt ? t : 0) >> 1) * 40 + (5 * qk + i) * 2;
                    const unsigned sw = ao + 16 * ((2 * (t & 1) + (p4 >> 1)) ^ (2 * (g4 & 1)));
                    const s16x4 lo = lds_tr(L + wo_lds(p) + sw), hi = lds_tr(L + wo_lds(p + 1) + sw);
                    af[t][i] = __builtin_shufflevector(lo, hi, 0, 1, 2, 3, 4, 5, 6, 7);
                }
        }
        LBAR();
        unsigned pend = 0x3ffu;
        for (unsigned spins = 0;; ++spins) {
#pragma unroll
            for (int i = 0; i < 5; ++i)
#pragma unroll
                for (int nn = 0; nn < 2; ++nn)
                    if ((pend & (1u << (2 * i + nn))) && __all(bf[i][nn][0] != O_EMPTY && bf[i][nn][1] != O_EMPTY && bf[i][nn][2] != O_EMPTY && bf[i][nn][3] != O_EMPTY)) pend &= ~(1u << (2 * i + nn));
            if (pend == 0u) break;
            if (spins > (1u << 17)) { __hip_atomic_store((GAS unsigned*)(flags + FL_TMO), tag, RLX_AGENT); break; }
            __builtin_amdgcn_s_sleep(8);
#pragma unroll
            for (int i = 0; i < 5; ++i)
#pragma unroll
                for (int nn = 0; nn < 2; ++nn)
                    if (pend & (1u << (2 * i + nn))) bf[i][nn] = __builtin_amdgcn_raw_buffer_load_b128(ro, bo[i], nn * 16 * 80, 16);
        }
#pragma unroll
        for (int i = 0; i < 5; ++i)
#pragma unroll
            for (int t = 0; t < 5; ++t) {
                fa[t][0] = MFMA16(af[t][i], __builtin_bit_cast(bf16x8, bf[i][0]), fa[t][0]); fa[t][1] = MFMA16(af[t][i], __builtin_bit_cast(bf16x8, bf[i][1]), fa[t][1]);
            }
        LAS f32x4* ex = (LAS f32x4*)L;
#pragma unroll
        for (int t = 0; t < 5; ++t) { ex[(w * 10 + 2 * t) * 64 + l] = fa[t][0]; ex[(w * 10 + 2 * t + 1) * 64 + l] = fa[t][1]; }
        LBAR();
#pragma unroll
        for (int u = 0; u < 3; ++u) {
            const int tt = qk + 4 * u;
            if (tt < 2 * nmt) {
                f32x4 v = ex[((0 * 2 + pr) * 10 + tt) * 64 + l];
#pragma unroll
                for (int k = 1; k < 4; ++k) v += ex[((k * 2 + pr) * 10 + tt) * 64 + l];
                const size_t off = (size_t)(128 * r + 64 * tgf + 32 * pr + 16 * (tt & 1) + tok) * DM + 16 * (m0 + (tt >> 1)) + 4 * g4;
                *(u32x2*)(out + off) = (u32x2){cvtpk(v[0] + bflo(xv[u][0]), v[1] + bfhi(xv[u][0])), cvtpk(v[2] + bflo(xv[u][1]), v[3] + bfhi(xv[u][1]))};
            }
        }
    }
#undef l
#undef h5
    asm volatile("fused_block_code_end:");
}

extern "C" void kernel_launch(void* const* d_in, const int* in_sizes, int n_in, void* d_out, int out_size, void* d_ws, size_t ws_size, hipStream_t stream) {
    const u16* x = (const u16*)d_in[0]; const u16* wq = (const u16*)d_in[1]; const u16* wk = (const u16*)d_in[2]; const u16* wv = (const u16*)d_in[3];
    const u16* wo = (const u16*)d_in[4]; const u16* wg = (const u16*)d_in[6]; const u16* wu = (const u16*)d_in[7]; const u16* wd = (const u16*)d_in[8];
    fused_block<<<256, 512, 0, stream>>>(x, wq, wk, wv, wo, wg, wu, wd, (char*)d_ws, (u16*)d_out);
}
```

```cpp
#include <hip/hip_runtime.h>
#include <hip/hip_bf16.h>
#include <stdint.h>
#include <type_traits>

typedef unsigned short u16;
typedef short bf16x8 __attribute__((ext_vector_type(8)));
typedef short s16x4 __attribute__((ext_vector_type(4)));
typedef float f32x4 __attribute__((ext_vector_type(4)));
typedef float f32x16 __attribute__((ext_vector_type(16)));
typedef int i32x4 __attribute__((ext_vector_type(4)));
typedef unsigned u32x4 __attribute__((ext_vector_type(4)));
typedef unsigned u32x2 __attribute__((ext_vector_type(2)));
typedef __bf16 bf16x2_t __attribute__((ext_vector_type(2)));
typedef float f32x2 __attribute__((ext_vector_type(2)));
typedef short v4i16_t __attribute__((ext_vector_type(4)));
extern "C" __device__ unsigned long long amd_dispatch_id() __asm("llvm.amdgcn.dispatch.id");
#define LAS __attribute__((address_space(3)))
#define GAS __attribute__((address_space(1)))
#define DI __device__ __forceinline__

constexpr int SEQ = 2048, DM = 576, NH = 32, DH = 18, FF = 4608;

constexpr int KVCH = 37888;
constexpr int KV_V = 16384, KV_K2 = 35840;
constexpr int WOIMG = 120 * 1024;
constexpr size_t WS_FLAGS = 0;
constexpr size_t WS_XN    = 16384;
constexpr size_t WS_WQX   = WS_XN + (size_t)SEQ * DM * 2;
constexpr size_t WS_WO    = WS_WQX + (size_t)DM * 1024 * 2;
constexpr size_t WS_KV    = WS_WO + (size_t)8 * WOIMG;
constexpr size_t WS_O     = WS_KV + 4 * KVCH;
constexpr size_t WS_PART  = WS_O + (size_t)256 * 128 * 80;
constexpr size_t WS_END   = WS_PART + (size_t)256 * 8 * 18 * 1024;
constexpr int FL_PRO = 0, FL_P = 1024, FL_KV = 2048, FL_TMO = 8192;
constexpr unsigned O_EMPTY = 0xffffffffu;

DI float bflo(unsigned v) { return __uint_as_float(v << 16); }
DI float bfhi(unsigned v) { return __uint_as_float(v & 0xffff0000u); }
DI unsigned cvtpk(float lo, float hi) { return __builtin_bit_cast(unsigned, __builtin_convertvector((f32x2){lo, hi}, bf16x2_t)); }
DI float rbf(float f) { return bflo(cvtpk(f, f)); }
DI i32x4 make_srd(const void* p, unsigned bytes) {
    unsigned long long a = (unsigned long long)p; i32x4 s;
    s.x = __builtin_amdgcn_readfirstlane((unsigned)a); s.y = __builtin_amdgcn_readfirstlane((unsigned)(a >> 32) & 0xffffu);
    s.z = (int)bytes; s.w = 0x00020000; return s;
}
DI void dma16(i32x4 srd, unsigned voff, unsigned soff, unsigned lds_addr) {
    unsigned keep;
    asm volatile("s_mov_b32 %0, m0\n\ts_mov_b32 m0, %4\n\ts_nop 0\n\tbuffer_load_dwordx4 %1, %2, %3 offen lds\n\ts_mov_b32 m0, %0"
                 : "=&s"(keep) : "v"(voff), "s"(srd), "s"(soff), "s"(lds_addr) : "memory");
}
DI void dma16_sc1(i32x4 srd, unsigned voff, unsigned soff, unsigned lds_addr) {
    unsigned keep;
    asm volatile("s_mov_b32 %0, m0\n\ts_mov_b32 m0, %4\n\ts_nop 0\n\tbuffer_load_dwordx4 %1, %2, %3 offen sc1 lds\n\ts_mov_b32 m0, %0"
                 : "=&s"(keep) : "v"(voff), "s"(srd), "s"(soff), "s"(lds_addr) : "memory");
}
DI void dma4(i32x4 srd, unsigned voff, unsigned soff, unsigned lds_addr) {
    unsigned keep;
    asm volatile("s_mov_b32 %0, m0\n\ts_mov_b32 m0, %4\n\ts_nop 0\n\tbuffer_load_dword %1, %2, %3 offen lds\n\ts_mov_b32 m0, %0"
                 : "=&s"(keep) : "v"(voff), "s"(srd), "s"(soff), "s"(lds_addr) : "memory");
}
#define WAIT_BAR(N) asm volatile("s_waitcnt vmcnt(" #N ") lgkmcnt(0)\n\ts_barrier" ::: "memory")
#define LBAR() asm volatile("s_waitcnt lgkmcnt(0)\n\ts_barrier" ::: "memory")
#define SB() __builtin_amdgcn_sched_barrier(0)
#define FENCE() asm volatile("" ::: "memory")
#define PIN4(a) asm volatile("" : "+v"((a)[0]), "+v"((a)[1]), "+v"((a)[2]), "+v"((a)[3]))
#define MFMA16(a, b, c) __builtin_amdgcn_mfma_f32_16x16x32_bf16(a, b, c, 0, 0, 0)
#define MFMA32(a, b, c) __builtin_amdgcn_mfma_f32_32x32x16_bf16(a, b, c, 0, 0, 0)
#define MFMA32K8(a, b, c) __builtin_amdgcn_mfma_f32_32x32x8bf16_1k(a, b, c, 0, 0, 0)
#define EXP2(x) __builtin_amdgcn_exp2f(x)
DI s16x4 lds_tr(LAS const char* p) { return __builtin_bit_cast(s16x4, __builtin_amdgcn_ds_read_tr16_b64_v4i16((LAS v4i16_t*)p)); }
DI bf16x8 lds_b128(LAS const char* p) { return *(LAS const bf16x8*)p; }
DI bf16x8 lds_afrag(LAS const char* p) { const s16x4 lo = lds_tr(p), hi = lds_tr(p + 2048); return __builtin_shufflevector(lo, hi, 0, 1, 2, 3, 4, 5, 6, 7); }
#define RLX_AGENT __ATOMIC_RELAXED, __HIP_MEMORY_SCOPE_AGENT
template <int X> DI float xor_lane(float v) { return __int_as_float(__builtin_amdgcn_ds_swizzle(__float_as_int(v), (X << 10) | 0x1f)); }
DI float half_sum(float v) { auto r = __builtin_amdgcn_permlane32_swap(__float_as_uint(v), __float_as_uint(v), false, false); return __uint_as_float(r[0]) + __uint_as_float(r[1]); }
DI float half_max(float v) { auto r = __builtin_amdgcn_permlane32_swap(__float_as_uint(v), __float_as_uint(v), false, false); return fmaxf(__uint_as_float(r[0]), __uint_as_float(r[1])); }
DI float half_lo(float v) { auto r = __builtin_amdgcn_permlane32_swap(__float_as_uint(v), __float_as_uint(v), false, false); return __uint_as_float(r[0]); }
template <int CTRL, int ROWS> DI float dpp_add(float v) { return v + __int_as_float(__builtin_amdgcn_update_dpp(0, __float_as_int(v), CTRL, ROWS, 0xf, true)); }
DI float wave_sum_dpp(float v) {
    v = dpp_add<0x111, 0xf>(v); v = dpp_add<0x112, 0xf>(v); v = dpp_add<0x114, 0xf>(v); v = dpp_add<0x118, 0xf>(v);
    v = dpp_add<0x142, 0xa>(v); v = dpp_add<0x143, 0xc>(v);
    return __int_as_float(__builtin_amdgcn_readlane(__float_as_int(v), 63));
}
DI float wave_sum(float v) { v += xor_lane<1>(v); v += xor_lane<2>(v); v += xor_lane<4>(v); v += xor_lane<8>(v); v += xor_lane<16>(v); return half_sum(v); }
DI float wave_max(float v) { v = fmaxf(v, xor_lane<1>(v)); v = fmaxf(v, xor_lane<2>(v)); v = fmaxf(v, xor_lane<4>(v)); v = fmaxf(v, xor_lane<8>(v)); v = fmaxf(v, xor_lane<16>(v)); return half_max(v); }
DI int fresh_lane() { int t; asm volatile("v_mbcnt_lo_u32_b32 %0, -1, 0\n\tv_mbcnt_hi_u32_b32 %0, -1, %0" : "=v"(t)); return t; }
DI void st_agent64(void* p, unsigned lo, unsigned hi) { __hip_atomic_store((GAS unsigned long long*)p, ((unsigned long long)hi << 32) | lo, RLX_AGENT); }
DI void wait_tags(const char* flags, unsigned off, bool use, unsigned tag) {
    for (unsigned spins = 0;; ++spins) {
        const unsigned v = use ? __hip_atomic_load((GAS unsigned*)(flags + off), RLX_AGENT) : tag;
        if (__all(v == tag)) break;
        if (spins > (1u << 19)) { __hip_atomic_store((GAS unsigned*)(flags + FL_TMO), tag, RLX_AGENT); break; }
        __builtin_amdgcn_s_sleep(1);
    }
    FENCE();
}

#define DMA345 dma16_sc1
constexpr int ST1 = 49152;
constexpr int LDS_D0 = 0, LDS_D1 = 49152, LDS_H = 86016, LDS_Q1 = 36864, LDS_Q2 = 45056;
constexpr int LDS_PF = 147456;
constexpr int LDS_KM = 46080;
constexpr int LDS_D2 = 126976;
constexpr int LDS_KV0 = 125952;
constexpr int LDS_OT = 113664;
constexpr int LDS_MX = LDS_OT;
constexpr int LDS_ZERO = LDS_OT + 10240;
constexpr int LDS_CTL = LDS_ZERO + 640;
constexpr int LDS_RED = 2 * KVCH + 9 * 1024;
constexpr int LDS_BYTES = 163840;
DI unsigned wo_lds(int p) { return p < 37 ? LDS_KV0 + p * 1024 : p < 74 ? (p - 37) * 1024 : p < 111 ? KVCH + (p - 74) * 1024 : 2 * KVCH + (p - 111) * 1024; }

template <int NM, bool FIRST, int ND, int DPG, int AD = 3, bool PIN = false, class Dma>
DI void gemm_step(f32x4 (&acc)[10][4], bf16x8 (&ca)[2], bf16x8 (&cb)[4], bf16x8 (&nb)[4], LAS const char* ap, LAS const char* bp, const Dma& dma) {
    bf16x8 a[10];
#pragma unroll
    for (int n = 0; n < 4; ++n) nb[n] = lds_b128(bp + n * 1024);
    a[0] = lds_afrag(ap); a[1] = lds_afrag(ap + 4096); if (AD > 2) a[2] = lds_afrag(ap + 2 * 4096);
    SB();
    int d = 0;
    if (!FIRST) {
#pragma unroll
        for (int k = 0; k < DPG; ++k) if (d < ND) dma(d++);
#pragma unroll
        for (int n = 0; n < 4; ++n) acc[NM - 2][n] = MFMA16(ca[0], cb[n], acc[NM - 2][n]);
        if (PIN) PIN4(acc[NM - 2]); SB();
#pragma unroll
        for (int k = 0; k < DPG; ++k) if (d < ND) dma(d++);
#pragma unroll
        for (int n = 0; n < 4; ++n) acc[NM - 1][n] = MFMA16(ca[1], cb[n], acc[NM - 1][n]);
        if (PIN) PIN4(acc[NM - 1]); SB();
    }
#pragma unroll
    for (int m = 0; m < NM - 2; ++m) {
        if (m + AD < NM) a[m + AD] = lds_afrag(ap + (m + AD) * 4096);
#pragma unroll
        for (int k = 0; k < DPG; ++k) if (d < ND) dma(d++);
#pragma unroll
        for (int n = 0; n < 4; ++n) acc[m][n] = MFMA16(a[m], nb[n], acc[m][n]);
        if (PIN) PIN4(acc[m]); SB();
    }
    ca[0] = a[NM - 2]; ca[1] = a[NM - 1];
}
template <int NM>
DI void gemm_flush(f32x4 (&acc)[10][4], const bf16x8 (&ca)[2], const bf16x8 (&cb)[4]) {
#pragma unroll
    for (int n = 0; n < 4; ++n) acc[NM - 2][n] = MFMA16(ca[0], cb[n], acc[NM - 2][n]);
#pragma unroll
    for (int n = 0; n < 4; ++n) acc[NM - 1][n] = MFMA16(ca[1], cb[n], acc[NM - 1][n]);
}

__global__ __launch_bounds__(512, 2) void fused_block(const u16* __restrict__ x, const u16* __restrict__ wq, const u16* __restrict__ wk, const u16* __restrict__ wv,
                                                       const u16* __restrict__ wo, const u16* __restrict__ wg, const u16* __restrict__ wu, const u16* __restrict__ wd,
                                                       char* __restrict__ ws, u16* __restrict__ out) {
    __shared__ __attribute__((aligned(1024))) char lds[LDS_BYTES];
    const int tid = threadIdx.x, l = tid & 63;
    const int w = __builtin_amdgcn_readfirstlane(tid >> 6);
    const int b = blockIdx.x, s = 2 * (b & 7) + ((b >> 3) & 1), r = b >> 4, wgi = r * 16 + s;
    const int tg = w >> 2, fg = w & 3;
    constexpr unsigned lds0 = 0;
    LAS char* L = (LAS char*)lds;
    char* flags = ws + WS_FLAGS;
    const unsigned tag = (((unsigned)amd_dispatch_id() + 1u) * 0x9E3779B1u) ^ ((unsigned)((unsigned long long)__builtin_amdgcn_dispatch_ptr() >> 6) * 0x85EBCA77u) ^ 0x5bd1e995u;

    const int dmat = w & 1, dR = (w >> 1) & 1, dcg0 = 5 * (w >> 2);
    const int di = l >> 2, dsig = l & 3;
    const int dmi = dsig ^ (2 * dmat + ((di >> 2) & 1)), dkrow = 8 * (di >> 2) + (di & 3);
    const i32x4 srd_w = make_srd(dmat ? wu : wg, DM * FF * 2);
    const i32x4 srd_q = make_srd(ws + WS_WQX, DM * 1024 * 2);
    const i32x4 srd_x = make_srd(ws + WS_XN, SEQ * DM * 2);
    const unsigned vo_w = dkrow * (FF * 2) + 16 * dmi, vo_q = dkrow * 2048 + 16 * dmi, vo_x = l * 16;
    const unsigned dlds = lds0 + dR * 2048 + dmat * 1024;
    auto pieceW = [&](int j, unsigned slot, int cg) { dma16(srd_w, vo_w, (32 * j + 4 * dR) * (FF * 2) + 288 * s * 2 + cg * 64, dlds + slot + cg * 4096); };
    auto pieceQ = [&](int j, unsigned slot) { dma16_sc1(srd_q, vo_q, (32 * j + 4 * dR) * 2048 + (64 * s + 32 * dmat) * 2, dlds + slot + 9 * 4096); };
    auto pieceX = [&](int j, unsigned slot, int n) { dma16_sc1(srd_x, vo_x, ((r * 18 + j) * 8 + n) * 1024, lds0 + slot + 40960 + n * 1024); };
    auto piece1k = [&](int j, unsigned slot, int i, int kind) {
        if (i < 3) { if (kind & 1) pieceW(j, slot, dcg0 + i); }
        else if (i == 3) { if (w == 3) { if (kind & 2) pieceQ(j, slot); } else if (kind & 1) pieceW(j, slot, dcg0 + 3); }
        else if (i == 4) {
            if (w < 3) { if (kind & 1) pieceW(j, slot, 4); } else if (w == 3) { if (kind & 2) pieceX(j, slot, 7); }
            else if (w < 7) { if (kind & 2) pieceQ(j, slot); } else if (kind & 1) pieceW(j, slot, 3);
        } else { if (w == 7) { if (kind & 1) pieceW(j, slot, 4); } else if (kind & 2) pieceX(j, slot, w); }
    };
    auto piece2 = [&](int j, unsigned slot, int i) {
        const int ii = (w >= 4 && i == 4) ? 3 : i;
        const unsigned vo_d = ((vo_w & ~63u) >> 3) + ((vo_w & 48u) << 1);
        dma16(make_srd(wd, FF * DM * 2), vo_d, (288 * s + 32 * j + 4 * dR) * (DM * 2) + 16 * dmat + (dcg0 + ii) * 128, dlds + slot + (dcg0 + ii) * 4096);
    };

    const u32x4* xs = (const u32x4*)(x + (size_t)(128 * r + 8 * s + w) * DM);
    const u32x4 v0 = xs[l];
    u32x4 v1 = xs[64 + (l & 7)];
    FENCE();
    if (w == 7) {
        unsigned long long pc; unsigned len;
        asm volatile("s_getpc_b64 %0\n\tcode_here_%=:\n\ts_mov_b32 %1, fused_block_code_end-code_here_%=" : "=s"(pc), "=s"(len));
        i32x4 sc; sc.x = (int)(unsigned)pc; sc.y = (int)((unsigned)(pc >> 32) & 0xffffu); sc.z = (int)len; sc.w = 0x00020000;
        dma4(sc, l < 20 ? (unsigned)(((b >> 3) * 20 + l) * 128) : 0x80000000u, 0, lds0 + LDS_PF);
    }
    {
        unsigned qv[4];
        {
            const int tq = min(tid, 287), k = 36 * r + (tq >> 3), mat = (tq >> 2) & 1, t = tq & 3;
            const bool odd = t & 1;
            const __amdgpu_buffer_rsrc_t rq_ = __builtin_amdgcn_make_buffer_rsrc((void*)wq, 0, DM * DM * 2, 0x00020000);
            const __amdgpu_buffer_rsrc_t rc_ = __builtin_amdgcn_make_buffer_rsrc((void*)((s & 1) ? wv : wk), 0, DM * DH * 2, 0x00020000);
            const u32x4 a = __builtin_amdgcn_raw_buffer_load_b128(rq_, (k * DM + (2 * s + (t >> 1)) * DH + (odd ? 16 : 8 * mat)) * 2, 0, 0);
            const u32x4 c = __builtin_amdgcn_raw_buffer_load_b128(rc_, (k * DH + (t == 1 ? 4 * mat : 12 + 4 * mat)) * 2, 0, 0);
            qv[0] = (!odd || mat == 0) ? a[0] : c[0];
            qv[1] = !odd ? a[1] : (mat == 1 && t == 1) ? c[1] : 0u;
            qv[2] = !odd ? a[2] : mat == 0 ? c[0] : t == 1 ? c[2] : 0u;
            qv[3] = !odd ? a[3] : mat == 0 ? c[1] : t == 1 ? c[3] : 0u;
        }
        FENCE();
        if (l >= 8) v1 = (u32x4){0u, 0u, 0u, 0u};
        float sm = 0.f;
#pragma unroll
        for (int e = 0; e < 4; ++e) sm += bflo(v0[e]) + bfhi(v0[e]) + bflo(v1[e]) + bfhi(v1[e]);
        const float mean = wave_sum_dpp(sm) * (1.0f / DM);
        float q = 0.f, q1 = 0.f;
#pragma unroll
        for (int e = 0; e < 4; ++e) { const float a = bflo(v0[e]) - mean, c = bfhi(v0[e]) - mean; q += a * a + c * c; }
#pragma unroll
        for (int e = 0; e < 4; ++e) { const float a = bflo(v1[e]) - mean, c = bfhi(v1[e]) - mean; q1 += a * a + c * c; }
        if (l < 8) q += q1;
        const float m = rbf(mean), var = rbf(wave_sum_dpp(q) * (1.0f / DM));
        const float rs = 1.0f / rbf(sqrtf(rbf(var + 1e-5f)));
        LAS char* xst = L + 2 * ST1;
        {
            u32x4 o;
#pragma unroll
            for (int e = 0; e < 4; ++e) o[e] = cvtpk(rbf(bflo(v0[e]) - m) * rs, rbf(bfhi(v0[e]) - m) * rs);
            *(LAS u32x4*)(xst + l * 128 + w * 16) = o;
#pragma unroll
            for (int e = 0; e < 4; ++e) o[e] = cvtpk(rbf(bflo(v1[e]) - m) * rs, rbf(bfhi(v1[e]) - m) * rs);
            if (l < 8) *(LAS u32x4*)(xst + (64 + l) * 128 + w * 16) = o;
        }
        LBAR();
        if (w < 7) {
            const __amdgpu_buffer_rsrc_t rx = __builtin_amdgcn_make_buffer_rsrc(ws + WS_XN, 0, SEQ * DM * 2, 0x00020000);
#pragma unroll
            for (int u = 0; u < 2; ++u) {
                const int i = tid + 448 * u, c = i >> 3, t = i & 7;
                const u32x4 v = *(LAS const u32x4*)(xst + min(i, 575) * 16);
                __builtin_amdgcn_raw_buffer_store_b128(v, rx, i < 576 ? (unsigned)(((r * 18 + (c >> 2)) * 8 + (s >> 1)) * 1024 + ((c & 3) * 16 + 8 * (s & 1) + t) * 16) : 0xfffffff0u, 0, 16);
            }
            {
                const u32x4 o = (u32x4){qv[0], qv[1], qv[2], qv[3]};
                const __amdgpu_buffer_rsrc_t rq = __builtin_amdgcn_make_buffer_rsrc(ws + WS_WQX, 0, DM * 1024 * 2, 0x00020000);
                __builtin_amdgcn_raw_buffer_store_b128(o, rq, tid < 288 ? (unsigned)(((36 * r + (tid >> 3)) * 128 + s * 8 + (tid & 7)) * 16) : 0xfffffff0u, 0, 16);
            }
            WAIT_BAR(0);
        } else {
            const unsigned o = ((l & 7) < 5 && l < 32) ? (unsigned)((4 * r + (l >> 3)) * (FF * 2) + ((288 * s * 2) & ~127) + (l & 7) * 128) : 0x80000000u;
            dma4(make_srd(wg, DM * FF * 2), o, 0, lds0 + LDS_PF); dma4(srd_w, o, 0, lds0 + LDS_PF);
            LBAR();
        }
        if (tid == 0) __hip_atomic_store((GAS unsigned*)(flags + FL_PRO + wgi * 4), tag, RLX_AGENT);
#pragma unroll
        for (int i = 0; i < 6; ++i) piece1k(0, 0, i, 1);
#pragma unroll
        for (int i = 0; i < 6; ++i) piece1k(1, ST1, i, 1);
        if (w == 0) wait_tags(flags, FL_PRO + (l < 16 ? (r * 16 + l) * 4 : ((l - 16) * 16 + s) * 4), l < 32, tag);
        asm volatile("s_barrier" ::: "memory");
#pragma unroll
        for (int i = 3; i < 6; ++i) piece1k(0, 0, i, 2);
#pragma unroll
        for (int i = 3; i < 6; ++i) piece1k(1, ST1, i, 2);
        if (w == 7) {
            const unsigned o = (l & 7) < 5 ? (unsigned)(((l >> 3) * 16 + r) * (FF * 2) + ((288 * s * 2) & ~127) + (l & 7) * 128) : 0x80000000u;
#pragma unroll
            for (int g = 0; g < 2; ++g) { dma4(make_srd(wg, DM * FF * 2), o, g * 128 * (FF * 2), lds0 + LDS_PF); dma4(srd_w, o, g * 128 * (FF * 2), lds0 + LDS_PF); }
        }
    }
    const int lg = fresh_lane();
    const unsigned a_off = ((lg & 3) >> 1) * 1024 + 16 * (4 * (4 * (lg >> 4) + ((lg >> 2) & 3)) + (fg ^ (2 * ((lg & 3) >> 1) + ((lg >> 4) & 1)))) + 8 * (lg & 1);

    f32x4 acc[10][4];
#pragma unroll
    for (int m = 0; m < 10; ++m)
#pragma unroll
        for (int n = 0; n < 4; ++n) acc[m][n] = (f32x4){0.f, 0.f, 0.f, 0.f};
    bf16x8 ca[2], bA[4], bB[4];
    {
        const unsigned bo = 40960 + tg * 4096 + lg * 16;
        if (w == 7) { WAIT_BAR(4); } else { WAIT_BAR(0); }
        const bool q3 = w == 3, w4 = w < 3 || w == 7, x4 = w == 3, w5 = w == 7;
        const i32x4 srd3 = q3 ? srd_q : srd_w, srd4 = w4 ? srd_w : x4 ? srd_x : srd_q, srd5 = w5 ? srd_w : srd_x;
        const unsigned vo3 = q3 ? vo_q : vo_w, vo4 = w4 ? vo_w : x4 ? vo_x : vo_q, vo5 = w5 ? vo_w : vo_x;
        const unsigned soW = 4 * dR * (FF * 2) + 288 * s * 2, soQ = 4 * dR * 2048 + (64 * s + 32 * dmat) * 2, soX = r * 18 * 8192;
        const unsigned stW = 32 * FF * 2, stQ = 32 * 2048, stX = 8192;
        const unsigned so3 = q3 ? soQ : soW + (dcg0 + 3) * 64, st3 = q3 ? stQ : stW, lo3 = q3 ? dlds + 9 * 4096 : dlds + (dcg0 + 3) * 4096;
        const int cg4 = w < 3 ? 4 : 3;
        const unsigned so4 = w4 ? soW + cg4 * 64 : x4 ? soX + 7 * 1024 : soQ, st4 = w4 ? stW : x4 ? stX : stQ, lo4 = w4 ? dlds + cg4 * 4096 : x4 ? lds0 + 40960 + 7 * 1024 : dlds + 9 * 4096;
        const unsigned so5 = w5 ? soW + 4 * 64 : soX + w * 1024, st5 = w5 ? stW : stX, lo5 = w5 ? dlds + 4 * 4096 : lds0 + 40960 + w * 1024;
        unsigned vo_pf;
        { const int lp = fresh_lane(); vo_pf = (lp & 7) < 5 ? (unsigned)(((lp >> 3) * 16 + r) * (FF * 2) + ((288 * s * 2) & ~127) + (lp & 7) * 128) : 0x80000000u; }
        auto stage1 = [&](int j, unsigned slot, int i) {
            if (i < 3) dma16(srd_w, vo_w, soW + (dcg0 + i) * 64 + j * stW, dlds + (dcg0 + i) * 4096 + slot);
            else if (i == 3) DMA345(srd3, vo3, so3 + j * st3, lo3 + slot);
            else if (i == 4) DMA345(srd4, vo4, so4 + j * st4, lo4 + slot);
            else if (i == 5) DMA345(srd5, vo5, so5 + j * st5, lo5 + slot);
            else if (w >= 6) {
                const int t = j - 2;
                if (t == 6) { const int lp = fresh_lane(); vo_pf = (w == 7 && (lp & 15) < 9) ? (unsigned)((288 * s + (lp >> 4) * 16 + r) * (DM * 2) + (lp & 15) * 128) : 0x80000000u; }
                if (t == 11) { const int lp = fresh_lane(); vo_pf = (w == 7 && lp < 21) ? (unsigned)((b * 21 + lp) * 128) : 0x80000000u; }
                const unsigned so = t < 6 ? ((t & 1) ? 0x70000000u : (unsigned)(2 + (t >> 1)) * 128 * (FF * 2)) : t < 11 ? (unsigned)(t - 6) * 64 * (DM * 2) : t == 11 ? 0u : 0x70000000u;
                dma4(t < 6 ? srd_w : t < 11 ? make_srd(wd, FF * DM * 2) : make_srd(wo, DM * DM * 2), vo_pf, so, lds0 + LDS_PF);
            }
        };
        gemm_step<10, true, 7, 1>(acc, ca, bB, bA, L + a_off, L + bo, [&](int i) { stage1(2, 2 * ST1, i); });
        unsigned slot = ST1, slot2 = 0;
        if (w >= 6) { WAIT_BAR(7); } else { WAIT_BAR(6); }
        gemm_step<10, false, 7, 1>(acc, ca, bA, bB, L + slot + a_off, L + slot + bo, [&](int i) { stage1(3, slot2, i); });
        slot = 2 * ST1; slot2 = ST1;
#pragma unroll 1
        for (int j = 2; j < 16; j += 2) {
            if (w >= 6) { WAIT_BAR(8); } else { WAIT_BAR(6); }
            gemm_step<10, false, 7, 1>(acc, ca, bB, bA, L + slot + a_off, L + slot + bo, [&](int i) { stage1(j + 2, slot2, i); });
            slot = (slot == 2 * ST1) ? 0 : slot + ST1; slot2 = (slot2 == 2 * ST1) ? 0 : slot2 + ST1;
            if (w >= 6) { WAIT_BAR(8); } else { WAIT_BAR(6); }
            gemm_step<10, false, 7, 1>(acc, ca, bA, bB, L + slot + a_off, L + slot + bo, [&](int i) { stage1(j + 3, slot2, i); });
            slot = (slot == 2 * ST1) ? 0 : slot + ST1; slot2 = (slot2 == 2 * ST1) ? 0 : slot2 + ST1;
        }
#pragma unroll
        for (int n = 0; n < 4; ++n) bA[n] = bB[n];
#pragma unroll 1
        for (int j = 16; j < 18; ++j) {
            if (j == 16 && w >= 6) { WAIT_BAR(8); } else if (j == 16) { WAIT_BAR(6); } else { WAIT_BAR(5); }
            gemm_step<10, false, 5, 1, 3, true>(acc, ca, bA, bB, L + slot + a_off, L + slot + bo, [&](int i) { piece2(j - 16, j == 16 ? LDS_D0 : LDS_D1, i); });
#pragma unroll
            for (int n = 0; n < 4; ++n) bA[n] = bB[n];
            slot += ST1;
        }
    }
    WAIT_BAR(0);
    gemm_flush<10>(acc, ca, bA);
    u32x4 wov; bool wo_act; unsigned wo_off;
    {
        const int l = fresh_lane(), tw = w * 64 + l;
        const int cgp = s & 7, wp = 4 * (r * 2 + (s >> 3)) + ((min(max(tw, 192), 447) - 192) >> 6);
        wo_act = tw >= 192 && tw < 448 && wp < 120;
        const int m0 = cgp < 4 ? 5 * cgp : 20 + 4 * (cgp - 4), wpc = min(wp, 119);
        const int pp = wpc / 40, js = (wpc % 40) >> 1, R = wpc & 1, i = l >> 2, ch = (l & 3) ^ (2 * ((i >> 2) & 1));
        const int kp = 32 * js + 8 * (i >> 2) + (i & 3) + 4 * R, sp = kp / 40, d = kp - 40 * sp, hd = d >= 20 ? 1 : 0, dd = d - 20 * hd;
        const int row = 36 * sp + 18 * hd + (dd < 18 ? dd : 17), col = min(16 * (m0 + 2 * pp) + 8 * ch, DM - 8);
        wov = *(const u32x4*)(wo + (size_t)row * DM + col);
        wo_off = wo_act ? (unsigned)(cgp * WOIMG + wp * 1024 + l * 16) : 0xfffffff0u;
    }
    auto h_tiles = [&](auto CG0, auto CG1) {
        const int l = fresh_lane(), tok = l & 15, g4 = l >> 4;
#pragma unroll
        for (int cg = CG0; cg < CG1; ++cg)
#pragma unroll
            for (int pr = 0; pr < 2; ++pr) {
                float h[4];
#pragma unroll
                for (int e = 0; e < 4; ++e) {
                    auto sw = __builtin_amdgcn_permlane32_swap(__float_as_uint(acc[cg][2 * pr][e]), __float_as_uint(acc[cg][2 * pr + 1][e]), false, false);
                    h[e] = __int_as_float(max((int)sw[0], 0)) * __uint_as_float(sw[1]);
                }
                const int n = 4 * tg + 2 * pr + (l >> 5);
                u32x2 o; o[0] = cvtpk(h[0], h[1]); o[1] = cvtpk(h[2], h[3]);
                *(LAS u32x2*)(L + LDS_H + ((8 - cg) * 8 + n) * 1024 + (fg * 16 + tok) * 16 + 8 * (g4 & 1)) = o;
            }
    };
    float kn2 = 0.f;
    if ((fg & 1) && tg == (s >> 3)) {
        const int l = fresh_lane(), tok = l & 15, g4 = l >> 4;
        const int e = s >> 1;
        f32x4 cv = acc[9][0];
        if ((e & 3) == 1) cv = acc[9][1]; else if ((e & 3) == 2) cv = acc[9][2]; else if ((e & 3) == 3) cv = acc[9][3];
        const int c0 = (fg == 1 ? 0 : 12) + 4 * (g4 - 1);
        const bool act = g4 >= 1 && c0 < 18;
        const unsigned p0 = cvtpk(cv[0], cv[1]), p1 = cvtpk(cv[2], cv[3]);
        const int kt = 4 * r + (e >> 1);
        char* kvb = ws + WS_KV + ((kt >> 3) & 3) * KVCH; const int lt = 8 * (kt >> 5) + (kt & 7);
        if ((s & 1) == 0) {
            const int key = 16 * (e & 1) + tok;
            if (act) {
                if (c0 < 16) st_agent64(kvb + lt * 1024 + ((c0 >> 3) * 32 + key) * 16 + (c0 & 4) * 2, p0, p1);
                else __hip_atomic_store((GAS unsigned*)(kvb + KV_K2 + lt * 128 + key * 4), p0, RLX_AGENT);
            }
            float n2 = 0.f;
            if (act) { n2 = bflo(p0) * bflo(p0) + bfhi(p0) * bfhi(p0); if (c0 < 16) n2 += bflo(p1) * bflo(p1) + bfhi(p1) * bfhi(p1); }
            kn2 = n2;
        } else {
            if (fg == 1 && l < 2) {
                char* op = kvb + KV_V + lt * 1216 + (e & 1) * 608 + l * 304 + 18 * 16;
                st_agent64(op, 0x3f803f80u, 0x3f803f80u); st_agent64(op + 8, 0x3f803f80u, 0x3f803f80u);
            }
            const int hv = (tok >> 2) & 1, jj = 4 * (tok >> 3) + (tok & 3);
            GAS u16* vb = (GAS u16*)(kvb + KV_V + lt * 1216 + (e & 1) * 608 + hv * 304 + jj * 2);
            if (act) {
                __hip_atomic_store(vb + (c0 + 0) * 8, (u16)(p0 & 0xffff), RLX_AGENT);
                __hip_atomic_store(vb + (c0 + 1) * 8, (u16)(p0 >> 16), RLX_AGENT);
                if (c0 < 16) { __hip_atomic_store(vb + (c0 + 2) * 8, (u16)(p1 & 0xffff), RLX_AGENT);
                               __hip_atomic_store(vb + (c0 + 3) * 8, (u16)(p1 >> 16), RLX_AGENT); }
            }
        }
    }
    h_tiles(std::integral_constant<int, 0>{}, std::integral_constant<int, 9>{});
    {
        const int l = fresh_lane(), tok = l & 15, g4 = l >> 4;
        const float qs = 0.23570226039551584f * 1.4426950408889634f;
        const int hh = fg >> 1;
        unsigned p0[4], p1[4];
#pragma unroll
        for (int n = 0; n < 4; ++n) { p0[n] = cvtpk(acc[9][n][0] * qs, acc[9][n][1] * qs); p1[n] = cvtpk(acc[9][n][2] * qs, acc[9][n][3] * qs); }
        if ((fg & 1) == 0) {
            LAS char* qd = L + LDS_Q1 + (hh * 4 + 2 * tg) * 1024 + ((g4 >> 1) * 32 + tok) * 16 + (g4 & 1) * 8;
#pragma unroll
            for (int n = 0; n < 4; ++n) *(LAS u32x2*)(qd + (n >> 1) * 1024 + (n & 1) * 256) = (u32x2){p0[n], p1[n]};
        } else if (g4 == 0) {
            LAS char* qd = L + LDS_Q2 + (hh * 4 + 2 * tg) * 128 + tok * 4;
#pragma unroll
            for (int n = 0; n < 4; ++n) *(LAS unsigned*)(qd + (n >> 1) * 128 + (n & 1) * 64) = p0[n];
        }
    }
    {
        const int l = fresh_lane(), tw = w * 64 + l;
        const __amdgpu_buffer_rsrc_t rw = __builtin_amdgcn_make_buffer_rsrc(ws + WS_WO, 0, 8 * WOIMG, 0x00020000);
        __builtin_amdgcn_raw_buffer_store_b128(wov, rw, wo_off, 0, 16);
        const __amdgpu_buffer_rsrc_t ro = __builtin_amdgcn_make_buffer_rsrc(ws + WS_O + (size_t)wgi * 128 * 80, 0, 128 * 80, 0x00020000);
        const u32x4 e = (u32x4){O_EMPTY, O_EMPTY, O_EMPTY, O_EMPTY};
        __builtin_amdgcn_raw_buffer_store_b128(e, ro, tw * 16, 0, 16);
        __builtin_amdgcn_raw_buffer_store_b128(e, ro, (tw + 512) * 16, 0, 16);
    }
    if ((fg & 1) && tg == (s >> 3)) {
        kn2 += xor_lane<16>(kn2); kn2 = wave_max(half_sum(kn2));
        if (fresh_lane() == 0) *(LAS float*)(L + LDS_KM + (fg == 1 ? 0 : 4)) = kn2;
    }
    LBAR();
    const i32x4 srd_kv = make_srd(ws + WS_KV, 4 * KVCH);
    auto piece_kv = [&](int c, int i) {
        const int p = min(w + 8 * i, 36);
        dma16_sc1(srd_kv, vo_x, c * KVCH + p * 1024, lds0 + (c == 0 ? LDS_KV0 : (c - 1) * KVCH) + p * 1024);
    };
#pragma unroll
    for (int m = 0; m < 9; ++m)
#pragma unroll
        for (int n = 0; n < 4; ++n) acc[m][n] = (f32x4){0.f, 0.f, 0.f, 0.f};
    {
        LAS const char* hb = L + LDS_H + 8 * 8192 + 4 * tg * 1024 + l * 16;
        gemm_step<9, true, 0, 1>(acc, ca, bB, bA, L + LDS_D0 + a_off, hb, [](int) {});
#pragma unroll 1
        for (int j = 1; j < 8; ++j) {
            WAIT_BAR(0);
            if (j == 1 && w == 0 && fresh_lane() == 0) {
                st_agent64(flags + FL_KV + wgi * 16, tag, *(LAS const unsigned*)(L + LDS_KM));
                st_agent64(flags + FL_KV + wgi * 16 + 8, tag, *(LAS const unsigned*)(L + LDS_KM + 4));
            }
            const unsigned cur = (j & 1) ? LDS_D1 : LDS_D0, nxt = (j & 1) ? LDS_D0 : LDS_D1;
            gemm_step<9, false, 5, 2, 3, true>(acc, ca, bA, bB, L + cur + a_off, hb - j * 8192, [&](int i) { piece2(j + 1, nxt, i); });
#pragma unroll
            for (int n = 0; n < 4; ++n) bA[n] = bB[n];
        }
        if (w == 0) {
            const int lk = fresh_lane();
            const __amdgpu_buffer_rsrc_t rk = __builtin_amdgcn_make_buffer_rsrc(flags + FL_KV, 0, 4096, 0x00020000);
            float ma, mb;
            for (unsigned spins = 0;; ++spins) {
                bool ok = true; ma = 0.f; mb = 0.f;
#pragma unroll
                for (int i = 0; i < 4; ++i) {
                    const u32x4 v = __builtin_amdgcn_raw_buffer_load_b128(rk, (i * 64 + lk) * 16, 0, 16);
                    ok = ok && v[0] == tag && v[2] == tag; ma = fmaxf(ma, __uint_as_float(v[1])); mb = fmaxf(mb, __uint_as_float(v[3]));
                }
                if (__all(ok)) break;
                if (spins > (1u << 19)) { __hip_atomic_store((GAS unsigned*)(flags + FL_TMO), tag, RLX_AGENT); break; }
                __builtin_amdgcn_s_sleep(1);
            }
            ma = wave_max(ma); mb = wave_max(mb);
            if (lk == 0) *(LAS float*)(L + LDS_KM + 8) = ma + mb;
        }
        WAIT_BAR(0);
        gemm_step<9, false, 5, 2>(acc, ca, bA, bB, L + LDS_D0 + a_off, hb - 8 * 8192, [&](int i) { piece_kv(0, i); });
        gemm_flush<9>(acc, ca, bB);
        SB(); FENCE();
    }
    const int la = fresh_lane();
#define l la
    const int hh = w >> 2, kh = (w >> 1) & 1, cp = w & 1;
#define h5 (l >> 5)
    bf16x8 q1[2]; u32x2 q2[2];
#pragma unroll
    for (int c = 0; c < 2; ++c) {
        q1[c] = lds_b128(L + LDS_Q1 + (hh * 4 + 2 * cp + c) * 1024 + l * 16);
        q2[c][0] = *(LAS const unsigned*)(L + LDS_Q2 + (hh * 4 + 2 * cp + c) * 128 + (l & 31) * 4);
    }
    u32x4 pk[18];
#pragma unroll
    for (int m = 0; m < 9; ++m)
#pragma unroll
        for (int pr = 0; pr < 2; ++pr) {
            u32x4 o; o[0] = cvtpk(acc[m][2 * pr][0], acc[m][2 * pr][1]); o[1] = cvtpk(acc[m][2 * pr][2], acc[m][2 * pr][3]);
            o[2] = cvtpk(acc[m][2 * pr + 1][0], acc[m][2 * pr + 1][1]); o[3] = cvtpk(acc[m][2 * pr + 1][2], acc[m][2 * pr + 1][3]);
            pk[m * 2 + pr] = o;
        }
    {
        const float kmax2 = *(LAS const float*)(L + LDS_KM + 8);
#pragma unroll
        for (int c = 0; c < 2; ++c) {
            const u32x4 a = __builtin_bit_cast(u32x4, q1[c]); const unsigned t = h5 ? 0u : q2[c][0];
            float n2 = bflo(t) * bflo(t) + bfhi(t) * bfhi(t);
#pragma unroll
            for (int e = 0; e < 4; ++e) n2 += bflo(a[e]) * bflo(a[e]) + bfhi(a[e]) * bfhi(a[e]);
            n2 = half_sum(n2);
            const float cb = sqrtf(n2 * kmax2) * 1.001f + 0.01f;
            const float chi = rbf(cb), clo = cb - chi;
            q2[c][0] = t; q2[c][1] = h5 ? 0u : cvtpk(-chi, -clo);
        }
    }
    if (w == 0 && l < 40) *(LAS u32x4*)(L + LDS_ZERO + l * 16) = (u32x4){0u, 0u, 0u, 0u};
    if (w == 0 && l == 40) *(LAS unsigned*)(L + LDS_CTL) = 0u;
    WAIT_BAR(0);
#undef l
    const int lb = fresh_lane();
#define l lb
    const unsigned kone = h5 ? 0u : 0x3f803f80u;
    const bool vrow = (l & 31) < 19;
    const unsigned k1_base = 8 * kh * 1024 + l * 16, k2_base = KV_K2 + 8 * kh * 128 + (l & 31) * 4;
    const unsigned v_base = vrow ? KV_V + 8 * kh * 1216 + h5 * 304 + (l & 31) * 16 : LDS_ZERO, v_mul = vrow ? 1u : 0u;
    const int tgf = s >> 3, cgp = s & 7, nmt = cgp < 4 ? 5 : 4, m0 = cgp < 4 ? 5 * cgp : 20 + 4 * (cgp - 4);
    const int pr = w & 1, qk = w >> 1;
    const i32x4 srd_wo = make_srd(ws + WS_WO + (size_t)cgp * WOIMG, WOIMG);
    auto piece_wo = [&](int p) { dma16_sc1(srd_wo, vo_x, p * 1024, lds0 + wo_lds(p)); };
    const __amdgpu_buffer_rsrc_t rpart = __builtin_amdgcn_make_buffer_rsrc(ws + WS_PART + (size_t)r * 16 * 8 * 18 * 1024, 0, 16 * 8 * 18 * 1024, 0x00020000);
    float own[11];
    u32x4 pl[5][4];

    f32x16 o0, o1, sX, sY; bf16x8 kA, kB, v0A, v1A, v0B, v1B; unsigned k2A, k2B; u32x4 p0, p1; float e3[4];
    unsigned aK, aK2, aV;
#define SLOT(SC, SN, KN, K2N, CN, OP, V1P, OC, V0C) do { float ea[4], eb[4]; \
        SN = MFMA32(KN, q1[CN], (f32x16){}); SB(); \
        p1[2] = cvtpk(e3[0], e3[1]); p1[3] = cvtpk(e3[2], e3[3]); SB(); \
        ea[0] = EXP2(SC[0]); ea[1] = EXP2(SC[1]); ea[2] = EXP2(SC[2]); ea[3] = EXP2(SC[3]); SB(); \
        OP = MFMA32(V1P, __builtin_bit_cast(bf16x8, p1), OP); SB(); \
        p0[0] = cvtpk(ea[0], ea[1]); p0[1] = cvtpk(ea[2], ea[3]); SB(); \
        eb[0] = EXP2(SC[4]); eb[1] = EXP2(SC[5]); eb[2] = EXP2(SC[6]); eb[3] = EXP2(SC[7]); SB(); \
        SN = MFMA32K8(__builtin_bit_cast(s16x4, ((u32x2){K2N, kone})), __builtin_bit_cast(s16x4, q2[CN]), SN); SB(); \
        p0[2] = cvtpk(eb[0], eb[1]); p0[3] = cvtpk(eb[2], eb[3]); SB(); \
        ea[0] = EXP2(SC[8]); ea[1] = EXP2(SC[9]); ea[2] = EXP2(SC[10]); ea[3] = EXP2(SC[11]); SB(); \
        OC = MFMA32(V0C, __builtin_bit_cast(bf16x8, p0), OC); SB(); \
        p1[0] = cvtpk(ea[0], ea[1]); p1[1] = cvtpk(ea[2], ea[3]); SB(); \
        e3[0] = EXP2(SC[12]); e3[1] = EXP2(SC[13]); e3[2] = EXP2(SC[14]); e3[3] = EXP2(SC[15]); SB(); } while (0)
#define PAIR(DK, DK2, DV, H0, H1, H2, H3) do { \
        kB = lds_b128(L + aK + 1024); k2B = *(LAS const unsigned*)(L + aK2 + 128); v0B = lds_b128(L + aV + v_mul * 1216); SB(); \
        H0; SLOT(sX, sY, kA, k2A, 1, o1, v1B, o0, v0A); \
        v1B = lds_b128(L + aV + v_mul * 1216 + 608); SB(); \
        H1; SLOT(sY, sX, kB, k2B, 0, o0, v1A, o1, v0A); \
        aK += (DK); aK2 += (DK2); aV += v_mul * (DV); \
        kA = lds_b128(L + aK); k2A = *(LAS const unsigned*)(L + aK2); v0A = lds_b128(L + aV); SB(); \
        H2; SLOT(sX, sY, kB, k2B, 1, o1, v1A, o0, v0B); \
        v1A = lds_b128(L + aV + 608); SB(); \
        H3; SLOT(sY, sX, kA, k2A, 0, o0, v1B, o1, v0B); } while (0)
#define PAIR_IN(H0, H1, H2, H3) PAIR(2048, 256, 2432, H0, H1, H2, H3)
#define PAIR_JUMP(J, H0, H1, H2, H3) PAIR((J) - 6 * 1024, (J) - 6 * 128, (J) - 6 * 1216, H0, H1, H2, H3)
#define NOP_ ((void)0)
#define ATT_INIT() do { o0 = (f32x16){}; o1 = (f32x16){}; aK = k1_base + LDS_KV0; aK2 = k2_base + LDS_KV0; aV = v_base + v_mul * LDS_KV0; \
        kA = lds_b128(L + aK); k2A = *(LAS const unsigned*)(L + aK2); v0A = lds_b128(L + aV); v1A = lds_b128(L + aV + 608); v1B = v1A; \
        sX = MFMA32(kA, q1[0], (f32x16){}); sX = MFMA32K8(__builtin_bit_cast(s16x4, ((u32x2){k2A, kone})), __builtin_bit_cast(s16x4, q2[0]), sX); \
        p1 = (u32x4){0u, 0u, 0u, 0u}; e3[0] = e3[1] = e3[2] = e3[3] = 0.f; } while (0)
#define ATT_FIN() do { p1[2] = cvtpk(e3[0], e3[1]); p1[3] = cvtpk(e3[2], e3[3]); o1 = MFMA32(v1B, __builtin_bit_cast(bf16x8, p1), o1); } while (0)
    constexpr int J01 = 0 - LDS_KV0, J12 = KVCH;
    {
        const __amdgpu_buffer_rsrc_t rp = __builtin_amdgcn_make_buffer_rsrc(ws + WS_PART + (size_t)(r * 16 + s) * 8 * 18 * 1024, 0, 8 * 18 * 1024, 0x00020000);
#define KV(C, I) do { piece_kv(C, I); SB(); } while (0)
#define ST(I) do { __builtin_amdgcn_raw_buffer_store_b128(pk[I], rp, vo_x, (w * 18 + (I)) * 1024, 16); SB(); } while (0)
#define WO(P0, I) do { piece_wo(min((P0) + w + 8 * (I), (P0) + 36)); SB(); } while (0)
#define PL(T, SP) do { const int m_ = m0 + ((T) < nmt ? (T) : 0); \
            pl[T][SP] = __builtin_amdgcn_raw_buffer_load_b128(rpart, vo_x, ((((4 * qk + (SP)) * 8 + tgf * 4 + (m_ & 3)) * 9 + (m_ >> 2)) * 2 + pr) * 1024, 16); SB(); } while (0)
#define PL2(A) do { PL((A) >> 2, (A) & 3); PL(((A) + 1) >> 2, ((A) + 1) & 3); } while (0)
        ATT_INIT();
        PAIR_IN(KV(1, 0), KV(1, 1), KV(1, 2), KV(1, 3));
        PAIR_IN(KV(1, 4), ST(0), KV(2, 0), ST(1));
        PAIR_IN(KV(2, 1), ST(2), KV(2, 2), ST(3));
        WAIT_BAR(7);
        PAIR_JUMP(J01, KV(2, 3), ST(4), KV(2, 4), ST(5));
        PAIR_IN(KV(3, 0), ST(6), KV(3, 1), ST(7));
        PAIR_IN(KV(3, 2), ST(8), KV(3, 3), ST(9));
        PAIR_IN(KV(3, 4), ST(10), NOP_, ST(11));
        WAIT_BAR(12);
        PAIR_JUMP(J12, WO(0, 0), ST(12), WO(0, 1), ST(13));
        PAIR_IN(WO(0, 2), ST(14), WO(0, 3), ST(15));
        PAIR_IN(WO(0, 4), ST(16), NOP_, ST(17));
        PAIR_IN(NOP_, NOP_, NOP_, NOP_);
        WAIT_BAR(0);
        if (w == 0 && fresh_lane() == 0) __hip_atomic_store((GAS unsigned*)(flags + FL_P + wgi * 4), tag, RLX_AGENT);
        PAIR_JUMP(J12, WO(37, 0), WO(37, 1), WO(37, 2), WO(37, 3));
        const unsigned pflag = __hip_atomic_load((GAS unsigned*)(flags + FL_P + (r * 16 + (l & 15)) * 4), RLX_AGENT);
        PAIR_IN(WO(37, 4), NOP_, NOP_, NOP_);
        LBAR();
        if (!__all(pflag == tag)) wait_tags(flags, FL_P + (r * 16 + (l & 15)) * 4, true, tag);
#define WP(I, A) do { WO(74, I); PL((A) >> 2, (A) & 3); } while (0)
        PAIR_IN(WP(0, 0), WP(1, 1), WP(2, 2), WP(3, 3));
        PAIR_IN(WP(4, 4), PL2(5), PL2(7), PL2(9));
        PAIR(0, 0, 0, PL2(11), PL2(13), PL2(15), PL2(17));
        PL(4, 3);
        ATT_FIN();
    }
#undef l
    const int lc = fresh_lane();
#define l lc
    for (int attempt = 0;; ++attempt) {
        if (attempt == 1) {
            const unsigned kone = h5 ? 0u : 0x3f803f80u;
            const bool vrow = (l & 31) < 19;
            const unsigned k1_base = 8 * kh * 1024 + l * 16, k2_base = KV_K2 + 8 * kh * 128 + (l & 31) * 4;
            const unsigned v_base = vrow ? KV_V + 8 * kh * 1216 + h5 * 304 + (l & 31) * 16 : LDS_ZERO, v_mul = vrow ? 1u : 0u;
            WAIT_BAR(0);
#pragma unroll
            for (int c = 0; c < 4; ++c)
#pragma unroll
                for (int i = 0; i < 5; ++i) piece_kv(c, i);
            if (w == 0 && l < 40) *(LAS u32x4*)(L + LDS_ZERO + l * 16) = (u32x4){0u, 0u, 0u, 0u};
            WAIT_BAR(0);
            float m[2] = {-INFINITY, -INFINITY};
            for (int i = 0; i < 32; ++i) {
                const unsigned to = (i >> 3) == 0 ? LDS_KV0 : ((i >> 3) - 1) * KVCH;
                const bf16x8 k1 = lds_b128(L + k1_base + to + (i & 7) * 1024);
                const u32x2 k2 = {*(LAS const unsigned*)(L + k2_base + to + (i & 7) * 128), kone};
#pragma unroll
                for (int c = 0; c < 2; ++c) {
                    const u32x2 qz = {q2[c][0], 0u};
                    f32x16 sc = MFMA32(k1, q1[c], (f32x16){});
                    sc = MFMA32K8(__builtin_bit_cast(s16x4, k2), __builtin_bit_cast(s16x4, qz), sc);
#pragma unroll
                    for (int e = 0; e < 16; ++e) m[c] = fmaxf(m[c], sc[e]);
                }
            }
            LAS float* mx = (LAS float*)(L + LDS_MX);
#pragma unroll
            for (int c = 0; c < 2; ++c) { m[c] = half_max(m[c]); if (l < 32) mx[(w * 2 + c) * 32 + l] = m[c]; }
            LBAR();
#pragma unroll
            for (int c = 0; c < 2; ++c) {
                const float mm = fmaxf(m[c], mx[((w ^ 2) * 2 + c) * 32 + (l & 31)]);
                const float chi = rbf(mm), clo = mm - chi;
                q2[c][1] = h5 ? 0u : cvtpk(-chi, -clo);
            }
            o0 = (f32x16){}; o1 = (f32x16){};
            for (int i = 0; i < 32; ++i) {
                const unsigned to = (i >> 3) == 0 ? LDS_KV0 : ((i >> 3) - 1) * KVCH;
                const bf16x8 k1 = lds_b128(L + k1_base + to + (i & 7) * 1024);
                const u32x2 k2 = {*(LAS const unsigned*)(L + k2_base + to + (i & 7) * 128), kone};
                const unsigned va = v_base + v_mul * (to + (i & 7) * 1216);
                const bf16x8 v0 = lds_b128(L + va), v1 = lds_b128(L + va + 608);
#pragma unroll
                for (int c = 0; c < 2; ++c) {
                    f32x16 sc = MFMA32(k1, q1[c], (f32x16){});
                    sc = MFMA32K8(__builtin_bit_cast(s16x4, k2), __builtin_bit_cast(s16x4, q2[c]), sc);
                    u32x4 pa, pb;
#pragma unroll
                    for (int e = 0; e < 4; ++e) { pa[e] = cvtpk(EXP2(sc[2 * e]), EXP2(sc[2 * e + 1])); pb[e] = cvtpk(EXP2(sc[8 + 2 * e]), EXP2(sc[9 + 2 * e])); }
                    if (c == 0) { o0 = MFMA32(v0, __builtin_bit_cast(bf16x8, pa), o0); o0 = MFMA32(v1, __builtin_bit_cast(bf16x8, pb), o0); }
                    else { o1 = MFMA32(v0, __builtin_bit_cast(bf16x8, pa), o1); o1 = MFMA32(v1, __builtin_bit_cast(bf16x8, pb), o1); }
                }
            }
        }
        LAS float* red = (LAS float*)(L + LDS_RED);
        LBAR();
        if (attempt == 0) {
            piece_wo(111 + w); if (w == 0) piece_wo(119);
        } else {
#pragma unroll
            for (int q = 0; q < 15; ++q) piece_wo(w * 15 + q);
        }
#pragma unroll
        for (int e = 0; e < 11; ++e) { red[(w * 11 + e) * 64 + l] = kh ? o0[e] : o1[e]; own[e] = kh ? o1[e] : o0[e]; }
        LBAR();
#pragma unroll
        for (int e = 0; e < 11; ++e) own[e] += red[((w ^ 2) * 11 + e) * 64 + l];
        const float lsum = half_lo(own[10]);
        if (!(lsum >= 8.67e-19f && lsum < 3.0e38f) && attempt == 0) *(LAS unsigned*)(L + LDS_CTL) = 1u;
        const float rl = 1.0f / lsum;
        LAS char* ot = L + LDS_OT + (32 * (2 * cp + kh) + (l & 31)) * 80 + 40 * hh;
        *(LAS u32x2*)(ot + 8 * h5) = (u32x2){cvtpk(own[0] * rl, own[1] * rl), cvtpk(own[2] * rl, own[3] * rl)};
        *(LAS u32x2*)(ot + 16 + 8 * h5) = (u32x2){cvtpk(own[4] * rl, own[5] * rl), cvtpk(own[6] * rl, own[7] * rl)};
        if (h5 == 0) *(LAS u32x2*)(ot + 32) = (u32x2){cvtpk(own[8] * rl, own[9] * rl), 0u};
        LBAR();
        const unsigned isbad = *(LAS const unsigned*)(L + LDS_CTL);
        if (!(isbad && attempt == 0)) break;
    }
    {
        const __amdgpu_buffer_rsrc_t rw = __builtin_amdgcn_make_buffer_rsrc(ws + WS_O + (size_t)(r * 16 + s) * 128 * 80, 0, 128 * 80, 0x00020000);
        __builtin_amdgcn_raw_buffer_store_b128(*(LAS const u32x4*)(L + LDS_OT + w * 1024 + l * 16), rw, l * 16, w * 1024, 16);
        if (w < 2) __builtin_amdgcn_raw_buffer_store_b128(*(LAS const u32x4*)(L + LDS_OT + (8 + w) * 1024 + l * 16), rw, l * 16, (8 + w) * 1024, 16);
    }
    {
        const int tok = l & 15, g4 = l >> 4, q4 = (l >> 2) & 3, p4 = l & 3;
        const __amdgpu_buffer_rsrc_t ro = __builtin_amdgcn_make_buffer_rsrc(ws + WS_O + (size_t)r * 16 * 128 * 80, 0, 16 * 128 * 80, 0x00020000);
        u32x4 bf[5][2]; unsigned bo[5];
#pragma unroll
        for (int i = 0; i < 5; ++i) {
            const int kp = 32 * (5 * qk + i) + 8 * g4, sp = kp / 40, d = kp - 40 * sp;
            bo[i] = (sp * 128 + 64 * tgf + 32 * pr + tok) * 80 + d * 2;
#pragma unroll
            for (int nn = 0; nn < 2; ++nn) bf[i][nn] = __builtin_amdgcn_raw_buffer_load_b128(ro, bo[i], nn * 16 * 80, 16);
        }
        SB();
        u32x2 xv[3];
#pragma unroll
        for (int u = 0; u < 3; ++u) {
            const int tt = min(qk + 4 * u, 2 * nmt - 1);
            xv[u] = *(const u32x2*)(x + (size_t)(128 * r + 64 * tgf + 32 * pr + 16 * (tt & 1) + tok) * DM + 16 * (m0 + (tt >> 1)) + 4 * g4);
        }
        f32x4 fa[5][2];
#pragma unroll
        for (int t = 0; t < 5; ++t) {
            fa[t][0] = (f32x4){0.f, 0.f, 0.f, 0.f}; fa[t][1] = (f32x4){0.f, 0.f, 0.f, 0.f};
#pragma unroll
            for (int sp = 0; sp < 4; ++sp) {
                const u32x4 p = pl[t][sp];
                fa[t][0][0] += bflo(p[0]); fa[t][0][1] += bfhi(p[0]); fa[t][0][2] += bflo(p[1]); fa[t][0][3] += bfhi(p[1]);
                fa[t][1][0] += bflo(p[2]); fa[t][1][1] += bfhi(p[2]); fa[t][1][2] += bflo(p[3]); fa[t][1][3] += bfhi(p[3]);
            }
        }
#pragma unroll
        for (int t = 0; t < 5; ++t) asm volatile("" : "+v"(fa[t][0]), "+v"(fa[t][1]));
        WAIT_BAR(0);
        bf16x8 af[5][5];
        {
            const unsigned ao = (4 * g4 + q4) * 64 + 8 * (p4 & 1);
#pragma unroll
            for (int t = 0; t < 5; ++t)
#pragma unroll
                for (int i = 0; i < 5; ++i) {
                    const int p = ((t < nmt ? t : 0) >> 1) * 40 + (5 * qk + i) * 2;
                    const unsigned sw = ao + 16 * ((2 * (t & 1) + (p4 >> 1)) ^ (2 * (g4 & 1)));
                    const s16x4 lo = lds_tr(L + wo_lds(p) + sw), hi = lds_tr(L + wo_lds(p + 1) + sw);
                    af[t][i] = __builtin_shufflevector(lo, hi, 0, 1, 2, 3, 4, 5, 6, 7);
                }
        }
        LBAR();
        unsigned pend = 0x3ffu;
        for (unsigned spins = 0;; ++spins) {
#pragma unroll
            for (int i = 0; i < 5; ++i)
#pragma unroll
                for (int nn = 0; nn < 2; ++nn)
                    if ((pend & (1u << (2 * i + nn))) && __all(bf[i][nn][0] != O_EMPTY && bf[i][nn][1] != O_EMPTY && bf[i][nn][2] != O_EMPTY && bf[i][nn][3] != O_EMPTY)) pend &= ~(1u << (2 * i + nn));
            if (pend == 0u) break;
            if (spins > (1u << 17)) { __hip_atomic_store((GAS unsigned*)(flags + FL_TMO), tag, RLX_AGENT); break; }
            __builtin_amdgcn_s_sleep(8);
#pragma unroll
            for (int i = 0; i < 5; ++i)
#pragma unroll
                for (int nn = 0; nn < 2; ++nn)
                    if (pend & (1u << (2 * i + nn))) bf[i][nn] = __builtin_amdgcn_raw_buffer_load_b128(ro, bo[i], nn * 16 * 80, 16);
        }
#pragma unroll
        for (int i = 0; i < 5; ++i)
#pragma unroll
            for (int t = 0; t < 5; ++t) {
                fa[t][0] = MFMA16(af[t][i], __builtin_bit_cast(bf16x8, bf[i][0]), fa[t][0]); fa[t][1] = MFMA16(af[t][i], __builtin_bit_cast(bf16x8, bf[i][1]), fa[t][1]);
            }
        LAS f32x4* ex = (LAS f32x4*)L;
#pragma unroll
        for (int t = 0; t < 5; ++t) { ex[(w * 10 + 2 * t) * 64 + l] = fa[t][0]; ex[(w * 10 + 2 * t + 1) * 64 + l] = fa[t][1]; }
        LBAR();
#pragma unroll
        for (int u = 0; u < 3; ++u) {
            const int tt = qk + 4 * u;
            if (tt < 2 * nmt) {
                f32x4 v = ex[((0 * 2 + pr) * 10 + tt) * 64 + l];
#pragma unroll
                for (int k = 1; k < 4; ++k) v += ex[((k * 2 + pr) * 10 + tt) * 64 + l];
                const size_t off = (size_t)(128 * r + 64 * tgf + 32 * pr + 16 * (tt & 1) + tok) * DM + 16 * (m0 + (tt >> 1)) + 4 * g4;
                *(u32x2*)(out + off) = (u32x2){cvtpk(v[0] + bflo(xv[u][0]), v[1] + bfhi(xv[u][0])), cvtpk(v[2] + bflo(xv[u][1]), v[3] + bfhi(xv[u][1]))};
            }
        }
    }
#undef l
#undef h5
    asm volatile("fused_block_code_end:");
}

extern "C" void kernel_launch(void* const* d_in, const int* in_sizes, int n_in, void* d_out, int out_size, void* d_ws, size_t ws_size, hipStream_t stream) {
    const u16* x = (const u16*)d_in[0]; const u16* wq = (const u16*)d_in[1]; const u16* wk = (const u16*)d_in[2]; const u16* wv = (const u16*)d_in[3];
    const u16* wo = (const u16*)d_in[4]; const u16* wg = (const u16*)d_in[6]; const u16* wu = (const u16*)d_in[7]; const u16* wd = (const u16*)d_in[8];
    fused_block<<<256, 512, 0, stream>>>(x, wq, wk, wv, wo, wg, wu, wd, (char*)d_ws, (u16*)d_out);
}
```

```cpp
#include <hip/hip_runtime.h>
#include <hip/hip_bf16.h>
#include <stdint.h>
#include <type_traits>

typedef unsigned short u16;
typedef short bf16x8 __attribute__((ext_vector_type(8)));
typedef short s16x4 __attribute__((ext_vector_type(4)));
typedef float f32x4 __attribute__((ext_vector_type(4)));
typedef float f32x16 __attribute__((ext_vector_type(16)));
typedef int i32x4 __attribute__((ext_vector_type(4)));
typedef unsigned u32x4 __attribute__((ext_vector_type(4)));
typedef unsigned u32x2 __attribute__((ext_vector_type(2)));
typedef __bf16 bf16x2_t __attribute__((ext_vector_type(2)));
typedef float f32x2 __attribute__((ext_vector_type(2)));
typedef short v4i16_t __attribute__((ext_vector_type(4)));
extern "C" __device__ unsigned long long amd_dispatch_id() __asm("llvm.amdgcn.dispatch.id");
#define LAS __attribute__((address_space(3)))
#define GAS __attribute__((address_space(1)))
#define DI __device__ __forceinline__

constexpr int SEQ = 2048, DM = 576, NH = 32, DH = 18, FF = 4608;

constexpr int KVCH = 37888;
constexpr int KV_V = 16384, KV_K2 = 35840;
constexpr int WOIMG = 120 * 1024;
constexpr size_t WS_FLAGS = 0;
constexpr size_t WS_XN    = 16384;
constexpr size_t WS_WQX   = WS_XN + (size_t)SEQ * DM * 2;
constexpr size_t WS_WO    = WS_WQX + (size_t)DM * 1024 * 2;
constexpr size_t WS_KV    = WS_WO + (size_t)8 * WOIMG;
constexpr size_t WS_O     = WS_KV + 4 * KVCH;
constexpr size_t WS_PART  = WS_O + (size_t)256 * 128 * 80;
constexpr size_t WS_END   = WS_PART + (size_t)256 * 8 * 18 * 1024;
constexpr int FL_PRO = 0, FL_P = 1024, FL_KV = 2048, FL_TMO = 8192;
constexpr unsigned O_EMPTY = 0xffffffffu;

DI float bflo(unsigned v) { return __uint_as_float(v << 16); }
DI float bfhi(unsigned v) { return __uint_as_float(v & 0xffff0000u); }
DI unsigned cvtpk(float lo, float hi) { return __builtin_bit_cast(unsigned, __builtin_convertvector((f32x2){lo, hi}, bf16x2_t)); }
DI float rbf(float f) { return bflo(cvtpk(f, f)); }
DI i32x4 make_srd(const void* p, unsigned bytes) {
    unsigned long long a = (unsigned long long)p; i32x4 s;
    s.x = __builtin_amdgcn_readfirstlane((unsigned)a); s.y = __builtin_amdgcn_readfirstlane((unsigned)(a >> 32) & 0xffffu);
    s.z = (int)bytes; s.w = 0x00020000; return s;
}
DI void dma16(i32x4 srd, unsigned voff, unsigned soff, unsigned lds_addr) {
    unsigned keep;
    asm volatile("s_mov_b32 %0, m0\n\ts_mov_b32 m0, %4\n\ts_nop 0\n\tbuffer_load_dwordx4 %1, %2, %3 offen lds\n\ts_mov_b32 m0, %0"
                 : "=&s"(keep) : "v"(voff), "s"(srd), "s"(soff), "s"(lds_addr) : "memory");
}
DI void dma16_sc1(i32x4 srd, unsigned voff, unsigned soff, unsigned lds_addr) {
    unsigned keep;
    asm volatile("s_mov_b32 %0, m0\n\ts_mov_b32 m0, %4\n\ts_nop 0\n\tbuffer_load_dwordx4 %1, %2, %3 offen sc1 lds\n\ts_mov_b32 m0, %0"
                 : "=&s"(keep) : "v"(voff), "s"(srd), "s"(soff), "s"(lds_addr) : "memory");
}
DI void dma4(i32x4 srd, unsigned voff, unsigned soff, unsigned lds_addr) {
    unsigned keep;
    asm volatile("s_mov_b32 %0, m0\n\ts_mov_b32 m0, %4\n\ts_nop 0\n\tbuffer_load_dword %1, %2, %3 offen lds\n\ts_mov_b32 m0, %0"
                 : "=&s"(keep) : "v"(voff), "s"(srd), "s"(soff), "s"(lds_addr) : "memory");
}
#define WAIT_BAR(N) asm volatile("s_waitcnt vmcnt(" #N ") lgkmcnt(0)\n\ts_barrier" ::: "memory")
#define LBAR() asm volatile("s_waitcnt lgkmcnt(0)\n\ts_barrier" ::: "memory")
#define SB() __builtin_amdgcn_sched_barrier(0)
#define FENCE() asm volatile("" ::: "memory")
#define PIN4(a) asm volatile("" : "+v"((a)[0]), "+v"((a)[1]), "+v"((a)[2]), "+v"((a)[3]))
#define MFMA16(a, b, c) __builtin_amdgcn_mfma_f32_16x16x32_bf16(a, b, c, 0, 0, 0)
#define MFMA32(a, b, c) __builtin_amdgcn_mfma_f32_32x32x16_bf16(a, b, c, 0, 0, 0)
#define MFMA32K8(a, b, c) __builtin_amdgcn_mfma_f32_32x32x8bf16_1k(a, b, c, 0, 0, 0)
#define EXP2(x) __builtin_amdgcn_exp2f(x)
DI s16x4 lds_tr(LAS const char* p) { return __builtin_bit_cast(s16x4, __builtin_amdgcn_ds_read_tr16_b64_v4i16((LAS v4i16_t*)p)); }
DI bf16x8 lds_b128(LAS const char* p) { return *(LAS const bf16x8*)p; }
DI bf16x8 lds_afrag(LAS const char* p) { const s16x4 lo = lds_tr(p), hi = lds_tr(p + 2048); return __builtin_shufflevector(lo, hi, 0, 1, 2, 3, 4, 5, 6, 7); }
#define RLX_AGENT __ATOMIC_RELAXED, __HIP_MEMORY_SCOPE_AGENT
template <int X> DI float xor_lane(float v) { return __int_as_float(__builtin_amdgcn_ds_swizzle(__float_as_int(v), (X << 10) | 0x1f)); }
DI float half_sum(float v) { auto r = __builtin_amdgcn_permlane32_swap(__float_as_uint(v), __float_as_uint(v), false, false); return __uint_as_float(r[0]) + __uint_as_float(r[1]); }
DI float half_max(float v) { auto r = __builtin_amdgcn_permlane32_swap(__float_as_uint(v), __float_as_uint(v), false, false); return fmaxf(__uint_as_float(r[0]), __uint_as_float(r[1])); }
DI float half_lo(float v) { auto r = __builtin_amdgcn_permlane32_swap(__float_as_uint(v), __float_as_uint(v), false, false); return __uint_as_float(r[0]); }
template <int CTRL, int ROWS> DI float dpp_add(float v) { return v + __int_as_float(__builtin_amdgcn_update_dpp(0, __float_as_int(v), CTRL, ROWS, 0xf, true)); }
DI float wave_sum_dpp(float v) {
    v = dpp_add<0x111, 0xf>(v); v = dpp_add<0x112, 0xf>(v); v = dpp_add<0x114, 0xf>(v); v = dpp_add<0x118, 0xf>(v);
    v = dpp_add<0x142, 0xa>(v); v = dpp_add<0x143, 0xc>(v);
    return __int_as_float(__builtin_amdgcn_readlane(__float_as_int(v), 63));
}
DI float wave_sum(float v) { v += xor_lane<1>(v); v += xor_lane<2>(v); v += xor_lane<4>(v); v += xor_lane<8>(v); v += xor_lane<16>(v); return half_sum(v); }
DI float wave_max(float v) { v = fmaxf(v, xor_lane<1>(v)); v = fmaxf(v, xor_lane<2>(v)); v = fmaxf(v, xor_lane<4>(v)); v = fmaxf(v, xor_lane<8>(v)); v = fmaxf(v, xor_lane<16>(v)); return half_max(v); }
DI int fresh_lane() { int t; asm volatile("v_mbcnt_lo_u32_b32 %0, -1, 0\n\tv_mbcnt_hi_u32_b32 %0, -1, %0" : "=v"(t)); return t; }
DI void st_agent64(void* p, unsigned lo, unsigned hi) { __hip_atomic_store((GAS unsigned long long*)p, ((unsigned long long)hi << 32) | lo, RLX_AGENT); }
DI void wait_tags(const char* flags, unsigned off, bool use, unsigned tag) {
    for (unsigned spins = 0;; ++spins) {
        const unsigned v = use ? __hip_atomic_load((GAS unsigned*)(flags + off), RLX_AGENT) : tag;
        if (__all(v == tag)) break;
        if (spins > (1u << 19)) { __hip_atomic_store((GAS unsigned*)(flags + FL_TMO), tag, RLX_AGENT); break; }
        __builtin_amdgcn_s_sleep(1);
    }
    FENCE();
}

#define DMA345 dma16_sc1
constexpr int ST1 = 49152;
constexpr int LDS_D0 = 0, LDS_D1 = 49152, LDS_H = 86016, LDS_Q1 = 36864, LDS_Q2 = 45056;
constexpr int LDS_PF = 147456;
constexpr int LDS_KM = 46080;
constexpr int LDS_D2 = 126976;
constexpr int LDS_KV0 = 125952;
constexpr int LDS_OT = 113664;
constexpr int LDS_MX = LDS_OT;
constexpr int LDS_ZERO = LDS_OT + 10240;
constexpr int LDS_CTL = LDS_ZERO + 640;
constexpr int LDS_RED = 2 * KVCH + 9 * 1024;
constexpr int LDS_BYTES = 163840;
DI unsigned wo_lds(int p) { return p < 37 ? LDS_KV0 + p * 1024 : p < 74 ? (p - 37) * 1024 : p < 111 ? KVCH + (p - 74) * 1024 : 2 * KVCH + (p - 111) * 1024; }

template <int NM, bool FIRST, int ND, int DPG, int AD = 3, bool PIN = false, class Dma>
DI void gemm_step(f32x4 (&acc)[10][4], bf16x8 (&ca)[2], bf16x8 (&cb)[4], bf16x8 (&nb)[4], LAS const char* ap, LAS const char* bp, const Dma& dma) {
    bf16x8 a[10];
#pragma unroll
    for (int n = 0; n < 4; ++n) nb[n] = lds_b128(bp + n * 1024);
    a[0] = lds_afrag(ap); a[1] = lds_afrag(ap + 4096); if (AD > 2) a[2] = lds_afrag(ap + 2 * 4096);
    SB();
    int d = 0;
    if (!FIRST) {
#pragma unroll
        for (int k = 0; k < DPG; ++k) if (d < ND) dma(d++);
#pragma unroll
        for (int n = 0; n < 4; ++n) acc[NM - 2][n] = MFMA16(ca[0], cb[n], acc[NM - 2][n]);
        if (PIN) PIN4(acc[NM - 2]); SB();
#pragma unroll
        for (int k = 0; k < DPG; ++k) if (d < ND) dma(d++);
#pragma unroll
        for (int n = 0; n < 4; ++n) acc[NM - 1][n] = MFMA16(ca[1], cb[n], acc[NM - 1][n]);
        if (PIN) PIN4(acc[NM - 1]); SB();
    }
#pragma unroll
    for (int m = 0; m < NM - 2; ++m) {
        if (m + AD < NM) a[m + AD] = lds_afrag(ap + (m + AD) * 4096);
#pragma unroll
        for (int k = 0; k < DPG; ++k) if (d < ND) dma(d++);
#pragma unroll
        for (int n = 0; n < 4; ++n) acc[m][n] = MFMA16(a[m], nb[n], acc[m][n]);
        if (PIN) PIN4(acc[m]); SB();
    }
    ca[0] = a[NM - 2]; ca[1] = a[NM - 1];
}
template <int NM>
DI void gemm_flush(f32x4 (&acc)[10][4], const bf16x8 (&ca)[2], const bf16x8 (&cb)[4]) {
#pragma unroll
    for (int n = 0; n < 4; ++n) acc[NM - 2][n] = MFMA16(ca[0], cb[n], acc[NM - 2][n]);
#pragma unroll
    for (int n = 0; n < 4; ++n) acc[NM - 1][n] = MFMA16(ca[1], cb[n], acc[NM - 1][n]);
}

__global__ __launch_bounds__(512, 2) void fused_block(const u16* __restrict__ x, const u16* __restrict__ wq, const u16* __restrict__ wk, const u16* __restrict__ wv,
                                                       const u16* __restrict__ wo, const u16* __restrict__ wg, const u16* __restrict__ wu, const u16* __restrict__ wd,
                                                       char* __restrict__ ws, u16* __restrict__ out) {
    __shared__ __attribute__((aligned(1024))) char lds[LDS_BYTES];
    const int tid = threadIdx.x, l = tid & 63;
    const int w = __builtin_amdgcn_readfirstlane(tid >> 6);
    const int b = blockIdx.x, s = 2 * (b & 7) + ((b >> 3) & 1), r = b >> 4, wgi = r * 16 + s;
    const int tg = w >> 2, fg = w & 3;
    constexpr unsigned lds0 = 0;
    LAS char* L = (LAS char*)lds;
    char* flags = ws + WS_FLAGS;
    const unsigned tag = (((unsigned)amd_dispatch_id() + 1u) * 0x9E3779B1u) ^ ((unsigned)((unsigned long long)__builtin_amdgcn_dispatch_ptr() >> 6) * 0x85EBCA77u) ^ 0x5bd1e995u;

    const int dmat = w & 1, dR = (w >> 1) & 1, dcg0 = 5 * (w >> 2);
    const int di = l >> 2, dsig = l & 3;
    const int dmi = dsig ^ (2 * dmat + ((di >> 2) & 1)), dkrow = 8 * (di >> 2) + (di & 3);
    const i32x4 srd_w = make_srd(dmat ? wu : wg, DM * FF * 2);
    const i32x4 srd_q = make_srd(ws + WS_WQX, DM * 1024 * 2);
    const i32x4 srd_x = make_srd(ws + WS_XN, SEQ * DM * 2);
    const unsigned vo_w = dkrow * (FF * 2) + 16 * dmi, vo_q = dkrow * 2048 + 16 * dmi, vo_x = l * 16;
    const unsigned dlds = lds0 + dR * 2048 + dmat * 1024;
    auto pieceW = [&](int j, unsigned slot, int cg) { dma16(srd_w, vo_w, (32 * j + 4 * dR) * (FF * 2) + 288 * s * 2 + cg * 64, dlds + slot + cg * 4096); };
    auto pieceQ = [&](int j, unsigned slot) { dma16_sc1(srd_q, vo_q, (32 * j + 4 * dR) * 2048 + (64 * s + 32 * dmat) * 2, dlds + slot + 9 * 4096); };
    auto pieceX = [&](int j, unsigned slot, int n) { dma16_sc1(srd_x, vo_x, ((r * 18 + j) * 8 + n) * 1024, lds0 + slot + 40960 + n * 1024); };
    auto piece1k = [&](int j, unsigned slot, int i, int kind) {
        if (i < 3) { if (kind & 1) pieceW(j, slot, dcg0 + i); }
        else if (i == 3) { if (w == 3) { if (kind & 2) pieceQ(j, slot); } else if (kind & 1) pieceW(j, slot, dcg0 + 3); }
        else if (i == 4) {
            if (w < 3) { if (kind & 1) pieceW(j, slot, 4); } else if (w == 3) { if (kind & 2) pieceX(j, slot, 7); }
            else if (w < 7) { if (kind & 2) pieceQ(j, slot); } else if (kind & 1) pieceW(j, slot, 3);
        } else { if (w == 7) { if (kind & 1) pieceW(j, slot, 4); } else if (kind & 2) pieceX(j, slot, w); }
    };
    auto piece2 = [&](int j, unsigned slot, int i) {
        const int ii = (w >= 4 && i == 4) ? 3 : i;
        const unsigned vo_d = ((vo_w & ~63u) >> 3) + ((vo_w & 48u) << 1);
        dma16(make_srd(wd, FF * DM * 2), vo_d, (288 * s + 32 * j + 4 * dR) * (DM * 2) + 16 * dmat + (dcg0 + ii) * 128, dlds + slot + (dcg0 + ii) * 4096);
    };

    const u32x4* xs = (const u32x4*)(x + (size_t)(128 * r + 8 * s + w) * DM);
    const u32x4 v0 = xs[l];
    u32x4 v1 = xs[64 + (l & 7)];
    FENCE();
    if (w == 7) {
        unsigned long long pc; unsigned len;
        asm volatile("s_getpc_b64 %0\n\tcode_here_%=:\n\ts_mov_b32 %1, fused_block_code_end-code_here_%=" : "=s"(pc), "=s"(len));
        i32x4 sc; sc.x = (int)(unsigned)pc; sc.y = (int)((unsigned)(pc >> 32) & 0xffffu); sc.z = (int)len; sc.w = 0x00020000;
        dma4(sc, l < 20 ? (unsigned)(((b >> 3) * 20 + l) * 128) : 0x80000000u, 0, lds0 + LDS_PF);
    }
    {
        unsigned qv[4];
        {
            const int tq = min(tid, 287), k = 36 * r + (tq >> 3), mat = (tq >> 2) & 1, t = tq & 3;
            const bool odd = t & 1;
            const __amdgpu_buffer_rsrc_t rq_ = __builtin_amdgcn_make_buffer_rsrc((void*)wq, 0, DM * DM * 2, 0x00020000);
            const __amdgpu_buffer_rsrc_t rc_ = __builtin_amdgcn_make_buffer_rsrc((void*)((s & 1) ? wv : wk), 0, DM * DH * 2, 0x00020000);
            const u32x4 a = __builtin_amdgcn_raw_buffer_load_b128(rq_, (k * DM + (2 * s + (t >> 1)) * DH + (odd ? 16 : 8 * mat)) * 2, 0, 0);
            const u32x4 c = __builtin_amdgcn_raw_buffer_load_b128(rc_, (k * DH + (t == 1 ? 4 * mat : 12 + 4 * mat)) * 2, 0, 0);
            qv[0] = (!odd || mat == 0) ? a[0] : c[0];
            qv[1] = !odd ? a[1] : (mat == 1 && t == 1) ? c[1] : 0u;
            qv[2] = !odd ? a[2] : mat == 0 ? c[0] : t == 1 ? c[2] : 0u;
            qv[3] = !odd ? a[3] : mat == 0 ? c[1] : t == 1 ? c[3] : 0u;
        }
        FENCE();
        if (l >= 8) v1 = (u32x4){0u, 0u, 0u, 0u};
        float sm = 0.f;
#pragma unroll
        for (int e = 0; e < 4; ++e) sm += bflo(v0[e]) + bfhi(v0[e]) + bflo(v1[e]) + bfhi(v1[e]);
        const float mean = wave_sum_dpp(sm) * (1.0f / DM);
        float q = 0.f, q1 = 0.f;
#pragma unroll
        for (int e = 0; e < 4; ++e) { const float a = bflo(v0[e]) - mean, c = bfhi(v0[e]) - mean; q += a * a + c * c; }
#pragma unroll
        for (int e = 0; e < 4; ++e) { const float a = bflo(v1[e]) - mean, c = bfhi(v1[e]) - mean; q1 += a * a + c * c; }
        if (l < 8) q += q1;
        const float m = rbf(mean), var = rbf(wave_sum_dpp(q) * (1.0f / DM));
        const float rs = 1.0f / rbf(sqrtf(rbf(var + 1e-5f)));
        LAS char* xst = L + 2 * ST1;
        {
            u32x4 o;
#pragma unroll
            for (int e = 0; e < 4; ++e) o[e] = cvtpk(rbf(bflo(v0[e]) - m) * rs, rbf(bfhi(v0[e]) - m) * rs);
            *(LAS u32x4*)(xst + l * 128 + w * 16) = o;
#pragma unroll
            for (int e = 0; e < 4; ++e) o[e] = cvtpk(rbf(bflo(v1[e]) - m) * rs, rbf(bfhi(v1[e]) - m) * rs);
            if (l < 8) *(LAS u32x4*)(xst + (64 + l) * 128 + w * 16) = o;
        }
        LBAR();
        if (w < 7) {
            const __amdgpu_buffer_rsrc_t rx = __builtin_amdgcn_make_buffer_rsrc(ws + WS_XN, 0, SEQ * DM * 2, 0x00020000);
#pragma unroll
            for (int u = 0; u < 2; ++u) {
                const int i = tid + 448 * u, c = i >> 3, t = i & 7;
                const u32x4 v = *(LAS const u32x4*)(xst + min(i, 575) * 16);
                __builtin_amdgcn_raw_buffer_store_b128(v, rx, i < 576 ? (unsigned)(((r * 18 + (c >> 2)) * 8 + (s >> 1)) * 1024 + ((c & 3) * 16 + 8 * (s & 1) + t) * 16) : 0xfffffff0u, 0, 16);
            }
            {
                const u32x4 o = (u32x4){qv[0], qv[1], qv[2], qv[3]};
                const __amdgpu_buffer_rsrc_t rq = __builtin_amdgcn_make_buffer_rsrc(ws + WS_WQX, 0, DM * 1024 * 2, 0x00020000);
                __builtin_amdgcn_raw_buffer_store_b128(o, rq, tid < 288 ? (unsigned)(((36 * r + (tid >> 3)) * 128 + s * 8 + (tid & 7)) * 16) : 0xfffffff0u, 0, 16);
            }
            WAIT_BAR(0);
        } else {
            const unsigned o = ((l & 7) < 5 && l < 32) ? (unsigned)((4 * r + (l >> 3)) * (FF * 2) + ((288 * s * 2) & ~127) + (l & 7) * 128) : 0x80000000u;
            dma4(make_srd(wg, DM * FF * 2), o, 0, lds0 + LDS_PF); dma4(srd_w, o, 0, lds0 + LDS_PF);
            LBAR();
        }
        if (tid == 0) __hip_atomic_store((GAS unsigned*)(flags + FL_PRO + wgi * 4), tag, RLX_AGENT);
#pragma unroll
        for (int i = 0; i < 6; ++i) piece1k(0, 0, i, 1);
#pragma unroll
        for (int i = 0; i < 6; ++i) piece1k(1, ST1, i, 1);
        if (w < 7) {
            const __amdgpu_buffer_rsrc_t ro = __builtin_amdgcn_make_buffer_rsrc(ws + WS_O + (size_t)wgi * 128 * 80, 0, 128 * 80, 0x00020000);
            const u32x4 e = (u32x4){O_EMPTY, O_EMPTY, O_EMPTY, O_EMPTY};
            __builtin_amdgcn_raw_buffer_store_b128(e, ro, tid * 16, 0, 16);
            __builtin_amdgcn_raw_buffer_store_b128(e, ro, (tid + 448) * 16, 0, 16);
        }
        if (w == 0) wait_tags(flags, FL_PRO + (l < 16 ? (r * 16 + l) * 4 : ((l - 16) * 16 + s) * 4), l < 32, tag);
        asm volatile("s_barrier" ::: "memory");
#pragma unroll
        for (int i = 3; i < 6; ++i) piece1k(0, 0, i, 2);
#pragma unroll
        for (int i = 3; i < 6; ++i) piece1k(1, ST1, i, 2);
        if (w == 7) {
            const unsigned o = (l & 7) < 5 ? (unsigned)(((l >> 3) * 16 + r) * (FF * 2) + ((288 * s * 2) & ~127) + (l & 7) * 128) : 0x80000000u;
#pragma unroll
            for (int g = 0; g < 2; ++g) { dma4(make_srd(wg, DM * FF * 2), o, g * 128 * (FF * 2), lds0 + LDS_PF); dma4(srd_w, o, g * 128 * (FF * 2), lds0 + LDS_PF); }
        }
    }
    const int lg = fresh_lane();
    const unsigned a_off = ((lg & 3) >> 1) * 1024 + 16 * (4 * (4 * (lg >> 4) + ((lg >> 2) & 3)) + (fg ^ (2 * ((lg & 3) >> 1) + ((lg >> 4) & 1)))) + 8 * (lg & 1);

    f32x4 acc[10][4];
#pragma unroll
    for (int m = 0; m < 10; ++m)
#pragma unroll
        for (int n = 0; n < 4; ++n) acc[m][n] = (f32x4){0.f, 0.f, 0.f, 0.f};
    bf16x8 ca[2], bA[4], bB[4];
    {
        const unsigned bo = 40960 + tg * 4096 + lg * 16;
        if (w == 7) { WAIT_BAR(4); } else { WAIT_BAR(0); }
        const bool q3 = w == 3, w4 = w < 3 || w == 7, x4 = w == 3, w5 = w == 7;
        const i32x4 srd3 = q3 ? srd_q : srd_w, srd4 = w4 ? srd_w : x4 ? srd_x : srd_q, srd5 = w5 ? srd_w : srd_x;
        const unsigned vo3 = q3 ? vo_q : vo_w, vo4 = w4 ? vo_w : x4 ? vo_x : vo_q, vo5 = w5 ? vo_w : vo_x;
        const unsigned soW = 4 * dR * (FF * 2) + 288 * s * 2, soQ = 4 * dR * 2048 + (64 * s + 32 * dmat) * 2, soX = r * 18 * 8192;
        const unsigned stW = 32 * FF * 2, stQ = 32 * 2048, stX = 8192;
        const unsigned so3 = q3 ? soQ : soW + (dcg0 + 3) * 64, st3 = q3 ? stQ : stW, lo3 = q3 ? dlds + 9 * 4096 : dlds + (dcg0 + 3) * 4096;
        const int cg4 = w < 3 ? 4 : 3;
        const unsigned so4 = w4 ? soW + cg4 * 64 : x4 ? soX + 7 * 1024 : soQ, st4 = w4 ? stW : x4 ? stX : stQ, lo4 = w4 ? dlds + cg4 * 4096 : x4 ? lds0 + 40960 + 7 * 1024 : dlds + 9 * 4096;
        const unsigned so5 = w5 ? soW + 4 * 64 : soX + w * 1024, st5 = w5 ? stW : stX, lo5 = w5 ? dlds + 4 * 4096 : lds0 + 40960 + w * 1024;
        unsigned vo_pf;
        { const int lp = fresh_lane(); vo_pf = (lp & 7) < 5 ? (unsigned)(((lp >> 3) * 16 + r) * (FF * 2) + ((288 * s * 2) & ~127) + (lp & 7) * 128) : 0x80000000u; }
        auto stage1 = [&](int j, unsigned slot, int i) {
            if (i < 3) dma16(srd_w, vo_w, soW + (dcg0 + i) * 64 + j * stW, dlds + (dcg0 + i) * 4096 + slot);
            else if (i == 3) DMA345(srd3, vo3, so3 + j * st3, lo3 + slot);
            else if (i == 4) DMA345(srd4, vo4, so4 + j * st4, lo4 + slot);
            else if (i == 5) DMA345(srd5, vo5, so5 + j * st5, lo5 + slot);
            else if (w >= 6) {
                const int t = j - 2;
                if (t == 6) { const int lp = fresh_lane(); vo_pf = (w == 7 && (lp & 15) < 9) ? (unsigned)((288 * s + (lp >> 4) * 16 + r) * (DM * 2) + (lp & 15) * 128) : 0x80000000u; }
                if (t == 11) { const int lp = fresh_lane(); vo_pf = (w == 7 && lp < 21) ? (unsigned)((b * 21 + lp) * 128) : 0x80000000u; }
                const unsigned so = t < 6 ? ((t & 1) ? 0x70000000u : (unsigned)(2 + (t >> 1)) * 128 * (FF * 2)) : t < 11 ? (unsigned)(t - 6) * 64 * (DM * 2) : t == 11 ? 0u : 0x70000000u;
                dma4(t < 6 ? srd_w : t < 11 ? make_srd(wd, FF * DM * 2) : make_srd(wo, DM * DM * 2), vo_pf, so, lds0 + LDS_PF);
            }
        };
        gemm_step<10, true, 7, 1>(acc, ca, bB, bA, L + a_off, L + bo, [&](int i) { stage1(2, 2 * ST1, i); });
        unsigned slot = ST1, slot2 = 0;
        if (w >= 6) { WAIT_BAR(7); } else { WAIT_BAR(6); }
        gemm_step<10, false, 7, 1>(acc, ca, bA, bB, L + slot + a_off, L + slot + bo, [&](int i) { stage1(3, slot2, i); });
        slot = 2 * ST1; slot2 = ST1;
#pragma unroll 1
        for (int j = 2; j < 16; j += 2) {
            if (w >= 6) { WAIT_BAR(8); } else { WAIT_BAR(6); }
            gemm_step<10, false, 7, 1>(acc, ca, bB, bA, L + slot + a_off, L + slot + bo, [&](int i) { stage1(j + 2, slot2, i); });
            slot = (slot == 2 * ST1) ? 0 : slot + ST1; slot2 = (slot2 == 2 * ST1) ? 0 : slot2 + ST1;
            if (w >= 6) { WAIT_BAR(8); } else { WAIT_BAR(6); }
            gemm_step<10, false, 7, 1>(acc, ca, bA, bB, L + slot + a_off, L + slot + bo, [&](int i) { stage1(j + 3, slot2, i); });
            slot = (slot == 2 * ST1) ? 0 : slot + ST1; slot2 = (slot2 == 2 * ST1) ? 0 : slot2 + ST1;
        }
#pragma unroll
        for (int n = 0; n < 4; ++n) bA[n] = bB[n];
#pragma unroll 1
        for (int j = 16; j < 18; ++j) {
            if (j == 16 && w >= 6) { WAIT_BAR(8); } else if (j == 16) { WAIT_BAR(6); } else { WAIT_BAR(5); }
            gemm_step<10, false, 5, 1, 3, true>(acc, ca, bA, bB, L + slot + a_off, L + slot + bo, [&](int i) { piece2(j - 16, j == 16 ? LDS_D0 : LDS_D1, i); });
#pragma unroll
            for (int n = 0; n < 4; ++n) bA[n] = bB[n];
            slot += ST1;
        }
    }
    WAIT_BAR(0);
    gemm_flush<10>(acc, ca, bA);
    u32x4 wov; bool wo_act; unsigned wo_off;
    {
        const int l = fresh_lane(), tw = w * 64 + l;
        const int cgp = s & 7, wp = 4 * (r * 2 + (s >> 3)) + ((min(max(tw, 192), 447) - 192) >> 6);
        wo_act = tw >= 192 && tw < 448 && wp < 120;
        const int m0 = cgp < 4 ? 5 * cgp : 20 + 4 * (cgp - 4), wpc = min(wp, 119);
        const int pp = wpc / 40, js = (wpc % 40) >> 1, R = wpc & 1, i = l >> 2, ch = (l & 3) ^ (2 * ((i >> 2) & 1));
        const int kp = 32 * js + 8 * (i >> 2) + (i & 3) + 4 * R, sp = kp / 40, d = kp - 40 * sp, hd = d >= 20 ? 1 : 0, dd = d - 20 * hd;
        const int row = 36 * sp + 18 * hd + (dd < 18 ? dd : 17), col = min(16 * (m0 + 2 * pp) + 8 * ch, DM - 8);
        wov = *(const u32x4*)(wo + (size_t)row * DM + col);
        wo_off = wo_act ? (unsigned)(cgp * WOIMG + wp * 1024 + l * 16) : 0xfffffff0u;
    }
    auto h_tiles = [&](auto CG0, auto CG1) {
        const int l = fresh_lane(), tok = l & 15, g4 = l >> 4;
#pragma unroll
        for (int cg = CG0; cg < CG1; ++cg)
#pragma unroll
            for (int pr = 0; pr < 2; ++pr) {
                float h[4];
#pragma unroll
                for (int e = 0; e < 4; ++e) {
                    auto sw = __builtin_amdgcn_permlane32_swap(__float_as_uint(acc[cg][2 * pr][e]), __float_as_uint(acc[cg][2 * pr + 1][e]), false, false);
                    h[e] = __int_as_float(max((int)sw[0], 0)) * __uint_as_float(sw[1]);
                }
                const int n = 4 * tg + 2 * pr + (l >> 5);
                u32x2 o; o[0] = cvtpk(h[0], h[1]); o[1] = cvtpk(h[2], h[3]);
                *(LAS u32x2*)(L + LDS_H + ((8 - cg) * 8 + n) * 1024 + (fg * 16 + tok) * 16 + 8 * (g4 & 1)) = o;
            }
    };
    float kn2 = 0.f;
    if ((fg & 1) && tg == (s >> 3)) {
        const int l = fresh_lane(), tok = l & 15, g4 = l >> 4;
        const int e = s >> 1;
        f32x4 cv = acc[9][0];
        if ((e & 3) == 1) cv = acc[9][1]; else if ((e & 3) == 2) cv = acc[9][2]; else if ((e & 3) == 3) cv = acc[9][3];
        const int c0 = (fg == 1 ? 0 : 12) + 4 * (g4 - 1);
        const bool act = g4 >= 1 && c0 < 18;
        const unsigned p0 = cvtpk(cv[0], cv[1]), p1 = cvtpk(cv[2], cv[3]);
        const int kt = 4 * r + (e >> 1);
        char* kvb = ws + WS_KV + ((kt >> 3) & 3) * KVCH; const int lt = 8 * (kt >> 5) + (kt & 7);
        if ((s & 1) == 0) {
            const int key = 16 * (e & 1) + tok;
            if (act) {
                if (c0 < 16) st_agent64(kvb + lt * 1024 + ((c0 >> 3) * 32 + key) * 16 + (c0 & 4) * 2, p0, p1);
                else __hip_atomic_store((GAS unsigned*)(kvb + KV_K2 + lt * 128 + key * 4), p0, RLX_AGENT);
            }
            float n2 = 0.f;
            if (act) { n2 = bflo(p0) * bflo(p0) + bfhi(p0) * bfhi(p0); if (c0 < 16) n2 += bflo(p1) * bflo(p1) + bfhi(p1) * bfhi(p1); }
            kn2 = n2;
        } else {
            if (fg == 1 && l < 2) {
                char* op = kvb + KV_V + lt * 1216 + (e & 1) * 608 + l * 304 + 18 * 16;
                st_agent64(op, 0x3f803f80u, 0x3f803f80u); st_agent64(op + 8, 0x3f803f80u, 0x3f803f80u);
            }
            const int hv = (tok >> 2) & 1, jj = 4 * (tok >> 3) + (tok & 3);
            GAS u16* vb = (GAS u16*)(kvb + KV_V + lt * 1216 + (e & 1) * 608 + hv * 304 + jj * 2);
            if (act) {
                __hip_atomic_store(vb + (c0 + 0) * 8, (u16)(p0 & 0xffff), RLX_AGENT);
                __hip_atomic_store(vb + (c0 + 1) * 8, (u16)(p0 >> 16), RLX_AGENT);
                if (c0 < 16) { __hip_atomic_store(vb + (c0 + 2) * 8, (u16)(p1 & 0xffff), RLX_AGENT);
                               __hip_atomic_store(vb + (c0 + 3) * 8, (u16)(p1 >> 16), RLX_AGENT); }
            }
        }
    }
    h_tiles(std::integral_constant<int, 0>{}, std::integral_constant<int, 9>{});
    {
        const int l = fresh_lane(), tok = l & 15, g4 = l >> 4;
        const float qs = 0.23570226039551584f * 1.4426950408889634f;
        const int hh = fg >> 1;
        unsigned p0[4], p1[4];
#pragma unroll
        for (int n = 0; n < 4; ++n) { p0[n] = cvtpk(acc[9][n][0] * qs, acc[9][n][1] * qs); p1[n] = cvtpk(acc[9][n][2] * qs, acc[9][n][3] * qs); }
        if ((fg & 1) == 0) {
            LAS char* qd = L + LDS_Q1 + (hh * 4 + 2 * tg) * 1024 + ((g4 >> 1) * 32 + tok) * 16 + (g4 & 1) * 8;
#pragma unroll
            for (int n = 0; n < 4; ++n) *(LAS u32x2*)(qd + (n >> 1) * 1024 + (n & 1) * 256) = (u32x2){p0[n], p1[n]};
        } else if (g4 == 0) {
            LAS char* qd = L + LDS_Q2 + (hh * 4 + 2 * tg) * 128 + tok * 4;
#pragma unroll
            for (int n = 0; n < 4; ++n) *(LAS unsigned*)(qd + (n >> 1) * 128 + (n & 1) * 64) = p0[n];
        }
    }
    {
        const __amdgpu_buffer_rsrc_t rw = __builtin_amdgcn_make_buffer_rsrc(ws + WS_WO, 0, 8 * WOIMG, 0x00020000);
        __builtin_amdgcn_raw_buffer_store_b128(wov, rw, wo_off, 0, 16);
    }
    if ((fg & 1) && tg == (s >> 3)) {
        kn2 += xor_lane<16>(kn2); kn2 = wave_max(half_sum(kn2));
        if (fresh_lane() == 0) *(LAS float*)(L + LDS_KM + (fg == 1 ? 0 : 4)) = kn2;
    }
    LBAR();
    const i32x4 srd_kv = make_srd(ws + WS_KV, 4 * KVCH);
    auto piece_kv = [&](int c, int i) {
        const int p = min(w + 8 * i, 36);
        dma16_sc1(srd_kv, vo_x, c * KVCH + p * 1024, lds0 + (c == 0 ? LDS_KV0 : (c - 1) * KVCH) + p * 1024);
    };
#pragma unroll
    for (int m = 0; m < 9; ++m)
#pragma unroll
        for (int n = 0; n < 4; ++n) acc[m][n] = (f32x4){0.f, 0.f, 0.f, 0.f};
    {
        LAS const char* hb = L + LDS_H + 8 * 8192 + 4 * tg * 1024 + l * 16;
        gemm_step<9, true, 0, 1>(acc, ca, bB, bA, L + LDS_D0 + a_off, hb, [](int) {});
        WAIT_BAR(0);
        if (w == 0 && fresh_lane() == 0) {
            st_agent64(flags + FL_KV + wgi * 16, tag, *(LAS const unsigned*)(L + LDS_KM));
            st_agent64(flags + FL_KV + wgi * 16 + 8, tag, *(LAS const unsigned*)(L + LDS_KM + 4));
        }
        gemm_step<9, false, 5, 2>(acc, ca, bA, bB, L + LDS_D1 + a_off, hb - 1 * 8192, [&](int i) { piece2(2, LDS_D0, i); });
        WAIT_BAR(0);
        gemm_step<9, false, 5, 2>(acc, ca, bB, bA, L + LDS_D0 + a_off, hb - 2 * 8192, [&](int i) { piece2(3, LDS_D1, i); });
        WAIT_BAR(0);
        gemm_step<9, false, 5, 2>(acc, ca, bA, bB, L + LDS_D1 + a_off, hb - 3 * 8192, [&](int i) { piece2(4, LDS_D0, i); });
        WAIT_BAR(0);
        gemm_step<9, false, 10, 2>(acc, ca, bB, bA, L + LDS_D0 + a_off, hb - 4 * 8192, [&](int i) { if (i < 5) piece2(5, LDS_D1, i); else piece2(6, LDS_D2, i - 5); });
        WAIT_BAR(5);
        gemm_step<9, false, 5, 1>(acc, ca, bA, bB, L + LDS_D1 + a_off, hb - 5 * 8192, [&](int i) { piece2(7, LDS_D0, i); });
        WAIT_BAR(5);
        gemm_step<9, false, 5, 1>(acc, ca, bB, bA, L + LDS_D2 + a_off, hb - 6 * 8192, [&](int i) { piece2(8, LDS_D1, i); });
        if (w == 0) {
            const int lk = fresh_lane();
            const __amdgpu_buffer_rsrc_t rk = __builtin_amdgcn_make_buffer_rsrc(flags + FL_KV, 0, 4096, 0x00020000);
            float ma, mb;
            for (unsigned spins = 0;; ++spins) {
                bool ok = true; ma = 0.f; mb = 0.f;
#pragma unroll
                for (int i = 0; i < 4; ++i) {
                    const u32x4 v = __builtin_amdgcn_raw_buffer_load_b128(rk, (i * 64 + lk) * 16, 0, 16);
                    ok = ok && v[0] == tag && v[2] == tag; ma = fmaxf(ma, __uint_as_float(v[1])); mb = fmaxf(mb, __uint_as_float(v[3]));
                }
                if (__all(ok)) break;
                if (spins > (1u << 19)) { __hip_atomic_store((GAS unsigned*)(flags + FL_TMO), tag, RLX_AGENT); break; }
                __builtin_amdgcn_s_sleep(1);
            }
            ma = wave_max(ma); mb = wave_max(mb);
            if (lk == 0) *(LAS float*)(L + LDS_KM + 8) = ma + mb;
        }
        WAIT_BAR(5);
        gemm_step<9, false, 5, 1>(acc, ca, bA, bB, L + LDS_D0 + a_off, hb - 7 * 8192, [&](int i) { piece_kv(0, i); });
        WAIT_BAR(5);
        gemm_step<9, false, 0, 1>(acc, ca, bB, bA, L + LDS_D1 + a_off, hb - 8 * 8192, [](int) {});
        gemm_flush<9>(acc, ca, bA);
        SB(); FENCE();
    }
    const int la = fresh_lane();
#define l la
    const int hh = w >> 2, kh = (w >> 1) & 1, cp = w & 1;
#define h5 (l >> 5)
    bf16x8 q1[2]; u32x2 q2[2];
#pragma unroll
    for (int c = 0; c < 2; ++c) {
        q1[c] = lds_b128(L + LDS_Q1 + (hh * 4 + 2 * cp + c) * 1024 + l * 16);
        q2[c][0] = *(LAS const unsigned*)(L + LDS_Q2 + (hh * 4 + 2 * cp + c) * 128 + (l & 31) * 4);
    }
    u32x4 pk[18];
#pragma unroll
    for (int m = 0; m < 9; ++m)
#pragma unroll
        for (int pr = 0; pr < 2; ++pr) {
            u32x4 o; o[0] = cvtpk(acc[m][2 * pr][0], acc[m][2 * pr][1]); o[1] = cvtpk(acc[m][2 * pr][2], acc[m][2 * pr][3]);
            o[2] = cvtpk(acc[m][2 * pr + 1][0], acc[m][2 * pr + 1][1]); o[3] = cvtpk(acc[m][2 * pr + 1][2], acc[m][2 * pr + 1][3]);
            pk[m * 2 + pr] = o;
        }
    {
        const float kmax2 = *(LAS const float*)(L + LDS_KM + 8);
#pragma unroll
        for (int c = 0; c < 2; ++c) {
            const u32x4 a = __builtin_bit_cast(u32x4, q1[c]); const unsigned t = h5 ? 0u : q2[c][0];
            float n2 = bflo(t) * bflo(t) + bfhi(t) * bfhi(t);
#pragma unroll
            for (int e = 0; e < 4; ++e) n2 += bflo(a[e]) * bflo(a[e]) + bfhi(a[e]) * bfhi(a[e]);
            n2 = half_sum(n2);
            const float cb = sqrtf(n2 * kmax2) * 1.001f + 0.01f;
            const float chi = rbf(cb), clo = cb - chi;
            q2[c][0] = t; q2[c][1] = h5 ? 0u : cvtpk(-chi, -clo);
        }
    }
    if (w == 0 && l < 40) *(LAS u32x4*)(L + LDS_ZERO + l * 16) = (u32x4){0u, 0u, 0u, 0u};
    if (w == 0 && l == 40) *(LAS unsigned*)(L + LDS_CTL) = 0u;
    WAIT_BAR(0);
#undef l
    const int lb = fresh_lane();
#define l lb
    const unsigned kone = h5 ? 0u : 0x3f803f80u;
    const bool vrow = (l & 31) < 19;
    const unsigned k1_base = 8 * kh * 1024 + l * 16, k2_base = KV_K2 + 8 * kh * 128 + (l & 31) * 4;
    const unsigned v_base = vrow ? KV_V + 8 * kh * 1216 + h5 * 304 + (l & 31) * 16 : LDS_ZERO, v_mul = vrow ? 1u : 0u;
    const int tgf = s >> 3, cgp = s & 7, nmt = cgp < 4 ? 5 : 4, m0 = cgp < 4 ? 5 * cgp : 20 + 4 * (cgp - 4);
    const int pr = w & 1, qk = w >> 1;
    const i32x4 srd_wo = make_srd(ws + WS_WO + (size_t)cgp * WOIMG, WOIMG);
    auto piece_wo = [&](int p) { dma16_sc1(srd_wo, vo_x, p * 1024, lds0 + wo_lds(p)); };
    const __amdgpu_buffer_rsrc_t rpart = __builtin_amdgcn_make_buffer_rsrc(ws + WS_PART + (size_t)r * 16 * 8 * 18 * 1024, 0, 16 * 8 * 18 * 1024, 0x00020000);
    float own[11];
    u32x4 pl[5][4];

    f32x16 o0, o1, sX, sY; bf16x8 kA, kB, v0A, v1A, v0B, v1B; unsigned k2A, k2B; u32x4 p0, p1; float e3[4];
    unsigned aK, aK2, aV;
#define SLOT(SC, SN, KN, K2N, CN, OP, V1P, OC, V0C) do { float ea[4], eb[4]; \
        SN = MFMA32(KN, q1[CN], (f32x16){}); SB(); \
        p1[2] = cvtpk(e3[0], e3[1]); p1[3] = cvtpk(e3[2], e3[3]); SB(); \
        ea[0] = EXP2(SC[0]); ea[1] = EXP2(SC[1]); ea[2] = EXP2(SC[2]); ea[3] = EXP2(SC[3]); SB(); \
        OP = MFMA32(V1P, __builtin_bit_cast(bf16x8, p1), OP); SB(); \
        p0[0] = cvtpk(ea[0], ea[1]); p0[1] = cvtpk(ea[2], ea[3]); SB(); \
        eb[0] = EXP2(SC[4]); eb[1] = EXP2(SC[5]); eb[2] = EXP2(SC[6]); eb[3] = EXP2(SC[7]); SB(); \
        SN = MFMA32K8(__builtin_bit_cast(s16x4, ((u32x2){K2N, kone})), __builtin_bit_cast(s16x4, q2[CN]), SN); SB(); \
        p0[2] = cvtpk(eb[0], eb[1]); p0[3] = cvtpk(eb[2], eb[3]); SB(); \
        ea[0] = EXP2(SC[8]); ea[1] = EXP2(SC[9]); ea[2] = EXP2(SC[10]); ea[3] = EXP2(SC[11]); SB(); \
        OC = MFMA32(V0C, __builtin_bit_cast(bf16x8, p0), OC); SB(); \
        p1[0] = cvtpk(ea[0], ea[1]); p1[1] = cvtpk(ea[2], ea[3]); SB(); \
        e3[0] = EXP2(SC[12]); e3[1] = EXP2(SC[13]); e3[2] = EXP2(SC[14]); e3[3] = EXP2(SC[15]); SB(); } while (0)
#define PAIR(DK, DK2, DV, H0, H1, H2, H3) do { \
        kB = lds_b128(L + aK + 1024); k2B = *(LAS const unsigned*)(L + aK2 + 128); v0B = lds_b128(L + aV + v_mul * 1216); SB(); \
        H0; SLOT(sX, sY, kA, k2A, 1, o1, v1B, o0, v0A); \
        v1B = lds_b128(L + aV + v_mul * 1216 + 608); SB(); \
        H1; SLOT(sY, sX, kB, k2B, 0, o0, v1A, o1, v0A); \
        aK += (DK); aK2 += (DK2); aV += v_mul * (DV); \
        kA = lds_b128(L + aK); k2A = *(LAS const unsigned*)(L + aK2); v0A = lds_b128(L + aV); SB(); \
        H2; SLOT(sX, sY, kB, k2B, 1, o1, v1A, o0, v0B); \
        v1A = lds_b128(L + aV + 608); SB(); \
        H3; SLOT(sY, sX, kA, k2A, 0, o0, v1B, o1, v0B); } while (0)
#define PAIR_IN(H0, H1, H2, H3) PAIR(2048, 256, 2432, H0, H1, H2, H3)
#define PAIR_JUMP(J, H0, H1, H2, H3) PAIR((J) - 6 * 1024, (J) - 6 * 128, (J) - 6 * 1216, H0, H1, H2, H3)
#define NOP_ ((void)0)
#define ATT_INIT() do { o0 = (f32x16){}; o1 = (f32x16){}; aK = k1_base + LDS_KV0; aK2 = k2_base + LDS_KV0; aV = v_base + v_mul * LDS_KV0; \
        kA = lds_b128(L + aK); k2A = *(LAS const unsigned*)(L + aK2); v0A = lds_b128(L + aV); v1A = lds_b128(L + aV + 608); v1B = v1A; \
        sX = MFMA32(kA, q1[0], (f32x16){}); sX = MFMA32K8(__builtin_bit_cast(s16x4, ((u32x2){k2A, kone})), __builtin_bit_cast(s16x4, q2[0]), sX); \
        p1 = (u32x4){0u, 0u, 0u, 0u}; e3[0] = e3[1] = e3[2] = e3[3] = 0.f; } while (0)
#define ATT_FIN() do { p1[2] = cvtpk(e3[0], e3[1]); p1[3] = cvtpk(e3[2], e3[3]); o1 = MFMA32(v1B, __builtin_bit_cast(bf16x8, p1), o1); } while (0)
    constexpr int J01 = 0 - LDS_KV0, J12 = KVCH;
    {
        const __amdgpu_buffer_rsrc_t rp = __builtin_amdgcn_make_buffer_rsrc(ws + WS_PART + (size_t)(r * 16 + s) * 8 * 18 * 1024, 0, 8 * 18 * 1024, 0x00020000);
#define KV(C, I) do { piece_kv(C, I); SB(); } while (0)
#define ST(I) do { __builtin_amdgcn_raw_buffer_store_b128(pk[I], rp, vo_x, (w * 18 + (I)) * 1024, 16); SB(); } while (0)
#define WO(P0, I) do { piece_wo(min((P0) + w + 8 * (I), (P0) + 36)); SB(); } while (0)
#define PL(T, SP) do { const int m_ = m0 + ((T) < nmt ? (T) : 0); \
            pl[T][SP] = __builtin_amdgcn_raw_buffer_load_b128(rpart, vo_x, ((((4 * qk + (SP)) * 8 + tgf * 4 + (m_ & 3)) * 9 + (m_ >> 2)) * 2 + pr) * 1024, 16); SB(); } while (0)
#define PL2(A) do { PL((A) >> 2, (A) & 3); PL(((A) + 1) >> 2, ((A) + 1) & 3); } while (0)
        ATT_INIT();
        PAIR_IN(KV(1, 0), KV(1, 1), KV(1, 2), KV(1, 3));
        PAIR_IN(KV(1, 4), ST(0), KV(2, 0), ST(1));
        PAIR_IN(KV(2, 1), ST(2), KV(2, 2), ST(3));
        WAIT_BAR(7);
        PAIR_JUMP(J01, KV(2, 3), ST(4), KV(2, 4), ST(5));
        PAIR_IN(KV(3, 0), ST(6), KV(3, 1), ST(7));
        PAIR_IN(KV(3, 2), ST(8), KV(3, 3), ST(9));
        PAIR_IN(KV(3, 4), ST(10), NOP_, ST(11));
        WAIT_BAR(12);
        PAIR_JUMP(J12, WO(0, 0), ST(12), WO(0, 1), ST(13));
        PAIR_IN(WO(0, 2), ST(14), WO(0, 3), ST(15));
        PAIR_IN(WO(0, 4), ST(16), NOP_, ST(17));
        PAIR_IN(NOP_, NOP_, NOP_, NOP_);
        WAIT_BAR(0);
        if (w == 0 && fresh_lane() == 0) __hip_atomic_store((GAS unsigned*)(flags + FL_P + wgi * 4), tag, RLX_AGENT);
        PAIR_JUMP(J12, WO(37, 0), WO(37, 1), WO(37, 2), WO(37, 3));
        const unsigned pflag = __hip_atomic_load((GAS unsigned*)(flags + FL_P + (r * 16 + (l & 15)) * 4), RLX_AGENT);
        PAIR_IN(WO(37, 4), NOP_, NOP_, NOP_);
        LBAR();
        if (!__all(pflag == tag)) wait_tags(flags, FL_P + (r * 16 + (l & 15)) * 4, true, tag);
#define WP(I, A) do { WO(74, I); PL((A) >> 2, (A) & 3); } while (0)
        PAIR_IN(WP(0, 0), WP(1, 1), WP(2, 2), WP(3, 3));
        PAIR_IN(WP(4, 4), PL2(5), PL2(7), PL2(9));
        PAIR(0, 0, 0, PL2(11), PL2(13), PL2(15), PL2(17));
        PL(4, 3);
        ATT_FIN();
    }
#undef l
    const int lc = fresh_lane();
#define l lc
    for (int attempt = 0;; ++attempt) {
        if (attempt == 1) {
            const unsigned kone = h5 ? 0u : 0x3f803f80u;
            const bool vrow = (l & 31) < 19;
            const unsigned k1_base = 8 * kh * 1024 + l * 16, k2_base = KV_K2 + 8 * kh * 128 + (l & 31) * 4;
            const unsigned v_base = vrow ? KV_V + 8 * kh * 1216 + h5 * 304 + (l & 31) * 16 : LDS_ZERO, v_mul = vrow ? 1u : 0u;
            WAIT_BAR(0);
#pragma unroll
            for (int c = 0; c < 4; ++c)
#pragma unroll
                for (int i = 0; i < 5; ++i) piece_kv(c, i);
            if (w == 0 && l < 40) *(LAS u32x4*)(L + LDS_ZERO + l * 16) = (u32x4){0u, 0u, 0u, 0u};
            WAIT_BAR(0);
            float m[2] = {-INFINITY, -INFINITY};
            for (int i = 0; i < 32; ++i) {
                const unsigned to = (i >> 3) == 0 ? LDS_KV0 : ((i >> 3) - 1) * KVCH;
                const bf16x8 k1 = lds_b128(L + k1_base + to + (i & 7) * 1024);
                const u32x2 k2 = {*(LAS const unsigned*)(L + k2_base + to + (i & 7) * 128), kone};
#pragma unroll
                for (int c = 0; c < 2; ++c) {
                    const u32x2 qz = {q2[c][0], 0u};
                    f32x16 sc = MFMA32(k1, q1[c], (f32x16){});
                    sc = MFMA32K8(__builtin_bit_cast(s16x4, k2), __builtin_bit_cast(s16x4, qz), sc);
#pragma unroll
                    for (int e = 0; e < 16; ++e) m[c] = fmaxf(m[c], sc[e]);
                }
            }
            LAS float* mx = (LAS float*)(L + LDS_MX);
#pragma unroll
            for (int c = 0; c < 2; ++c) { m[c] = half_max(m[c]); if (l < 32) mx[(w * 2 + c) * 32 + l] = m[c]; }
            LBAR();
#pragma unroll
            for (int c = 0; c < 2; ++c) {
                const float mm = fmaxf(m[c], mx[((w ^ 2) * 2 + c) * 32 + (l & 31)]);
                const float chi = rbf(mm), clo = mm - chi;
                q2[c][1] = h5 ? 0u : cvtpk(-chi, -clo);
            }
            o0 = (f32x16){}; o1 = (f32x16){};
            for (int i = 0; i < 32; ++i) {
                const unsigned to = (i >> 3) == 0 ? LDS_KV0 : ((i >> 3) - 1) * KVCH;
                const bf16x8 k1 = lds_b128(L + k1_base + to + (i & 7) * 1024);
                const u32x2 k2 = {*(LAS const unsigned*)(L + k2_base + to + (i & 7) * 128), kone};
                const unsigned va = v_base + v_mul * (to + (i & 7) * 1216);
                const bf16x8 v0 = lds_b128(L + va), v1 = lds_b128(L + va + 608);
#pragma unroll
                for (int c = 0; c < 2; ++c) {
                    f32x16 sc = MFMA32(k1, q1[c], (f32x16){});
                    sc = MFMA32K8(__builtin_bit_cast(s16x4, k2), __builtin_bit_cast(s16x4, q2[c]), sc);
                    u32x4 pa, pb;
#pragma unroll
                    for (int e = 0; e < 4; ++e) { pa[e] = cvtpk(EXP2(sc[2 * e]), EXP2(sc[2 * e + 1])); pb[e] = cvtpk(EXP2(sc[8 + 2 * e]), EXP2(sc[9 + 2 * e])); }
                    if (c == 0) { o0 = MFMA32(v0, __builtin_bit_cast(bf16x8, pa), o0); o0 = MFMA32(v1, __builtin_bit_cast(bf16x8, pb), o0); }
                    else { o1 = MFMA32(v0, __builtin_bit_cast(bf16x8, pa), o1); o1 = MFMA32(v1, __builtin_bit_cast(bf16x8, pb), o1); }
                }
            }
        }
        LAS float* red = (LAS float*)(L + LDS_RED);
        LBAR();
        if (attempt == 0) {
            piece_wo(111 + w); if (w == 0) piece_wo(119);
        } else {
#pragma unroll
            for (int q = 0; q < 15; ++q) piece_wo(w * 15 + q);
        }
#pragma unroll
        for (int e = 0; e < 11; ++e) { red[(w * 11 + e) * 64 + l] = kh ? o0[e] : o1[e]; own[e] = kh ? o1[e] : o0[e]; }
        LBAR();
#pragma unroll
        for (int e = 0; e < 11; ++e) own[e] += red[((w ^ 2) * 11 + e) * 64 + l];
        const float lsum = half_lo(own[10]);
        if (!(lsum >= 8.67e-19f && lsum < 3.0e38f) && attempt == 0) *(LAS unsigned*)(L + LDS_CTL) = 1u;
        const float rl = 1.0f / lsum;
        LAS char* ot = L + LDS_OT + (32 * (2 * cp + kh) + (l & 31)) * 80 + 40 * hh;
        *(LAS u32x2*)(ot + 8 * h5) = (u32x2){cvtpk(own[0] * rl, own[1] * rl), cvtpk(own[2] * rl, own[3] * rl)};
        *(LAS u32x2*)(ot + 16 + 8 * h5) = (u32x2){cvtpk(own[4] * rl, own[5] * rl), cvtpk(own[6] * rl, own[7] * rl)};
        if (h5 == 0) *(LAS u32x2*)(ot + 32) = (u32x2){cvtpk(own[8] * rl, own[9] * rl), 0u};
        LBAR();
        const unsigned isbad = *(LAS const unsigned*)(L + LDS_CTL);
        if (!(isbad && attempt == 0)) break;
    }
    {
        const __amdgpu_buffer_rsrc_t rw = __builtin_amdgcn_make_buffer_rsrc(ws + WS_O + (size_t)(r * 16 + s) * 128 * 80, 0, 128 * 80, 0x00020000);
        __builtin_amdgcn_raw_buffer_store_b128(*(LAS const u32x4*)(L + LDS_OT + w * 1024 + l * 16), rw, l * 16, w * 1024, 16);
        if (w < 2) __builtin_amdgcn_raw_buffer_store_b128(*(LAS const u32x4*)(L + LDS_OT + (8 + w) * 1024 + l * 16), rw, l * 16, (8 + w) * 1024, 16);
    }
    {
        const int tok = l & 15, g4 = l >> 4, q4 = (l >> 2) & 3, p4 = l & 3;
        const __amdgpu_buffer_rsrc_t ro = __builtin_amdgcn_make_buffer_rsrc(ws + WS_O + (size_t)r * 16 * 128 * 80, 0, 16 * 128 * 80, 0x00020000);
        u32x4 bf[5][2]; unsigned bo[5];
#pragma unroll
        for (int i = 0; i < 5; ++i) {
            const int kp = 32 * (5 * qk + i) + 8 * g4, sp = kp / 40, d = kp - 40 * sp;
            bo[i] = (sp * 128 + 64 * tgf + 32 * pr + tok) * 80 + d * 2;
#pragma unroll
            for (int nn = 0; nn < 2; ++nn) bf[i][nn] = __builtin_amdgcn_raw_buffer_load_b128(ro, bo[i], nn * 16 * 80, 16);
        }
        SB();
        u32x2 xv[3];
#pragma unroll
        for (int u = 0; u < 3; ++u) {
            const int tt = min(qk + 4 * u, 2 * nmt - 1);
            xv[u] = *(const u32x2*)(x + (size_t)(128 * r + 64 * tgf + 32 * pr + 16 * (tt & 1) + tok) * DM + 16 * (m0 + (tt >> 1)) + 4 * g4);
        }
        f32x4 fa[5][2];
#pragma unroll
        for (int t = 0; t < 5; ++t) {
            fa[t][0] = (f32x4){0.f, 0.f, 0.f, 0.f}; fa[t][1] = (f32x4){0.f, 0.f, 0.f, 0.f};
#pragma unroll
            for (int sp = 0; sp < 4; ++sp) {
                const u32x4 p = pl[t][sp];
                fa[t][0][0] += bflo(p[0]); fa[t][0][1] += bfhi(p[0]); fa[t][0][2] += bflo(p[1]); fa[t][0][3] += bfhi(p[1]);
                fa[t][1][0] += bflo(p[2]); fa[t][1][1] += bfhi(p[2]); fa[t][1][2] += bflo(p[3]); fa[t][1][3] += bfhi(p[3]);
            }
        }
#pragma unroll
        for (int t = 0; t < 5; ++t) asm volatile("" : "+v"(fa[t][0]), "+v"(fa[t][1]));
        WAIT_BAR(0);
        bf16x8 af[5][5];
        {
            const unsigned ao = (4 * g4 + q4) * 64 + 8 * (p4 & 1);
#pragma unroll
            for (int t = 0; t < 5; ++t)
#pragma unroll
                for (int i = 0; i < 5; ++i) {
                    const int p = ((t < nmt ? t : 0) >> 1) * 40 + (5 * qk + i) * 2;
                    const unsigned sw = ao + 16 * ((2 * (t & 1) + (p4 >> 1)) ^ (2 * (g4 & 1)));
                    const s16x4 lo = lds_tr(L + wo_lds(p) + sw), hi = lds_tr(L + wo_lds(p + 1) + sw);
                    af[t][i] = __builtin_shufflevector(lo, hi, 0, 1, 2, 3, 4, 5, 6, 7);
                }
        }
        LBAR();
        unsigned pend = 0x3ffu;
        for (unsigned spins = 0;; ++spins) {
#pragma unroll
            for (int i = 0; i < 5; ++i)
#pragma unroll
                for (int nn = 0; nn < 2; ++nn)
                    if ((pend & (1u << (2 * i + nn))) && __all(bf[i][nn][0] != O_EMPTY && bf[i][nn][1] != O_EMPTY && bf[i][nn][2] != O_EMPTY && bf[i][nn][3] != O_EMPTY)) pend &= ~(1u << (2 * i + nn));
            if (pend == 0u) break;
            if (spins > (1u << 17)) { __hip_atomic_store((GAS unsigned*)(flags + FL_TMO), tag, RLX_AGENT); break; }
            __builtin_amdgcn_s_sleep(8);
#pragma unroll
            for (int i = 0; i < 5; ++i)
#pragma unroll
                for (int nn = 0; nn < 2; ++nn)
                    if (pend & (1u << (2 * i + nn))) bf[i][nn] = __builtin_amdgcn_raw_buffer_load_b128(ro, bo[i], nn * 16 * 80, 16);
        }
#pragma unroll
        for (int i = 0; i < 5; ++i)
#pragma unroll
            for (int t = 0; t < 5; ++t) {
                fa[t][0] = MFMA16(af[t][i], __builtin_bit_cast(bf16x8, bf[i][0]), fa[t][0]); fa[t][1] = MFMA16(af[t][i], __builtin_bit_cast(bf16x8, bf[i][1]), fa[t][1]);
            }
        LAS f32x4* ex = (LAS f32x4*)L;
#pragma unroll
        for (int t = 0; t < 5; ++t) { ex[(w * 10 + 2 * t) * 64 + l] = fa[t][0]; ex[(w * 10 + 2 * t + 1) * 64 + l] = fa[t][1]; }
        LBAR();
#pragma unroll
        for (int u = 0; u < 3; ++u) {
            const int tt = qk + 4 * u;
            if (tt < 2 * nmt) {
                f32x4 v = ex[((0 * 2 + pr) * 10 + tt) * 64 + l];
#pragma unroll
                for (int k = 1; k < 4; ++k) v += ex[((k * 2 + pr) * 10 + tt) * 64 + l];
                const size_t off = (size_t)(128 * r + 64 * tgf + 32 * pr + 16 * (tt & 1) + tok) * DM + 16 * (m0 + (tt >> 1)) + 4 * g4;
                *(u32x2*)(out + off) = (u32x2){cvtpk(v[0] + bflo(xv[u][0]), v[1] + bfhi(xv[u][0])), cvtpk(v[2] + bflo(xv[u][1]), v[3] + bfhi(xv[u][1]))};
            }
        }
    }
#undef l
#undef h5
    asm volatile("fused_block_code_end:");
}

extern "C" void kernel_launch(void* const* d_in, const int* in_sizes, int n_in, void* d_out, int out_size, void* d_ws, size_t ws_size, hipStream_t stream) {
    const u16* x = (const u16*)d_in[0]; const u16* wq = (const u16*)d_in[1]; const u16* wk = (const u16*)d_in[2]; const u16* wv = (const u16*)d_in[3];
    const u16* wo = (const u16*)d_in[4]; const u16* wg = (const u16*)d_in[6]; const u16* wu = (const u16*)d_in[7]; const u16* wd = (const u16*)d_in[8];
    fused_block<<<256, 512, 0, stream>>>(x, wq, wk, wv, wo, wg, wu, wd, (char*)d_ws, (u16*)d_out);
}
```
